```python
import torch

def _softmax(x, axis=-1):
    return torch.softmax(x, dim=axis)
def _gelu(x):
    return torch.nn.functional.gelu(x, approximate="tanh")
def _silu(x):
    return torch.nn.functional.silu(x)
def _sigmoid(x):
    return torch.sigmoid(x)
def _layernorm(x, axis=-1, eps=1e-5):
    m = x.mean(dim=axis, keepdim=True)
    v = x.var(dim=axis, keepdim=True, unbiased=False)
    return (x - m) / torch.sqrt(v + eps)
def _scatter_add(tgt, idx, src, axis=0):
    return tgt.clone().index_add_(axis, idx, src)
def _segment_sum(src, seg, n):
    out = torch.zeros((n,) + src.shape[1:], dtype=src.dtype)
    return out.index_add_(0, seg, src)
def _pad(x, pw):
    flat = tuple(v for pair in reversed(pw) for v in pair)
    return torch.nn.functional.pad(x, flat)

kernel_name = 'concat_heads__mha_block_cached__mha_block_cached__near_ridge'

def setup_inputs():

    _w = (lambda a: (lambda t: t.to(torch.bfloat16) if t.is_floating_point() else t)(torch.as_tensor(a)))
    def _A_setup():
        B, Sq, D = 16, 192, 576
        H, Sk, Dh = 32, 512, 128
        return {
            "x": _w((lambda *s: torch.randn(*s, dtype=torch.bfloat16) * 0.8)(B, Sq, D)),
            "k_cache": _w((lambda *s: torch.randn(*s, dtype=torch.bfloat16) * 0.8)(B, H, Sk, Dh)),
            "v_cache": _w((lambda *s: torch.randn(*s, dtype=torch.bfloat16) * 0.05)(B, H, Sk, Dh)),
            "wq": _w((lambda *s: torch.randn(*s, dtype=torch.bfloat16) * 0.8)(D, H * Dh) * (D ** -0.5)),
            "wo": _w((lambda *s: torch.randn(*s, dtype=torch.bfloat16) * 0.05)(H * Dh, D) * ((H * Dh) ** -0.5)),
        }
    _A_in = _A_setup()

    def _B_setup():
        B, Sq, D = 16, 192, 576
        H, Sk, Dh = 32, 512, 128
        return {
            "x": _w((lambda *s: torch.randn(*s, dtype=torch.bfloat16) * 0.8)(B, Sq, D)),
            "k_cache": _w((lambda *s: torch.randn(*s, dtype=torch.bfloat16) * 0.8)(B, H, Sk, Dh)),
            "v_cache": _w((lambda *s: torch.randn(*s, dtype=torch.bfloat16) * 0.05)(B, H, Sk, Dh)),
            "wq": _w((lambda *s: torch.randn(*s, dtype=torch.bfloat16) * 0.8)(D, H * Dh) * (D ** -0.5)),
            "wo": _w((lambda *s: torch.randn(*s, dtype=torch.bfloat16) * 0.05)(H * Dh, D) * ((H * Dh) ** -0.5)),
        }
    _B_in = _B_setup()

    return {
        "A_x": _A_in["x"],
        "A_k_cache": _A_in["k_cache"],
        "A_v_cache": _A_in["v_cache"],
        "A_wq": _A_in["wq"],
        "A_wo": _A_in["wo"],
        "B_x": _B_in["x"],
        "B_k_cache": _B_in["k_cache"],
        "B_v_cache": _B_in["v_cache"],
        "B_wq": _B_in["wq"],
        "B_wo": _B_in["wo"],
    }

def reference(A_x, A_k_cache, A_v_cache, A_wq, A_wo, B_x, B_k_cache, B_v_cache, B_wq, B_wo):
    def _A_ref(x, k_cache, v_cache, wq, wo):
        B, Sq, D = x.shape
        H, Dh = 32, 128
        q = (lambda x, *d: x.permute(*d))((x @ wq).reshape(B, Sq, H, Dh), 0, 2, 1, 3)
        scores = torch.einsum("bhqd,bhkd->bhqk", q, k_cache) * (Dh ** -0.5)
        p = _softmax(scores, axis=-1)
        o = torch.einsum("bhqk,bhkd->bhqd", p, v_cache)
        return (lambda x, *d: x.permute(*d))(o, 0, 2, 1, 3).reshape(B, Sq, H * Dh) @ wo

    def _B_ref(x, k_cache, v_cache, wq, wo):
        B, Sq, D = x.shape
        H, Dh = 32, 128
        q = (lambda x, *d: x.permute(*d))((x @ wq).reshape(B, Sq, H, Dh), 0, 2, 1, 3)
        scores = torch.einsum("bhqd,bhkd->bhqk", q, k_cache) * (Dh ** -0.5)
        p = _softmax(scores, axis=-1)
        o = torch.einsum("bhqk,bhkd->bhqd", p, v_cache)
        return (lambda x, *d: x.permute(*d))(o, 0, 2, 1, 3).reshape(B, Sq, H * Dh) @ wo

    a = _A_ref(A_x, A_k_cache, A_v_cache, A_wq, A_wo)
    b = _B_ref(B_x, B_k_cache, B_v_cache, B_wq, B_wo)
    return torch.concatenate([a, b], axis=-1)
```

```cpp
#include <hip/hip_runtime.h>
#include <hip/hip_bf16.h>
typedef unsigned short u16;
__device__ __forceinline__ float bf2f(u16 v){ return __uint_as_float(((unsigned)v)<<16); }
__device__ __forceinline__ u16 f2bf(float f){ unsigned u=__float_as_uint(f); u += 0x7fffu + ((u>>16)&1u); return (u16)(u>>16); }
constexpr int B=16,SQ=192,D=576,H=32,SK=512,DH=128,HD=4096;
__global__ __launch_bounds__(256) void gemm_naive(const u16* A,const u16* W,u16* C,int M,int N,int K,int ldc,int coff){
  __shared__ float As[16][65], Ws[16][65];
  int tx=threadIdx.x&15, ty=threadIdx.x>>4; int m0=blockIdx.y*64,n0=blockIdx.x*64;
  float acc[4][4]={};
  for(int k0=0;k0<K;k0+=16){
    for(int i=threadIdx.x;i<1024;i+=256){ int r=i>>4,c=i&15; As[c][r]=bf2f(A[(long)(m0+r)*K+k0+c]); }
    for(int i=threadIdx.x;i<1024;i+=256){ int r=i>>6,c=i&63; Ws[r][c]=bf2f(W[(long)(k0+r)*N+n0+c]); }
    __syncthreads();
    for(int k=0;k<16;++k){ float a[4],w[4]; for(int i=0;i<4;++i){a[i]=As[k][ty*4+i]; w[i]=Ws[k][tx*4+i];}
      for(int i=0;i<4;++i)for(int j=0;j<4;++j)acc[i][j]+=a[i]*w[j]; }
    __syncthreads();
  }
  for(int i=0;i<4;++i)for(int j=0;j<4;++j) C[(long)(m0+ty*4+i)*ldc+coff+n0+tx*4+j]=f2bf(acc[i][j]);
}
__global__ __launch_bounds__(128) void attn_naive(const u16* Q,const u16* Kc,const u16* Vc,u16* O){
  int q=blockIdx.x, h=blockIdx.y, b=blockIdx.z, t=threadIdx.x;
  __shared__ float qs[128], s[512], red[128];
  qs[t]=bf2f(Q[((long)(b*SQ+q))*HD+h*DH+t]); __syncthreads();
  const u16* K=Kc+((long)(b*H+h))*SK*DH; const u16* V=Vc+((long)(b*H+h))*SK*DH;
  float mx=-1e30f;
  for(int k=t;k<SK;k+=128){ float a=0; for(int d=0;d<DH;++d)a+=qs[d]*bf2f(K[k*DH+d]); a*=0.08838834764831845f; s[k]=a; mx=fmaxf(mx,a);}
  red[t]=mx; __syncthreads(); for(int o=64;o>0;o>>=1){ if(t<o)red[t]=fmaxf(red[t],red[t+o]); __syncthreads(); }
  mx=red[0]; __syncthreads(); float sm=0; for(int k=t;k<SK;k+=128){ float e=__expf(s[k]-mx); s[k]=e; sm+=e; }
  red[t]=sm; __syncthreads(); for(int o=64;o>0;o>>=1){ if(t<o)red[t]+=red[t+o]; __syncthreads(); }
  sm=red[0]; float a=0; for(int k=0;k<SK;++k)a+=s[k]*bf2f(V[k*DH+t]);
  O[((long)(b*SQ+q))*HD+h*DH+t]=f2bf(a/sm);
}
extern "C" void kernel_launch(void* const* d_in, const int* in_sizes, int n_in, void* d_out, int out_size, void* d_ws, size_t ws_size, hipStream_t stream){
  u16* ws=(u16*)d_ws; const long NQ=(long)B*SQ*HD;
  for(int br=0;br<2;++br){
    const u16* x=(const u16*)d_in[br*5+0]; const u16* k=(const u16*)d_in[br*5+1]; const u16* v=(const u16*)d_in[br*5+2];
    const u16* wq=(const u16*)d_in[br*5+3]; const u16* wo=(const u16*)d_in[br*5+4];
    u16* q=ws+br*2*NQ; u16* o=q+NQ;
    gemm_naive<<<dim3(HD/64,B*SQ/64),256,0,stream>>>(x,wq,q,B*SQ,HD,D,HD,0);
    attn_naive<<<dim3(SQ,H,B),128,0,stream>>>(q,k,v,o);
    gemm_naive<<<dim3(D/64,B*SQ/64),256,0,stream>>>(o,wo,(u16*)d_out,B*SQ,D,HD,2*D,br*D);
  }
}
```

```cpp
#include <hip/hip_runtime.h>
typedef unsigned short u16;
typedef short bf16x8 __attribute__((ext_vector_type(8)));
typedef float f32x4 __attribute__((ext_vector_type(4)));
constexpr size_t WS_O = 0, WS_WT = 50331648, WS_F = WS_WT + 9437184, WS_P = WS_WT + 0x3000000;

__global__ __launch_bounds__(256, 1) void k1_qattn(const u16* __restrict__ xa, const u16* __restrict__ ka, const u16* __restrict__ va, const u16* __restrict__ wqa, const u16* __restrict__ woa,
                                                   const u16* __restrict__ xb, const u16* __restrict__ kb, const u16* __restrict__ vb, const u16* __restrict__ wqb, const u16* __restrict__ wob,
                                                   char* __restrict__ ws) {
  extern __shared__ __attribute__((aligned(16))) char lds[];
  const unsigned bid = blockIdx.x, xc = bid & 7u, i = bid >> 3, br = xc >> 2;
  const unsigned bh = (xc >> 1) & 1u, hp = (xc & 1u) * 8u + bh * 4u + (i & 3u), b = bh * 8u + (i >> 2), lw = (xc & 3u) * 32u + i;
  const int dh = bh ? -8 : 8;
  const u16* x = br ? xb : xa; const u16* k = br ? kb : ka; const u16* v = br ? vb : va; const u16* w = br ? wqb : wqa; const u16* wo = br ? wob : woa;
  char* o = ws + WS_O + (size_t)br * 25165824u; char* wt = ws + WS_WT + (size_t)br * 4718592u;
  const unsigned rot = ((i >> 2) + 2u * (i & 3u)) % 9u; const unsigned jr = (i + 2u * xc) & 15u; const unsigned ldsb = (unsigned)(size_t)lds; const unsigned tid = threadIdx.x;
  if (tid == 0) *(unsigned*)(ws + WS_F + bid * 128u) = 0u;
  asm volatile(
"v_lshrrev_b32 v16, 6, %[tid]\n"
"v_and_b32 v19, 63, %[tid]\n"
"v_readfirstlane_b32 s81, v16\n"
"s_mov_b64 s[44:45], %[x]\n"
"s_and_b32 s45, s45, 0xffff\n"
"s_mov_b32 s46, 3538944\n"
"s_mov_b32 s47, 0x20000\n"
"s_mov_b64 s[48:49], %[w]\n"
"s_and_b32 s49, s49, 0xffff\n"
"s_mov_b32 s50, 4718592\n"
"s_mov_b32 s51, 0x20000\n"
"s_lshr_b32 s72, s81, 1\n"
"s_and_b32 s73, s81, 1\n"
"s_lshl_b32 s84, %[hp], 1\n"
"s_add_u32 s84, s84, s72\n"
"s_lshl_b32 s87, s81, 12\n"
"s_add_u32 s60, s87, %[lds]\n"
"s_mul_i32 s87, s81, 3072\n"
"s_add_u32 s61, s87, %[lds]\n"
"s_lshl_b32 s87, s84, 8\n"
"s_lshl_b32 s88, s73, 18\n"
"s_add_u32 s58, s87, s88\n"
"s_mul_i32 s87, %[b], 192\n"
"s_mul_i32 s88, s72, 96\n"
"s_add_u32 s87, s87, s88\n"
"s_mul_i32 s88, s73, 24\n"
"s_add_u32 s87, s87, s88\n"
"s_mul_i32 s57, s87, 1152\n"
"s_lshl_b32 s82, %[rot], 7\n"
"s_lshl_b32 s83, %[rot], 19\n"
"v_bfe_u32 v16, v19, 2, 2\n"
"v_bfe_u32 v18, v19, 0, 2\n"
"v_bfe_u32 v17, v19, 2, 3\n"
"v_mul_u32_u24 v4, 1152, v17\n"
"v_bfe_u32 v17, v19, 5, 1\n"
"v_lshl_add_u32 v4, v17, 6, v4\n"
"v_lshl_add_u32 v4, v18, 4, v4\n"
"s_lshl_b32 s87, s73, 5\n"
"v_xor_b32 v4, s87, v4\n"
"v_xor_b32 v5, 32, v4\n"
"v_lshl_add_u32 v17, v17, 2, v16\n"
"v_lshlrev_b32 v3, 13, v17\n"
"v_bfe_u32 v17, v19, 4, 1\n"
"v_lshl_add_u32 v3, v17, 6, v3\n"
"v_lshl_add_u32 v3, v18, 4, v3\n"
"s_add_u32 m0, s61, 98304\n"
"s_add_u32 s74, s57, s82\n"
"s_add_u32 s64, s74, 0\n"
"buffer_load_dwordx4 v4, s[44:47], s64 offen offset:0 lds\n"
"s_add_u32 s65, s74, 8192\n"
"buffer_load_dwordx4 v5, s[44:47], s65 offen offset:1024 lds\n"
"s_add_u32 s66, s74, 16384\n"
"buffer_load_dwordx4 v4, s[44:47], s66 offen offset:2048 lds\n"
"s_add_u32 m0, s61, 131072\n"
"s_add_u32 s74, s57, s82\n"
"s_add_u32 s82, s82, 128\n"
"s_cmp_eq_u32 s82, 1152\n"
"s_cselect_b32 s82, 0, s82\n"
"s_add_u32 s67, s74, 55296\n"
"buffer_load_dwordx4 v4, s[44:47], s67 offen offset:0 lds\n"
"s_add_u32 s68, s74, 63488\n"
"buffer_load_dwordx4 v5, s[44:47], s68 offen offset:1024 lds\n"
"s_add_u32 s69, s74, 71680\n"
"buffer_load_dwordx4 v4, s[44:47], s69 offen offset:2048 lds\n"
"s_add_u32 m0, s60, 16384\n"
"s_add_u32 s75, s58, s83\n"
"s_add_u32 s70, s75, 0\n"
"buffer_load_dwordx4 v3, s[48:51], s70 offen offset:0 lds\n"
"s_add_u32 s71, s75, 64512\n"
"buffer_load_dwordx4 v3, s[48:51], s71 offen offset:1024 lds\n"
"s_add_u32 s64, s75, 129024\n"
"buffer_load_dwordx4 v3, s[48:51], s64 offen offset:2048 lds\n"
"s_add_u32 s65, s75, 193536\n"
"buffer_load_dwordx4 v3, s[48:51], s65 offen offset:3072 lds\n"
"s_add_u32 m0, s61, 0\n"
"s_add_u32 s74, s57, s82\n"
"s_add_u32 s66, s74, 0\n"
"buffer_load_dwordx4 v4, s[44:47], s66 offen offset:0 lds\n"
"s_add_u32 s67, s74, 8192\n"
"buffer_load_dwordx4 v5, s[44:47], s67 offen offset:1024 lds\n"
"s_add_u32 s68, s74, 16384\n"
"buffer_load_dwordx4 v4, s[44:47], s68 offen offset:2048 lds\n"
"s_mov_b64 s[36:37], %[k]\n"
"s_and_b32 s37, s37, 0xffff\n"
"s_mov_b32 s38, 67108864\n"
"s_mov_b32 s39, 0x20000\n"
"s_mov_b64 s[40:41], %[v]\n"
"s_and_b32 s41, s41, 0xffff\n"
"s_mov_b32 s42, 67108864\n"
"s_mov_b32 s43, 0x20000\n"
"s_mov_b64 s[52:53], %[o]\n"
"s_and_b32 s53, s53, 0xffff\n"
"s_mov_b32 s54, 0\n"
"s_mov_b32 s55, 0x20000\n"
"s_mov_b64 s[92:93], %[wo]\n"
"s_and_b32 s93, s93, 0xffff\n"
"s_mov_b32 s94, 4718592\n"
"s_mov_b32 s95, 0x20000\n"
"s_mov_b64 s[96:97], %[wt]\n"
"s_and_b32 s97, s97, 0xffff\n"
"s_mov_b32 s98, 4718592\n"
"s_mov_b32 s99, 0x20000\n"
"s_mov_b32 s80, 25165824\n"
"s_mov_b32 s78, 0x41000000\n"
"s_mov_b32 s79, 0x3e0293ee\n"
"s_mov_b32 s62, 2\n"
"s_mov_b32 s63, 0\n"
"s_lshl_b32 s85, %[b], 5\n"
"s_add_u32 s85, s85, s84\n"
"s_lshl_b32 s85, s85, 17\n"
"s_lshl_b32 s86, s73, 12\n"
"s_add_u32 s56, s85, s86\n"
"s_lshl_b32 s87, %[b], 2\n"
"s_lshl_b32 s88, s73, 1\n"
"s_add_u32 s87, s87, s88\n"
"s_lshl_b32 s87, s87, 7\n"
"s_lshl_b32 s88, s84, 2\n"
"s_add_u32 s87, s87, s88\n"
"s_mul_i32 s87, s87, 3072\n"
"s_mov_b32 s91, %[dh]\n"
"s_mul_i32 s88, s91, 12288\n"
"s_sub_u32 s59, s87, s88\n"
"v_lshlrev_b32 v11, 4, v19\n"
"v_lshlrev_b32 v1, 8, v16\n"
"v_bfe_u32 v17, v19, 4, 2\n"
"v_lshl_add_u32 v1, v17, 6, v1\n"
"v_lshl_add_u32 v1, v18, 4, v1\n"
"v_xor_b32 v2, 32, v1\n"
"v_bfe_u32 v23, v19, 4, 2\n"
"s_lshl_b32 s87, s72, 13\n"
"s_add_u32 s87, s87, %[lds]\n"
"v_and_b32 v17, 1, v16\n"
"v_lshlrev_b32 v17, 1, v17\n"
"v_xor_b32 v17, v17, v23\n"
"v_lshlrev_b32 v6, 10, v16\n"
"v_lshl_add_u32 v6, v18, 6, v6\n"
"v_lshl_add_u32 v6, v17, 4, v6\n"
"v_add_u32 v6, s87, v6\n"
"s_add_u32 s88, s87, 16384\n"
"v_lshlrev_b32 v7, 10, v23\n"
"v_lshl_add_u32 v7, v16, 6, v7\n"
"v_lshl_add_u32 v9, v18, 4, v7\n"
"v_add_u32 v9, s88, v9\n"
"v_lshl_add_u32 v7, v18, 3, v7\n"
"v_and_b32 v17, 1, v23\n"
"v_lshl_add_u32 v7, v17, 5, v7\n"
"v_xor_b32 v8, 32, v7\n"
"v_add_u32 v7, s88, v7\n"
"v_add_u32 v8, s88, v8\n"
"v_bfe_u32 v22, v19, 3, 1\n"
"v_lshlrev_b32 v17, 1, v22\n"
"v_xor_b32 v17, v17, v23\n"
"v_lshlrev_b32 v10, 10, v22\n"
"v_lshl_add_u32 v10, v17, 4, v10\n"
"v_bfe_u32 v17, v19, 0, 3\n"
"v_lshl_add_u32 v10, v17, 6, v10\n"
"s_mul_i32 s88, s73, 6144\n"
"s_add_u32 s88, s88, %[lds]\n"
"v_add_u32 v10, s88, v10\n"
"s_lshl_b32 s90, s81, 10\n"
"s_add_u32 s90, s90, %[lds]\n"
"s_add_u32 s90, s90, 131072\n"
"s_mul_i32 s88, %[lw], 36864\n"
"s_mul_i32 s85, s81, 9216\n"
"s_add_u32 s88, s88, s85\n"
"s_mul_i32 s89, %[lw], 36864\n"
"s_lshl_b32 s85, s81, 10\n"
"s_add_u32 s89, s89, s85\n"
"s_getpc_b64 s[68:69]\n"
"s_and_b32 s69, s69, 0xffff\n"
"s_mov_b32 s70, END_%=-LOOP_%=\n"
"s_mov_b32 s71, 0x20000\n"
"s_and_b32 s85, %[lw], 31\n"
"s_lshl_b32 s85, s85, 2\n"
"s_add_u32 s85, s85, s81\n"
"s_lshl_b32 s85, s85, 9\n"
"v_lshlrev_b32 v17, 3, v19\n"
"buffer_load_dword v15, v17, s[68:71], s85 offen offset:0\n"
"s_lshl_b32 s87, %[jr], 13\n"
"s_waitcnt vmcnt(11)\n"
"s_nop 0\n"
"s_waitcnt vmcnt(8)\n"
"s_barrier\n"
"v_add_u32 v10, 0x10000, v10\n"
"ds_read_b128 a[216:219], v10 offset:32768\n"
"ds_read_b128 a[220:223], v10 offset:33280\n"
"ds_read_b128 a[224:227], v10 offset:34816\n"
"ds_read_b128 a[228:231], v10 offset:35328\n"
"ds_read_b128 a[232:235], v10 offset:36864\n"
"ds_read_b128 a[236:239], v10 offset:37376\n"
"v_add_u32 v10, 0x10000, v10\n"
"ds_read_b128 a[240:243], v10 offset:0\n"
"ds_read_b128 a[244:247], v10 offset:512\n"
"ds_read_b128 a[248:251], v10 offset:2048\n"
"ds_read_b128 a[252:255], v10 offset:2560\n"
"ds_read_b128 v[232:235], v10 offset:4096\n"
"ds_read_b128 v[236:239], v10 offset:4608\n"
"s_waitcnt vmcnt(1) lgkmcnt(0)\n"
"s_barrier\n"
"ds_read_b64_tr_b16 v[208:209], v9 offset:0\n"
"ds_read_b64_tr_b16 v[210:211], v9 offset:512\n"
"ds_read_b64_tr_b16 v[212:213], v9 offset:4096\n"
"ds_read_b64_tr_b16 v[214:215], v9 offset:4608\n"
"ds_read_b64_tr_b16 v[216:217], v9 offset:8\n"
"ds_read_b64_tr_b16 v[218:219], v9 offset:520\n"
"ds_read_b64_tr_b16 v[220:221], v9 offset:4104\n"
"ds_read_b64_tr_b16 v[222:223], v9 offset:4616\n"
"s_add_u32 m0, s60, 49152\n"
"s_add_u32 s75, s58, s83\n"
"s_add_u32 s83, s83, 524288\n"
"s_cmp_eq_u32 s83, 4718592\n"
"s_cselect_b32 s83, 0, s83\n"
"s_add_u32 s69, s75, 128\n"
"buffer_load_dwordx4 v3, s[48:51], s69 offen offset:0 lds\n"
"s_add_u32 s70, s75, 64640\n"
"buffer_load_dwordx4 v3, s[48:51], s70 offen offset:1024 lds\n"
"s_add_u32 s71, s75, 129152\n"
"buffer_load_dwordx4 v3, s[48:51], s71 offen offset:2048 lds\n"
"s_add_u32 s64, s75, 193664\n"
"buffer_load_dwordx4 v3, s[48:51], s64 offen offset:3072 lds\n"
"s_add_u32 m0, s61, 32768\n"
"s_add_u32 s74, s57, s82\n"
"s_add_u32 s82, s82, 128\n"
"s_cmp_eq_u32 s82, 1152\n"
"s_cselect_b32 s82, 0, s82\n"
"s_add_u32 s65, s74, 55296\n"
"buffer_load_dwordx4 v4, s[44:47], s65 offen offset:0 lds\n"
"s_add_u32 s66, s74, 63488\n"
"buffer_load_dwordx4 v5, s[44:47], s66 offen offset:1024 lds\n"
"s_add_u32 s67, s74, 71680\n"
"buffer_load_dwordx4 v4, s[44:47], s67 offen offset:2048 lds\n"
"s_waitcnt lgkmcnt(0)\n"
"s_nop 1\n"
"LOOP_%=:\n"
"v_mfma_f32_16x16x32_bf16 v[136:139], v[208:211], a[216:219], 0\n"
"v_lshrrev_b32 v12, 7, v11\n"
"v_mul_u32_u24 v12, 1152, v12\n"
"v_mfma_f32_16x16x32_bf16 v[136:139], v[212:215], a[220:223], v[136:139]\n"
"v_and_b32 v13, 112, v11\n"
"s_add_u32 m0, s90, 0\n"
"v_mfma_f32_16x16x32_bf16 v[140:143], v[208:211], a[224:227], 0\n"
"v_add_u32 v12, v12, v13\n"
"ds_read_b64_tr_b16 v[224:225], v9 offset:256\n"
"v_mfma_f32_16x16x32_bf16 v[140:143], v[212:215], a[228:231], v[140:143]\n"
"buffer_load_dwordx4 v12, s[92:95], s88 offen offset:0 nt lds\n"
"v_mfma_f32_16x16x32_bf16 v[144:147], v[208:211], a[232:235], 0\n"
"s_add_u32 m0, s90, 3968\n"
"ds_read_b64_tr_b16 v[226:227], v9 offset:768\n"
"v_mfma_f32_16x16x32_bf16 v[144:147], v[212:215], a[236:239], v[144:147]\n"
"buffer_load_dwordx4 v12, s[92:95], s88 offen offset:128 nt lds\n"
"v_mfma_f32_16x16x32_bf16 v[148:151], v[208:211], a[240:243], 0\n"
"s_add_u32 m0, s90, 7936\n"
"ds_read_b64_tr_b16 v[228:229], v9 offset:4352\n"
"v_mfma_f32_16x16x32_bf16 v[148:151], v[212:215], a[244:247], v[148:151]\n"
"buffer_load_dwordx4 v12, s[92:95], s88 offen offset:256 nt lds\n"
"v_mfma_f32_16x16x32_bf16 v[152:155], v[208:211], a[248:251], 0\n"
"s_add_u32 m0, s90, 11904\n"
"ds_read_b64_tr_b16 v[230:231], v9 offset:4864\n"
"v_mfma_f32_16x16x32_bf16 v[152:155], v[212:215], a[252:255], v[152:155]\n"
"buffer_load_dwordx4 v12, s[92:95], s88 offen offset:384 nt lds\n"
"v_mfma_f32_16x16x32_bf16 v[156:159], v[208:211], v[232:235], 0\n"
"s_add_u32 m0, s90, 15872\n"
"s_add_u32 s75, s58, s83\n"
"v_mfma_f32_16x16x32_bf16 v[156:159], v[212:215], v[236:239], v[156:159]\n"
"buffer_load_dwordx4 v12, s[92:95], s88 offen offset:512 nt lds\n"
"v_mfma_f32_16x16x32_bf16 v[16:19], v[216:219], a[216:219], 0\n"
"s_add_u32 m0, s60, 81920\n"
"s_add_u32 s68, s75, 0\n"
"v_mfma_f32_16x16x32_bf16 v[16:19], v[220:223], a[220:223], v[16:19]\n"
"buffer_load_dwordx4 v3, s[48:51], s68 offen offset:0 lds\n"
"v_mfma_f32_16x16x32_bf16 v[20:23], v[216:219], a[224:227], 0\n"
"s_add_u32 s69, s75, 64512\n"
"s_add_u32 s70, s75, 129024\n"
"v_mfma_f32_16x16x32_bf16 v[20:23], v[220:223], a[228:231], v[20:23]\n"
"buffer_load_dwordx4 v3, s[48:51], s69 offen offset:1024 lds\n"
"v_mfma_f32_16x16x32_bf16 v[24:27], v[216:219], a[232:235], 0\n"
"buffer_load_dwordx4 v3, s[48:51], s70 offen offset:2048 lds\n"
"v_mfma_f32_16x16x32_bf16 v[24:27], v[220:223], a[236:239], v[24:27]\n"
"s_add_u32 s71, s75, 193536\n"
"s_add_u32 s74, s57, s82\n"
"v_mfma_f32_16x16x32_bf16 v[28:31], v[216:219], a[240:243], 0\n"
"buffer_load_dwordx4 v3, s[48:51], s71 offen offset:3072 lds\n"
"v_mfma_f32_16x16x32_bf16 v[28:31], v[220:223], a[244:247], v[28:31]\n"
"s_add_u32 m0, s61, 65536\n"
"s_add_u32 s64, s74, 0\n"
"v_mfma_f32_16x16x32_bf16 v[32:35], v[216:219], a[248:251], 0\n"
"buffer_load_dwordx4 v4, s[44:47], s64 offen offset:0 lds\n"
"v_mfma_f32_16x16x32_bf16 v[32:35], v[220:223], a[252:255], v[32:35]\n"
"s_add_u32 s65, s74, 8192\n"
"v_add_u32 v10, 0xfffe0000, v10\n"
"v_mfma_f32_16x16x32_bf16 v[36:39], v[216:219], v[232:235], 0\n"
"buffer_load_dwordx4 v5, s[44:47], s65 offen offset:1024 lds\n"
"v_mfma_f32_16x16x32_bf16 v[36:39], v[220:223], v[236:239], v[36:39]\n"
"ds_read_b128 a[0:3], v10 offset:0\n"
"s_waitcnt lgkmcnt(1)\n"
"v_mfma_f32_16x16x32_bf16 v[160:163], v[224:227], a[216:219], 0\n"
"ds_read_b128 a[4:7], v10 offset:512\n"
"ds_read_b128 a[8:11], v10 offset:2048\n"
"v_mfma_f32_16x16x32_bf16 v[160:163], v[228:231], a[220:223], v[160:163]\n"
"ds_read_b128 a[12:15], v10 offset:2560\n"
"ds_read_b128 a[16:19], v10 offset:4096\n"
"v_mfma_f32_16x16x32_bf16 v[164:167], v[224:227], a[224:227], 0\n"
"ds_read_b128 a[20:23], v10 offset:4608\n"
"ds_read_b64_tr_b16 v[208:209], v9 offset:264\n"
"ds_read_b64_tr_b16 v[210:211], v9 offset:776\n"
"ds_read_b64_tr_b16 v[212:213], v9 offset:4360\n"
"ds_read_b64_tr_b16 v[214:215], v9 offset:4872\n"
"v_mfma_f32_16x16x32_bf16 v[164:167], v[228:231], a[228:231], v[164:167]\n"
"s_add_u32 s66, s74, 16384\n"
"v_accvgpr_read_b32 v240, a40\n"
"v_mfma_f32_16x16x32_bf16 v[168:171], v[224:227], a[232:235], 0\n"
"buffer_load_dwordx4 v4, s[44:47], s66 offen offset:2048 lds\n"
"v_mfma_f32_16x16x32_bf16 v[168:171], v[228:231], a[236:239], v[168:171]\n"
"s_add_u32 s75, s58, s83\n"
"s_add_u32 m0, s60, 16384\n"
"v_mfma_f32_16x16x32_bf16 v[172:175], v[224:227], a[240:243], 0\n"
"s_add_u32 s67, s75, 128\n"
"v_accvgpr_read_b32 v241, a41\n"
"v_mfma_f32_16x16x32_bf16 v[172:175], v[228:231], a[244:247], v[172:175]\n"
"v_accvgpr_read_b32 v242, a42\n"
"v_accvgpr_read_b32 v243, a43\n"
"v_mfma_f32_16x16x32_bf16 v[176:179], v[224:227], a[248:251], 0\n"
"v_accvgpr_read_b32 v250, a44\n"
"v_accvgpr_read_b32 v251, a45\n"
"v_mfma_f32_16x16x32_bf16 v[176:179], v[228:231], a[252:255], v[176:179]\n"
"v_accvgpr_read_b32 v252, a46\n"
"v_accvgpr_read_b32 v253, a47\n"
"v_mfma_f32_16x16x32_bf16 v[180:183], v[224:227], v[232:235], 0\n"
"s_add_u32 s68, s75, 64640\n"
"v_mul_f32 v240, v249, v240\n"
"v_mfma_f32_16x16x32_bf16 v[180:183], v[228:231], v[236:239], v[180:183]\n"
"v_mul_f32 v241, v249, v241\n"
"s_waitcnt lgkmcnt(1)\n"
"v_mfma_f32_16x16x32_bf16 v[40:43], v[208:211], a[216:219], 0\n"
"s_waitcnt vmcnt(12) lgkmcnt(0)\n"
"s_barrier\n"
"buffer_load_dwordx4 v3, s[48:51], s67 offen offset:0 lds\n"
"v_mfma_f32_16x16x32_bf16 v[40:43], v[212:215], a[220:223], v[40:43]\n"
"ds_read_b64_tr_b16 v[216:217], v9 offset:32768\n"
"ds_read_b64_tr_b16 v[218:219], v9 offset:33280\n"
"v_mfma_f32_16x16x32_bf16 v[44:47], v[208:211], a[224:227], 0\n"
"ds_read_b64_tr_b16 v[220:221], v9 offset:36864\n"
"ds_read_b64_tr_b16 v[222:223], v9 offset:37376\n"
"v_mfma_f32_16x16x32_bf16 v[44:47], v[212:215], a[228:231], v[44:47]\n"
"buffer_load_dwordx4 v3, s[48:51], s68 offen offset:1024 lds\n"
"v_mfma_f32_16x16x32_bf16 v[48:51], v[208:211], a[232:235], 0\n"
"s_add_u32 s69, s75, 129152\n"
"ds_read_b64_tr_b16 v[224:225], v9 offset:32776\n"
"v_mfma_f32_16x16x32_bf16 v[48:51], v[212:215], a[236:239], v[48:51]\n"
"buffer_load_dwordx4 v3, s[48:51], s69 offen offset:2048 lds\n"
"v_mfma_f32_16x16x32_bf16 v[52:55], v[208:211], a[240:243], 0\n"
"ds_read_b64_tr_b16 v[226:227], v9 offset:33288\n"
"ds_read_b64_tr_b16 v[228:229], v9 offset:36872\n"
"v_mfma_f32_16x16x32_bf16 v[52:55], v[212:215], a[244:247], v[52:55]\n"
"ds_read_b64_tr_b16 v[230:231], v9 offset:37384\n"
"ds_read_b128 a[24:27], v10 offset:32768\n"
"v_mfma_f32_16x16x32_bf16 v[56:59], v[208:211], a[248:251], 0\n"
"ds_read_b128 a[28:31], v10 offset:33280\n"
"ds_read_b128 a[32:35], v10 offset:34816\n"
"v_mfma_f32_16x16x32_bf16 v[56:59], v[212:215], a[252:255], v[56:59]\n"
"s_add_u32 s70, s75, 193664\n"
"ds_read_b128 a[36:39], v10 offset:35328\n"
"v_mfma_f32_16x16x32_bf16 v[60:63], v[208:211], v[232:235], 0\n"
"buffer_load_dwordx4 v3, s[48:51], s70 offen offset:3072 lds\n"
"v_mfma_f32_16x16x32_bf16 v[60:63], v[212:215], v[236:239], v[60:63]\n"
"ds_read_b64_tr_b16 v[208:209], v9 offset:33024\n"
"s_waitcnt lgkmcnt(10)\n"
"v_mfma_f32_16x16x32_bf16 v[184:187], v[216:219], a[216:219], 0\n"
"ds_read_b64_tr_b16 v[210:211], v9 offset:33536\n"
"s_waitcnt lgkmcnt(10)\n"
"v_mfma_f32_16x16x32_bf16 v[184:187], v[220:223], a[220:223], v[184:187]\n"
"ds_read_b64_tr_b16 v[212:213], v9 offset:37120\n"
"ds_read_b64_tr_b16 v[214:215], v9 offset:37632\n"
"v_mfma_f32_16x16x32_bf16 v[72:75], v[216:219], a[224:227], 0\n"
"ds_read_b128 a[40:43], v10 offset:36864\n"
"ds_read_b128 a[44:47], v10 offset:37376\n"
"v_mfma_f32_16x16x32_bf16 v[72:75], v[220:223], a[228:231], v[72:75]\n"
"s_add_u32 s74, s57, s82\n"
"s_add_u32 m0, s61, 0\n"
"v_mfma_f32_16x16x32_bf16 v[88:91], v[216:219], a[232:235], 0\n"
"s_add_u32 s71, s74, 55296\n"
"v_add_u32 v10, 0x10000, v10\n"
"v_mfma_f32_16x16x32_bf16 v[88:91], v[220:223], a[236:239], v[88:91]\n"
"buffer_load_dwordx4 v4, s[44:47], s71 offen offset:0 lds\n"
"v_mfma_f32_16x16x32_bf16 v[104:107], v[216:219], a[240:243], 0\n"
"s_add_u32 s83, s83, 524288\n"
"s_cmp_eq_u32 s83, 4718592\n"
"s_cselect_b32 s83, 0, s83\n"
"v_mfma_f32_16x16x32_bf16 v[104:107], v[220:223], a[244:247], v[104:107]\n"
"s_add_u32 s82, s82, 128\n"
"s_cmp_eq_u32 s82, 1152\n"
"s_cselect_b32 s82, 0, s82\n"
"v_mfma_f32_16x16x32_bf16 v[120:123], v[216:219], a[248:251], 0\n"
"s_add_u32 s64, s74, 63488\n"
"v_mul_f32 v242, v249, v242\n"
"v_mfma_f32_16x16x32_bf16 v[120:123], v[220:223], a[252:255], v[120:123]\n"
"buffer_load_dwordx4 v5, s[44:47], s64 offen offset:1024 lds\n"
"v_mfma_f32_16x16x32_bf16 v[192:195], v[216:219], v[232:235], 0\n"
"ds_read_b64_tr_b16 v[216:217], v9 offset:33032\n"
"ds_read_b64_tr_b16 v[218:219], v9 offset:33544\n"
"v_mfma_f32_16x16x32_bf16 v[192:195], v[220:223], v[236:239], v[192:195]\n"
"ds_read_b64_tr_b16 v[220:221], v9 offset:37128\n"
"s_waitcnt lgkmcnt(4)\n"
"v_mfma_f32_16x16x32_bf16 v[64:67], v[224:227], a[216:219], 0\n"
"ds_read_b64_tr_b16 v[222:223], v9 offset:37640\n"
"v_add_u32 v9, 0x10000, v9\n"
"v_mfma_f32_16x16x32_bf16 v[64:67], v[228:231], a[220:223], v[64:67]\n"
"v_mul_f32 v243, v249, v243\n"
"v_cvt_pk_bf16_f32 v240, v240, v241\n"
"v_mfma_f32_16x16x32_bf16 v[76:79], v[224:227], a[224:227], 0\n"
"s_add_u32 s65, s74, 71680\n"
"v_mul_f32 v250, v249, v250\n"
"v_mfma_f32_16x16x32_bf16 v[76:79], v[228:231], a[228:231], v[76:79]\n"
"buffer_load_dwordx4 v4, s[44:47], s65 offen offset:2048 lds\n"
"v_mfma_f32_16x16x32_bf16 v[92:95], v[224:227], a[232:235], 0\n"
"v_mul_f32 v251, v249, v251\n"
"v_mul_f32 v252, v249, v252\n"
"v_mfma_f32_16x16x32_bf16 v[92:95], v[228:231], a[236:239], v[92:95]\n"
"v_mul_f32 v253, v249, v253\n"
"v_cvt_pk_bf16_f32 v241, v242, v243\n"
"v_mfma_f32_16x16x32_bf16 v[108:111], v[224:227], a[240:243], 0\n"
"v_cvt_pk_bf16_f32 v242, v250, v251\n"
"v_cvt_pk_bf16_f32 v243, v252, v253\n"
"v_mfma_f32_16x16x32_bf16 v[108:111], v[228:231], a[244:247], v[108:111]\n"
"s_add_u32 s73, s59, 393216\n"
"v_accvgpr_read_b32 v250, a52\n"
"v_mfma_f32_16x16x32_bf16 v[124:127], v[224:227], a[248:251], 0\n"
"buffer_store_dwordx4 v[240:243], v11, s[52:55], s73 offen offset:2048 sc1\n"
"v_mfma_f32_16x16x32_bf16 v[124:127], v[228:231], a[252:255], v[124:127]\n"
"s_nop 0\n"
"v_accvgpr_read_b32 v240, a48\n"
"v_accvgpr_read_b32 v241, a49\n"
"v_mfma_f32_16x16x32_bf16 v[196:199], v[224:227], v[232:235], 0\n"
"s_waitcnt vmcnt(8) lgkmcnt(0)\n"
"s_barrier\n"
"v_mfma_f32_16x16x32_bf16 v[196:199], v[228:231], v[236:239], v[196:199]\n"
"ds_read_b64_tr_b16 v[224:225], v9 offset:0\n"
"ds_read_b64_tr_b16 v[226:227], v9 offset:512\n"
"v_mfma_f32_16x16x32_bf16 v[188:191], v[208:211], a[216:219], 0\n"
"ds_read_b64_tr_b16 v[228:229], v9 offset:4096\n"
"ds_read_b64_tr_b16 v[230:231], v9 offset:4608\n"
"v_mfma_f32_16x16x32_bf16 v[188:191], v[212:215], a[220:223], v[188:191]\n"
"v_accvgpr_read_b32 v242, a50\n"
"v_accvgpr_read_b32 v243, a51\n"
"v_mfma_f32_16x16x32_bf16 v[80:83], v[208:211], a[224:227], 0\n"
"v_mul_f32 v240, v244, v240\n"
"v_mul_f32 v241, v244, v241\n"
"v_mfma_f32_16x16x32_bf16 v[80:83], v[212:215], a[228:231], v[80:83]\n"
"v_accvgpr_read_b32 v251, a53\n"
"v_accvgpr_read_b32 v252, a54\n"
"v_mfma_f32_16x16x32_bf16 v[96:99], v[208:211], a[232:235], 0\n"
"v_accvgpr_read_b32 v253, a55\n"
"v_mul_f32 v242, v244, v242\n"
"v_mfma_f32_16x16x32_bf16 v[96:99], v[212:215], a[236:239], v[96:99]\n"
"s_add_u32 s75, s58, s83\n"
"s_add_u32 m0, s60, 49152\n"
"v_mfma_f32_16x16x32_bf16 v[112:115], v[208:211], a[240:243], 0\n"
"s_add_u32 s66, s75, 0\n"
"v_mul_f32 v243, v244, v243\n"
"v_mfma_f32_16x16x32_bf16 v[112:115], v[212:215], a[244:247], v[112:115]\n"
"buffer_load_dwordx4 v3, s[48:51], s66 offen offset:0 lds\n"
"v_mfma_f32_16x16x32_bf16 v[128:131], v[208:211], a[248:251], 0\n"
"v_cvt_pk_bf16_f32 v240, v240, v241\n"
"v_mul_f32 v250, v244, v250\n"
"v_mfma_f32_16x16x32_bf16 v[128:131], v[212:215], a[252:255], v[128:131]\n"
"v_mul_f32 v251, v244, v251\n"
"v_mul_f32 v252, v244, v252\n"
"v_mfma_f32_16x16x32_bf16 v[200:203], v[208:211], v[232:235], 0\n"
"s_add_u32 s67, s75, 64512\n"
"ds_read_b64_tr_b16 v[208:209], v9 offset:8\n"
"v_mfma_f32_16x16x32_bf16 v[200:203], v[212:215], v[236:239], v[200:203]\n"
"buffer_load_dwordx4 v3, s[48:51], s67 offen offset:1024 lds\n"
"v_mfma_f32_16x16x32_bf16 v[68:71], v[216:219], a[216:219], 0\n"
"ds_read_b64_tr_b16 v[210:211], v9 offset:520\n"
"ds_read_b64_tr_b16 v[212:213], v9 offset:4104\n"
"v_mfma_f32_16x16x32_bf16 v[68:71], v[220:223], a[220:223], v[68:71]\n"
"ds_read_b64_tr_b16 v[214:215], v9 offset:4616\n"
"ds_read_b128 a[216:219], v10 offset:0\n"
"v_mfma_f32_16x16x32_bf16 v[84:87], v[216:219], a[224:227], 0\n"
"ds_read_b128 a[220:223], v10 offset:512\n"
"ds_read_b128 a[224:227], v10 offset:2048\n"
"v_mfma_f32_16x16x32_bf16 v[84:87], v[220:223], a[228:231], v[84:87]\n"
"ds_read_b128 a[228:231], v10 offset:2560\n"
"v_mul_f32 v253, v244, v253\n"
"v_mfma_f32_16x16x32_bf16 v[100:103], v[216:219], a[232:235], 0\n"
"s_add_u32 s68, s75, 129024\n"
"ds_read_b128 a[232:235], v10 offset:4096\n"
"v_mfma_f32_16x16x32_bf16 v[100:103], v[220:223], a[236:239], v[100:103]\n"
"buffer_load_dwordx4 v3, s[48:51], s68 offen offset:2048 lds\n"
"v_mfma_f32_16x16x32_bf16 v[116:119], v[216:219], a[240:243], 0\n"
"ds_read_b128 a[236:239], v10 offset:4608\n"
"v_add_u32 v10, 0xffff0000, v10\n"
"v_mfma_f32_16x16x32_bf16 v[116:119], v[220:223], a[244:247], v[116:119]\n"
"v_cvt_pk_bf16_f32 v241, v242, v243\n"
"v_cvt_pk_bf16_f32 v242, v250, v251\n"
"v_mfma_f32_16x16x32_bf16 v[132:135], v[216:219], a[248:251], 0\n"
"v_cvt_pk_bf16_f32 v243, v252, v253\n"
"s_add_u32 s72, s59, 3072\n"
"v_mfma_f32_16x16x32_bf16 v[132:135], v[220:223], a[252:255], v[132:135]\n"
"s_add_u32 s69, s75, 193536\n"
"v_accvgpr_read_b32 v250, a60\n"
"v_mfma_f32_16x16x32_bf16 v[204:207], v[216:219], v[232:235], 0\n"
"buffer_load_dwordx4 v3, s[48:51], s69 offen offset:3072 lds\n"
"v_mfma_f32_16x16x32_bf16 v[204:207], v[220:223], v[236:239], v[204:207]\n"
"ds_read_b64_tr_b16 v[216:217], v9 offset:256\n"
"s_waitcnt lgkmcnt(5)\n"
"v_mfma_f32_16x16x32_bf16 v[136:139], v[224:227], a[0:3], v[136:139]\n"
"ds_read_b64_tr_b16 v[218:219], v9 offset:768\n"
"ds_read_b64_tr_b16 v[220:221], v9 offset:4352\n"
"v_mfma_f32_16x16x32_bf16 v[136:139], v[228:231], a[4:7], v[136:139]\n"
"ds_read_b64_tr_b16 v[222:223], v9 offset:4864\n"
"v_accvgpr_read_b32 v251, a61\n"
"v_mfma_f32_16x16x32_bf16 v[140:143], v[224:227], a[8:11], v[140:143]\n"
"buffer_store_dwordx4 v[240:243], v11, s[52:55], s72 offen offset:0 sc1\n"
"v_mfma_f32_16x16x32_bf16 v[140:143], v[228:231], a[12:15], v[140:143]\n"
"s_add_u32 s74, s57, s82\n"
"s_add_u32 m0, s61, 32768\n"
"v_mfma_f32_16x16x32_bf16 v[144:147], v[224:227], a[16:19], v[144:147]\n"
"s_add_u32 s70, s74, 0\n"
"v_accvgpr_read_b32 v240, a56\n"
"v_mfma_f32_16x16x32_bf16 v[144:147], v[228:231], a[20:23], v[144:147]\n"
"buffer_load_dwordx4 v4, s[44:47], s70 offen offset:0 lds\n"
"v_mfma_f32_16x16x32_bf16 v[148:151], v[224:227], a[24:27], v[148:151]\n"
"v_accvgpr_read_b32 v241, a57\n"
"v_accvgpr_read_b32 v242, a58\n"
"v_mfma_f32_16x16x32_bf16 v[148:151], v[228:231], a[28:31], v[148:151]\n"
"v_accvgpr_read_b32 v243, a59\n"
"v_mul_f32 v240, v245, v240\n"
"v_mfma_f32_16x16x32_bf16 v[152:155], v[224:227], a[32:35], v[152:155]\n"
"s_add_u32 s71, s74, 8192\n"
"v_mul_f32 v241, v245, v241\n"
"v_mfma_f32_16x16x32_bf16 v[152:155], v[228:231], a[36:39], v[152:155]\n"
"buffer_load_dwordx4 v5, s[44:47], s71 offen offset:1024 lds\n"
"v_mfma_f32_16x16x32_bf16 v[156:159], v[224:227], a[40:43], v[156:159]\n"
"ds_read_b64_tr_b16 v[224:225], v9 offset:264\n"
"ds_read_b64_tr_b16 v[226:227], v9 offset:776\n"
"v_mfma_f32_16x16x32_bf16 v[156:159], v[228:231], a[44:47], v[156:159]\n"
"ds_read_b64_tr_b16 v[228:229], v9 offset:4360\n"
"ds_read_b64_tr_b16 v[230:231], v9 offset:4872\n"
"v_mfma_f32_16x16x32_bf16 v[16:19], v[208:211], a[0:3], v[16:19]\n"
"v_add_u32 v9, 0xffff0000, v9\n"
"v_accvgpr_read_b32 v252, a62\n"
"v_mfma_f32_16x16x32_bf16 v[16:19], v[212:215], a[4:7], v[16:19]\n"
"v_accvgpr_read_b32 v253, a63\n"
"v_mul_f32 v242, v245, v242\n"
"v_mfma_f32_16x16x32_bf16 v[20:23], v[208:211], a[8:11], v[20:23]\n"
"s_add_u32 s64, s74, 16384\n"
"v_mul_f32 v243, v245, v243\n"
"v_mfma_f32_16x16x32_bf16 v[20:23], v[212:215], a[12:15], v[20:23]\n"
"buffer_load_dwordx4 v4, s[44:47], s64 offen offset:2048 lds\n"
"v_mfma_f32_16x16x32_bf16 v[24:27], v[208:211], a[16:19], v[24:27]\n"
"v_cvt_pk_bf16_f32 v240, v240, v241\n"
"v_mul_f32 v250, v245, v250\n"
"v_mfma_f32_16x16x32_bf16 v[24:27], v[212:215], a[20:23], v[24:27]\n"
"v_mul_f32 v251, v245, v251\n"
"v_mul_f32 v252, v245, v252\n"
"v_mfma_f32_16x16x32_bf16 v[28:31], v[208:211], a[24:27], v[28:31]\n"
"v_mul_f32 v253, v245, v253\n"
"v_cvt_pk_bf16_f32 v241, v242, v243\n"
"v_mfma_f32_16x16x32_bf16 v[28:31], v[212:215], a[28:31], v[28:31]\n"
"v_cvt_pk_bf16_f32 v242, v250, v251\n"
"v_cvt_pk_bf16_f32 v243, v252, v253\n"
"v_mfma_f32_16x16x32_bf16 v[32:35], v[208:211], a[32:35], v[32:35]\n"
"s_add_u32 s73, s59, 3072\n"
"v_accvgpr_read_b32 v250, a68\n"
"v_mfma_f32_16x16x32_bf16 v[32:35], v[212:215], a[36:39], v[32:35]\n"
"s_waitcnt vmcnt(9) lgkmcnt(0)\n"
"s_barrier\n"
"v_mfma_f32_16x16x32_bf16 v[36:39], v[208:211], a[40:43], v[36:39]\n"
"ds_read_b64_tr_b16 v[208:209], v9 offset:0\n"
"ds_read_b64_tr_b16 v[210:211], v9 offset:512\n"
"v_mfma_f32_16x16x32_bf16 v[36:39], v[212:215], a[44:47], v[36:39]\n"
"ds_read_b64_tr_b16 v[212:213], v9 offset:4096\n"
"ds_read_b64_tr_b16 v[214:215], v9 offset:4608\n"
"v_mfma_f32_16x16x32_bf16 v[160:163], v[216:219], a[0:3], v[160:163]\n"
"ds_read_b128 a[240:243], v10 offset:0\n"
"ds_read_b128 a[244:247], v10 offset:512\n"
"v_mfma_f32_16x16x32_bf16 v[160:163], v[220:223], a[4:7], v[160:163]\n"
"ds_read_b128 a[248:251], v10 offset:2048\n"
"ds_read_b128 a[252:255], v10 offset:2560\n"
"v_mfma_f32_16x16x32_bf16 v[164:167], v[216:219], a[8:11], v[164:167]\n"
"ds_read_b128 v[232:235], v10 offset:4096\n"
"ds_read_b128 v[236:239], v10 offset:4608\n"
"v_mfma_f32_16x16x32_bf16 v[164:167], v[220:223], a[12:15], v[164:167]\n"
"buffer_store_dwordx4 v[240:243], v11, s[52:55], s73 offen offset:1024 sc1\n"
"v_mfma_f32_16x16x32_bf16 v[168:171], v[216:219], a[16:19], v[168:171]\n"
"s_nop 0\n"
"v_accvgpr_read_b32 v240, a64\n"
"v_accvgpr_read_b32 v241, a65\n"
"v_mfma_f32_16x16x32_bf16 v[168:171], v[220:223], a[20:23], v[168:171]\n"
"s_add_u32 s75, s58, s83\n"
"s_add_u32 m0, s60, 81920\n"
"v_mfma_f32_16x16x32_bf16 v[172:175], v[216:219], a[24:27], v[172:175]\n"
"s_add_u32 s65, s75, 128\n"
"v_accvgpr_read_b32 v242, a66\n"
"v_mfma_f32_16x16x32_bf16 v[172:175], v[220:223], a[28:31], v[172:175]\n"
"buffer_load_dwordx4 v3, s[48:51], s65 offen offset:0 lds\n"
"v_mfma_f32_16x16x32_bf16 v[176:179], v[216:219], a[32:35], v[176:179]\n"
"s_add_u32 s83, s83, 524288\n"
"s_cmp_eq_u32 s83, 4718592\n"
"s_cselect_b32 s83, 0, s83\n"
"v_mfma_f32_16x16x32_bf16 v[176:179], v[220:223], a[36:39], v[176:179]\n"
"v_accvgpr_read_b32 v243, a67\n"
"v_mul_f32 v240, v246, v240\n"
"v_mfma_f32_16x16x32_bf16 v[180:183], v[216:219], a[40:43], v[180:183]\n"
"s_add_u32 s66, s75, 64640\n"
"ds_read_b64_tr_b16 v[216:217], v9 offset:8\n"
"v_mfma_f32_16x16x32_bf16 v[180:183], v[220:223], a[44:47], v[180:183]\n"
"buffer_load_dwordx4 v3, s[48:51], s66 offen offset:1024 lds\n"
"v_mfma_f32_16x16x32_bf16 v[40:43], v[224:227], a[0:3], v[40:43]\n"
"ds_read_b64_tr_b16 v[218:219], v9 offset:520\n"
"ds_read_b64_tr_b16 v[220:221], v9 offset:4104\n"
"v_mfma_f32_16x16x32_bf16 v[40:43], v[228:231], a[4:7], v[40:43]\n"
"ds_read_b64_tr_b16 v[222:223], v9 offset:4616\n"
"v_mul_f32 v241, v246, v241\n"
"v_mfma_f32_16x16x32_bf16 v[44:47], v[224:227], a[8:11], v[44:47]\n"
"v_accvgpr_read_b32 v251, a69\n"
"v_accvgpr_read_b32 v252, a70\n"
"v_mfma_f32_16x16x32_bf16 v[44:47], v[228:231], a[12:15], v[44:47]\n"
"v_accvgpr_read_b32 v253, a71\n"
"v_mul_f32 v242, v246, v242\n"
"v_mfma_f32_16x16x32_bf16 v[48:51], v[224:227], a[16:19], v[48:51]\n"
"s_add_u32 s67, s75, 129152\n"
"v_mul_f32 v243, v246, v243\n"
"v_mfma_f32_16x16x32_bf16 v[48:51], v[228:231], a[20:23], v[48:51]\n"
"buffer_load_dwordx4 v3, s[48:51], s67 offen offset:2048 lds\n"
"v_mfma_f32_16x16x32_bf16 v[52:55], v[224:227], a[24:27], v[52:55]\n"
"v_cvt_pk_bf16_f32 v240, v240, v241\n"
"v_mul_f32 v250, v246, v250\n"
"v_mfma_f32_16x16x32_bf16 v[52:55], v[228:231], a[28:31], v[52:55]\n"
"v_mul_f32 v251, v246, v251\n"
"v_mul_f32 v252, v246, v252\n"
"v_mfma_f32_16x16x32_bf16 v[56:59], v[224:227], a[32:35], v[56:59]\n"
"v_mul_f32 v253, v246, v253\n"
"v_cvt_pk_bf16_f32 v241, v242, v243\n"
"v_mfma_f32_16x16x32_bf16 v[56:59], v[228:231], a[36:39], v[56:59]\n"
"s_add_u32 s68, s75, 193664\n"
"v_cvt_pk_bf16_f32 v242, v250, v251\n"
"v_mfma_f32_16x16x32_bf16 v[60:63], v[224:227], a[40:43], v[60:63]\n"
"buffer_load_dwordx4 v3, s[48:51], s68 offen offset:3072 lds\n"
"v_mfma_f32_16x16x32_bf16 v[60:63], v[228:231], a[44:47], v[60:63]\n"
"ds_read_b64_tr_b16 v[224:225], v9 offset:256\n"
"s_waitcnt lgkmcnt(1)\n"
"v_mfma_f32_16x16x32_bf16 v[184:187], v[208:211], a[0:3], v[184:187]\n"
"ds_read_b64_tr_b16 v[226:227], v9 offset:768\n"
"ds_read_b64_tr_b16 v[228:229], v9 offset:4352\n"
"v_mfma_f32_16x16x32_bf16 v[184:187], v[212:215], a[4:7], v[184:187]\n"
"ds_read_b64_tr_b16 v[230:231], v9 offset:4864\n"
"v_cvt_pk_bf16_f32 v243, v252, v253\n"
"v_mfma_f32_16x16x32_bf16 v[72:75], v[208:211], a[8:11], v[72:75]\n"
"s_add_u32 s72, s59, 3072\n"
"v_accvgpr_read_b32 v250, a76\n"
"v_mfma_f32_16x16x32_bf16 v[72:75], v[212:215], a[12:15], v[72:75]\n"
"s_add_u32 s74, s57, s82\n"
"s_add_u32 m0, s61, 65536\n"
"v_mfma_f32_16x16x32_bf16 v[88:91], v[208:211], a[16:19], v[88:91]\n"
"s_add_u32 s69, s74, 55296\n"
"v_accvgpr_read_b32 v251, a77\n"
"v_mfma_f32_16x16x32_bf16 v[88:91], v[212:215], a[20:23], v[88:91]\n"
"buffer_load_dwordx4 v4, s[44:47], s69 offen offset:0 lds\n"
"v_mfma_f32_16x16x32_bf16 v[104:107], v[208:211], a[24:27], v[104:107]\n"
"buffer_store_dwordx4 v[240:243], v11, s[52:55], s72 offen offset:2048 sc1\n"
"v_mfma_f32_16x16x32_bf16 v[104:107], v[212:215], a[28:31], v[104:107]\n"
"s_nop 0\n"
"v_accvgpr_read_b32 v240, a72\n"
"v_accvgpr_read_b32 v241, a73\n"
"v_mfma_f32_16x16x32_bf16 v[120:123], v[208:211], a[32:35], v[120:123]\n"
"s_add_u32 s70, s74, 63488\n"
"v_accvgpr_read_b32 v242, a74\n"
"v_mfma_f32_16x16x32_bf16 v[120:123], v[212:215], a[36:39], v[120:123]\n"
"buffer_load_dwordx4 v5, s[44:47], s70 offen offset:1024 lds\n"
"v_mfma_f32_16x16x32_bf16 v[192:195], v[208:211], a[40:43], v[192:195]\n"
"ds_read_b64_tr_b16 v[208:209], v9 offset:264\n"
"ds_read_b64_tr_b16 v[210:211], v9 offset:776\n"
"v_mfma_f32_16x16x32_bf16 v[192:195], v[212:215], a[44:47], v[192:195]\n"
"ds_read_b64_tr_b16 v[212:213], v9 offset:4360\n"
"ds_read_b64_tr_b16 v[214:215], v9 offset:4872\n"
"v_mfma_f32_16x16x32_bf16 v[64:67], v[216:219], a[0:3], v[64:67]\n"
"v_accvgpr_read_b32 v243, a75\n"
"v_accvgpr_read_b32 v252, a78\n"
"v_mfma_f32_16x16x32_bf16 v[64:67], v[220:223], a[4:7], v[64:67]\n"
"v_accvgpr_read_b32 v253, a79\n"
"v_mul_f32 v240, v247, v240\n"
"v_mfma_f32_16x16x32_bf16 v[76:79], v[216:219], a[8:11], v[76:79]\n"
"s_add_u32 s71, s74, 71680\n"
"v_mul_f32 v241, v247, v241\n"
"v_mfma_f32_16x16x32_bf16 v[76:79], v[220:223], a[12:15], v[76:79]\n"
"buffer_load_dwordx4 v4, s[44:47], s71 offen offset:2048 lds\n"
"v_mfma_f32_16x16x32_bf16 v[92:95], v[216:219], a[16:19], v[92:95]\n"
"v_mul_f32 v242, v247, v242\n"
"v_mul_f32 v243, v247, v243\n"
"v_mfma_f32_16x16x32_bf16 v[92:95], v[220:223], a[20:23], v[92:95]\n"
"v_cvt_pk_bf16_f32 v240, v240, v241\n"
"v_mul_f32 v250, v247, v250\n"
"v_mfma_f32_16x16x32_bf16 v[108:111], v[216:219], a[24:27], v[108:111]\n"
"v_mul_f32 v251, v247, v251\n"
"v_mul_f32 v252, v247, v252\n"
"v_mfma_f32_16x16x32_bf16 v[108:111], v[220:223], a[28:31], v[108:111]\n"
"v_mul_f32 v253, v247, v253\n"
"v_cvt_pk_bf16_f32 v241, v242, v243\n"
"v_mfma_f32_16x16x32_bf16 v[124:127], v[216:219], a[32:35], v[124:127]\n"
"v_cvt_pk_bf16_f32 v242, v250, v251\n"
"v_cvt_pk_bf16_f32 v243, v252, v253\n"
"v_mfma_f32_16x16x32_bf16 v[124:127], v[220:223], a[36:39], v[124:127]\n"
"s_waitcnt vmcnt(9) lgkmcnt(0)\n"
"s_barrier\n"
"v_mfma_f32_16x16x32_bf16 v[196:199], v[216:219], a[40:43], v[196:199]\n"
"ds_read_b64_tr_b16 a[72:73], v9 offset:32768\n"
"ds_read_b64_tr_b16 a[74:75], v9 offset:33280\n"
"v_mfma_f32_16x16x32_bf16 v[196:199], v[220:223], a[44:47], v[196:199]\n"
"ds_read_b64_tr_b16 a[76:77], v9 offset:36864\n"
"ds_read_b64_tr_b16 a[78:79], v9 offset:37376\n"
"v_mfma_f32_16x16x32_bf16 v[188:191], v[224:227], a[0:3], v[188:191]\n"
"s_add_u32 s73, s59, 396288\n"
"v_accvgpr_read_b32 v250, a84\n"
"v_mfma_f32_16x16x32_bf16 v[188:191], v[228:231], a[4:7], v[188:191]\n"
"buffer_store_dwordx4 v[240:243], v11, s[52:55], s73 offen offset:0 sc1\n"
"v_mfma_f32_16x16x32_bf16 v[80:83], v[224:227], a[8:11], v[80:83]\n"
"s_nop 0\n"
"v_accvgpr_read_b32 v240, a80\n"
"v_accvgpr_read_b32 v241, a81\n"
"v_mfma_f32_16x16x32_bf16 v[80:83], v[228:231], a[12:15], v[80:83]\n"
"v_accvgpr_read_b32 v242, a82\n"
"v_accvgpr_read_b32 v243, a83\n"
"v_mfma_f32_16x16x32_bf16 v[96:99], v[224:227], a[16:19], v[96:99]\n"
"ds_read_b64_tr_b16 a[80:81], v9 offset:32776\n"
"ds_read_b64_tr_b16 a[82:83], v9 offset:33288\n"
"v_mfma_f32_16x16x32_bf16 v[96:99], v[228:231], a[20:23], v[96:99]\n"
"s_add_u32 s75, s58, s83\n"
"s_add_u32 m0, s60, 16384\n"
"v_mfma_f32_16x16x32_bf16 v[112:115], v[224:227], a[24:27], v[112:115]\n"
"s_add_u32 s64, s75, 0\n"
"v_accvgpr_read_b32 v251, a85\n"
"v_mfma_f32_16x16x32_bf16 v[112:115], v[228:231], a[28:31], v[112:115]\n"
"buffer_load_dwordx4 v3, s[48:51], s64 offen offset:0 lds\n"
"v_mfma_f32_16x16x32_bf16 v[128:131], v[224:227], a[32:35], v[128:131]\n"
"v_accvgpr_read_b32 v252, a86\n"
"v_accvgpr_read_b32 v253, a87\n"
"v_mfma_f32_16x16x32_bf16 v[128:131], v[228:231], a[36:39], v[128:131]\n"
"ds_read_b64_tr_b16 a[84:85], v9 offset:36872\n"
"ds_read_b64_tr_b16 a[86:87], v9 offset:37384\n"
"v_mfma_f32_16x16x32_bf16 v[200:203], v[224:227], a[40:43], v[200:203]\n"
"s_add_u32 s65, s75, 64512\n"
"v_mul_f32 v240, v248, v240\n"
"v_mfma_f32_16x16x32_bf16 v[200:203], v[228:231], a[44:47], v[200:203]\n"
"buffer_load_dwordx4 v3, s[48:51], s65 offen offset:1024 lds\n"
"v_mfma_f32_16x16x32_bf16 v[68:71], v[208:211], a[0:3], v[68:71]\n"
"s_add_u32 s82, s82, 128\n"
"s_cmp_eq_u32 s82, 1152\n"
"s_cselect_b32 s82, 0, s82\n"
"v_mfma_f32_16x16x32_bf16 v[68:71], v[212:215], a[4:7], v[68:71]\n"
"v_mul_f32 v241, v248, v241\n"
"v_mul_f32 v242, v248, v242\n"
"v_mfma_f32_16x16x32_bf16 v[84:87], v[208:211], a[8:11], v[84:87]\n"
"v_mul_f32 v243, v248, v243\n"
"v_cvt_pk_bf16_f32 v240, v240, v241\n"
"v_mfma_f32_16x16x32_bf16 v[84:87], v[212:215], a[12:15], v[84:87]\n"
"v_mul_f32 v250, v248, v250\n"
"v_mul_f32 v251, v248, v251\n"
"v_mfma_f32_16x16x32_bf16 v[100:103], v[208:211], a[16:19], v[100:103]\n"
"s_add_u32 s66, s75, 129024\n"
"v_mul_f32 v252, v248, v252\n"
"v_mfma_f32_16x16x32_bf16 v[100:103], v[212:215], a[20:23], v[100:103]\n"
"buffer_load_dwordx4 v3, s[48:51], s66 offen offset:2048 lds\n"
"v_mfma_f32_16x16x32_bf16 v[116:119], v[208:211], a[24:27], v[116:119]\n"
"v_mul_f32 v253, v248, v253\n"
"v_cvt_pk_bf16_f32 v241, v242, v243\n"
"v_mfma_f32_16x16x32_bf16 v[116:119], v[212:215], a[28:31], v[116:119]\n"
"v_cvt_pk_bf16_f32 v242, v250, v251\n"
"v_cvt_pk_bf16_f32 v243, v252, v253\n"
"v_mfma_f32_16x16x32_bf16 v[132:135], v[208:211], a[32:35], v[132:135]\n"
"s_add_u32 s72, s59, 396288\n"
"v_accvgpr_read_b32 v250, a92\n"
"v_mfma_f32_16x16x32_bf16 v[132:135], v[212:215], a[36:39], v[132:135]\n"
"s_add_u32 s67, s75, 193536\n"
"v_accvgpr_read_b32 v251, a93\n"
"v_mfma_f32_16x16x32_bf16 v[204:207], v[208:211], a[40:43], v[204:207]\n"
"buffer_load_dwordx4 v3, s[48:51], s67 offen offset:3072 lds\n"
"v_mfma_f32_16x16x32_bf16 v[204:207], v[212:215], a[44:47], v[204:207]\n"
"v_accvgpr_read_b32 v252, a94\n"
"s_waitcnt lgkmcnt(0)\n"
"v_mfma_f32_16x16x32_bf16 v[136:139], a[72:75], a[216:219], v[136:139]\n"
"buffer_store_dwordx4 v[240:243], v11, s[52:55], s72 offen offset:1024 sc1\n"
"v_mfma_f32_16x16x32_bf16 v[136:139], a[76:79], a[220:223], v[136:139]\n"
"s_nop 0\n"
"v_accvgpr_read_b32 v240, a88\n"
"v_accvgpr_read_b32 v241, a89\n"
"v_mfma_f32_16x16x32_bf16 v[140:143], a[72:75], a[224:227], v[140:143]\n"
"v_accvgpr_read_b32 v242, a90\n"
"v_accvgpr_read_b32 v243, a91\n"
"v_mfma_f32_16x16x32_bf16 v[140:143], a[76:79], a[228:231], v[140:143]\n"
"s_add_u32 s74, s57, s82\n"
"s_add_u32 m0, s61, 0\n"
"v_mfma_f32_16x16x32_bf16 v[144:147], a[72:75], a[232:235], v[144:147]\n"
"s_add_u32 s68, s74, 0\n"
"ds_read_b64_tr_b16 a[88:89], v9 offset:33024\n"
"v_mfma_f32_16x16x32_bf16 v[144:147], a[76:79], a[236:239], v[144:147]\n"
"buffer_load_dwordx4 v4, s[44:47], s68 offen offset:0 lds\n"
"v_mfma_f32_16x16x32_bf16 v[148:151], a[72:75], a[240:243], v[148:151]\n"
"ds_read_b64_tr_b16 a[90:91], v9 offset:33536\n"
"v_accvgpr_read_b32 v253, a95\n"
"v_mfma_f32_16x16x32_bf16 v[148:151], a[76:79], a[244:247], v[148:151]\n"
"ds_read_b64_tr_b16 a[92:93], v9 offset:37120\n"
"ds_read_b64_tr_b16 a[94:95], v9 offset:37632\n"
"v_mfma_f32_16x16x32_bf16 v[152:155], a[72:75], a[248:251], v[152:155]\n"
"s_add_u32 s69, s74, 8192\n"
"v_mul_f32 v240, v249, v240\n"
"v_mfma_f32_16x16x32_bf16 v[152:155], a[76:79], a[252:255], v[152:155]\n"
"buffer_load_dwordx4 v5, s[44:47], s69 offen offset:1024 lds\n"
"v_mfma_f32_16x16x32_bf16 v[156:159], a[72:75], v[232:235], v[156:159]\n"
"v_mul_f32 v241, v249, v241\n"
"v_mul_f32 v242, v249, v242\n"
"v_mfma_f32_16x16x32_bf16 v[156:159], a[76:79], v[236:239], v[156:159]\n"
"v_mul_f32 v243, v249, v243\n"
"v_cvt_pk_bf16_f32 v240, v240, v241\n"
"v_mfma_f32_16x16x32_bf16 v[16:19], a[80:83], a[216:219], v[16:19]\n"
"v_mul_f32 v250, v249, v250\n"
"v_mul_f32 v251, v249, v251\n"
"v_mfma_f32_16x16x32_bf16 v[16:19], a[84:87], a[220:223], v[16:19]\n"
"v_mul_f32 v252, v249, v252\n"
"v_mul_f32 v253, v249, v253\n"
"v_mfma_f32_16x16x32_bf16 v[20:23], a[80:83], a[224:227], v[20:23]\n"
"s_add_u32 s70, s74, 16384\n"
"v_cvt_pk_bf16_f32 v241, v242, v243\n"
"v_mfma_f32_16x16x32_bf16 v[20:23], a[84:87], a[228:231], v[20:23]\n"
"buffer_load_dwordx4 v4, s[44:47], s70 offen offset:2048 lds\n"
"v_mfma_f32_16x16x32_bf16 v[24:27], a[80:83], a[232:235], v[24:27]\n"
"v_cvt_pk_bf16_f32 v242, v250, v251\n"
"v_cvt_pk_bf16_f32 v243, v252, v253\n"
"v_mfma_f32_16x16x32_bf16 v[24:27], a[84:87], a[236:239], v[24:27]\n"
"s_add_u32 s73, s59, 396288\n"
"v_lshrrev_b32 v14, 6, v11\n"
"v_mfma_f32_16x16x32_bf16 v[28:31], a[80:83], a[240:243], v[28:31]\n"
"buffer_store_dwordx4 v[240:243], v11, s[52:55], s73 offen offset:2048 sc1\n"
"v_mfma_f32_16x16x32_bf16 v[28:31], a[84:87], a[244:247], v[28:31]\n"
"v_lshlrev_b32 v14, 7, v14\n"
"v_bfe_u32 v13, v11, 4, 2\n"
"v_mfma_f32_16x16x32_bf16 v[32:35], a[80:83], a[248:251], v[32:35]\n"
"s_mul_i32 s71, s81, 992\n"
"v_lshl_add_u32 v14, v13, 3, v14\n"
"v_mfma_f32_16x16x32_bf16 v[32:35], a[84:87], a[252:255], v[32:35]\n"
"s_sub_u32 s71, s90, s71\n"
"v_accvgpr_read_b32 v240, a96\n"
"v_mfma_f32_16x16x32_bf16 v[36:39], a[80:83], v[232:235], v[36:39]\n"
"v_accvgpr_read_b32 v241, a97\n"
"v_accvgpr_read_b32 v242, a98\n"
"v_mfma_f32_16x16x32_bf16 v[36:39], a[84:87], v[236:239], v[36:39]\n"
"v_accvgpr_read_b32 v243, a99\n"
"s_waitcnt lgkmcnt(0)\n"
"v_mfma_f32_16x16x32_bf16 v[160:163], a[88:91], a[216:219], v[160:163]\n"
"v_accvgpr_read_b32 v250, a100\n"
"v_accvgpr_read_b32 v251, a101\n"
"v_mfma_f32_16x16x32_bf16 v[160:163], a[92:95], a[220:223], v[160:163]\n"
"v_accvgpr_read_b32 v252, a102\n"
"v_accvgpr_read_b32 v253, a103\n"
"v_add_u32 v14, s71, v14\n"
"ds_read_b64_tr_b16 a[48:49], v14 offset:0\n"
"ds_read_b64_tr_b16 a[50:51], v14 offset:2048\n"
"ds_read_b64_tr_b16 a[52:53], v14 offset:4096\n"
"ds_read_b64_tr_b16 a[54:55], v14 offset:6144\n"
"ds_read_b64_tr_b16 a[56:57], v14 offset:8192\n"
"ds_read_b64_tr_b16 a[58:59], v14 offset:10240\n"
"ds_read_b64_tr_b16 a[60:61], v14 offset:12288\n"
"ds_read_b64_tr_b16 a[62:63], v14 offset:14336\n"
"ds_read_b64_tr_b16 a[64:65], v14 offset:16384\n"
"ds_read_b64_tr_b16 a[66:67], v14 offset:18432\n"
"ds_read_b128 a[0:3], v10 offset:32768\n"
"ds_read_b128 a[4:7], v10 offset:33280\n"
"ds_read_b128 a[8:11], v10 offset:34816\n"
"ds_read_b128 a[12:15], v10 offset:35328\n"
"ds_read_b128 a[16:19], v10 offset:36864\n"
"ds_read_b128 a[20:23], v10 offset:37376\n"
"ds_read_b64_tr_b16 a[96:97], v9 offset:33032\n"
"ds_read_b64_tr_b16 a[98:99], v9 offset:33544\n"
"ds_read_b64_tr_b16 a[100:101], v9 offset:37128\n"
"ds_read_b64_tr_b16 a[102:103], v9 offset:37640\n"
"v_mfma_f32_16x16x32_bf16 v[164:167], a[88:91], a[224:227], v[164:167]\n"
"v_mul_f32 v240, v244, v240\n"
"v_mul_f32 v241, v244, v241\n"
"v_mfma_f32_16x16x32_bf16 v[164:167], a[92:95], a[228:231], v[164:167]\n"
"v_mul_f32 v242, v244, v242\n"
"v_mul_f32 v243, v244, v243\n"
"v_mfma_f32_16x16x32_bf16 v[168:171], a[88:91], a[232:235], v[168:171]\n"
"s_add_u32 m0, s60, 98304\n"
"s_add_u32 s64, s56, 0\n"
"v_mfma_f32_16x16x32_bf16 v[168:171], a[92:95], a[236:239], v[168:171]\n"
"v_cvt_pk_bf16_f32 v240, v240, v241\n"
"v_mul_f32 v250, v244, v250\n"
"v_mfma_f32_16x16x32_bf16 v[172:175], a[88:91], a[240:243], v[172:175]\n"
"v_mul_f32 v251, v244, v251\n"
"v_mul_f32 v252, v244, v252\n"
"v_mfma_f32_16x16x32_bf16 v[172:175], a[92:95], a[244:247], v[172:175]\n"
"v_mul_f32 v253, v244, v253\n"
"v_cvt_pk_bf16_f32 v241, v242, v243\n"
"v_mfma_f32_16x16x32_bf16 v[176:179], a[88:91], a[248:251], v[176:179]\n"
"v_cvt_pk_bf16_f32 v242, v250, v251\n"
"v_cvt_pk_bf16_f32 v243, v252, v253\n"
"v_mfma_f32_16x16x32_bf16 v[176:179], a[92:95], a[252:255], v[176:179]\n"
"s_add_u32 s72, s59, 6144\n"
"v_add_u32 v9, 0x10000, v9\n"
"v_mfma_f32_16x16x32_bf16 v[180:183], a[88:91], v[232:235], v[180:183]\n"
"buffer_store_dwordx4 v[240:243], v11, s[52:55], s72 offen offset:0 sc1\n"
"v_mfma_f32_16x16x32_bf16 v[180:183], a[92:95], v[236:239], v[180:183]\n"
"s_waitcnt vmcnt(11) lgkmcnt(0)\n"
"s_barrier\n"
"buffer_load_dwordx4 v1, s[36:39], s64 offen offset:0 nt lds\n"
"v_mfma_f32_16x16x32_bf16 v[40:43], a[96:99], a[216:219], v[40:43]\n"
"buffer_load_dwordx4 v2, s[36:39], s64 offen offset:1024 nt lds\n"
"v_mfma_f32_16x16x32_bf16 v[40:43], a[100:103], a[220:223], v[40:43]\n"
"buffer_load_dwordx4 v1, s[36:39], s64 offen offset:2048 nt lds\n"
"v_mfma_f32_16x16x32_bf16 v[44:47], a[96:99], a[224:227], v[44:47]\n"
"buffer_load_dwordx4 v2, s[36:39], s64 offen offset:3072 nt lds\n"
"v_accvgpr_read_b32 v240, a104\n"
"v_accvgpr_read_b32 v241, a105\n"
"v_accvgpr_read_b32 v242, a106\n"
"v_accvgpr_read_b32 v243, a107\n"
"s_add_u32 m0, s60, 114688\n"
"s_add_u32 s65, s56, 8192\n"
"v_mfma_f32_16x16x32_bf16 v[44:47], a[100:103], a[228:231], v[44:47]\n"
"ds_read_b64_tr_b16 a[104:105], v9 offset:0\n"
"ds_read_b64_tr_b16 a[106:107], v9 offset:512\n"
"buffer_load_dwordx4 v1, s[36:39], s65 offen offset:0 nt lds\n"
"v_accvgpr_read_b32 v250, a108\n"
"v_accvgpr_read_b32 v251, a109\n"
"v_accvgpr_read_b32 v252, a110\n"
"v_accvgpr_read_b32 v253, a111\n"
"v_mfma_f32_16x16x32_bf16 v[48:51], a[96:99], a[232:235], v[48:51]\n"
"ds_read_b64_tr_b16 a[108:109], v9 offset:4096\n"
"ds_read_b64_tr_b16 a[110:111], v9 offset:4608\n"
"buffer_load_dwordx4 v2, s[36:39], s65 offen offset:1024 nt lds\n"
"v_mfma_f32_16x16x32_bf16 v[48:51], a[100:103], a[236:239], v[48:51]\n"
"buffer_load_dwordx4 v1, s[36:39], s65 offen offset:2048 nt lds\n"
"v_mfma_f32_16x16x32_bf16 v[52:55], a[96:99], a[240:243], v[52:55]\n"
"buffer_load_dwordx4 v2, s[36:39], s65 offen offset:3072 nt lds\n"
"v_mfma_f32_16x16x32_bf16 v[52:55], a[100:103], a[244:247], v[52:55]\n"
"s_add_u32 s75, s58, s83\n"
"s_add_u32 m0, s60, 49152\n"
"v_mfma_f32_16x16x32_bf16 v[56:59], a[96:99], a[248:251], v[56:59]\n"
"s_add_u32 s66, s75, 128\n"
"s_add_u32 s67, s75, 64640\n"
"v_mfma_f32_16x16x32_bf16 v[56:59], a[100:103], a[252:255], v[56:59]\n"
"buffer_load_dwordx4 v3, s[48:51], s66 offen offset:0 lds\n"
"v_mfma_f32_16x16x32_bf16 v[60:63], a[96:99], v[232:235], v[60:63]\n"
"v_mul_f32 v240, v245, v240\n"
"v_mul_f32 v241, v245, v241\n"
"v_mfma_f32_16x16x32_bf16 v[60:63], a[100:103], v[236:239], v[60:63]\n"
"v_mul_f32 v242, v245, v242\n"
"s_waitcnt lgkmcnt(2)\n"
"v_mfma_f32_16x16x32_bf16 v[184:187], a[104:107], a[216:219], v[184:187]\n"
"v_mul_f32 v243, v245, v243\n"
"v_cvt_pk_bf16_f32 v240, v240, v241\n"
"v_mul_f32 v250, v245, v250\n"
"s_waitcnt lgkmcnt(0)\n"
"v_mfma_f32_16x16x32_bf16 v[184:187], a[108:111], a[220:223], v[184:187]\n"
"buffer_load_dwordx4 v3, s[48:51], s67 offen offset:1024 lds\n"
"v_mul_f32 v251, v245, v251\n"
"v_mul_f32 v252, v245, v252\n"
"v_mul_f32 v253, v245, v253\n"
"v_cvt_pk_bf16_f32 v241, v242, v243\n"
"v_cvt_pk_bf16_f32 v242, v250, v251\n"
"v_cvt_pk_bf16_f32 v243, v252, v253\n"
"s_add_u32 s73, s59, 6144\n"
"v_mfma_f32_16x16x32_bf16 v[72:75], a[104:107], a[224:227], v[72:75]\n"
"buffer_store_dwordx4 v[240:243], v11, s[52:55], s73 offen offset:1024 sc1\n"
"v_mfma_f32_16x16x32_bf16 v[72:75], a[108:111], a[228:231], v[72:75]\n"
"s_nop 0\n"
"v_accvgpr_read_b32 v240, a112\n"
"v_accvgpr_read_b32 v241, a113\n"
"v_accvgpr_read_b32 v242, a114\n"
"v_accvgpr_read_b32 v243, a115\n"
"ds_read_b64_tr_b16 a[112:113], v9 offset:8\n"
"ds_read_b64_tr_b16 a[114:115], v9 offset:520\n"
"v_accvgpr_read_b32 v250, a116\n"
"v_accvgpr_read_b32 v251, a117\n"
"v_accvgpr_read_b32 v252, a118\n"
"v_accvgpr_read_b32 v253, a119\n"
"v_mfma_f32_16x16x32_bf16 v[88:91], a[104:107], a[232:235], v[88:91]\n"
"ds_read_b64_tr_b16 a[116:117], v9 offset:4104\n"
"ds_read_b64_tr_b16 a[118:119], v9 offset:4616\n"
"s_add_u32 s68, s75, 129152\n"
"v_mfma_f32_16x16x32_bf16 v[88:91], a[108:111], a[236:239], v[88:91]\n"
"buffer_load_dwordx4 v3, s[48:51], s68 offen offset:2048 lds\n"
"v_mfma_f32_16x16x32_bf16 v[104:107], a[104:107], a[240:243], v[104:107]\n"
"s_add_u32 s69, s75, 193664\n"
"v_mul_f32 v240, v246, v240\n"
"v_mfma_f32_16x16x32_bf16 v[104:107], a[108:111], a[244:247], v[104:107]\n"
"buffer_load_dwordx4 v3, s[48:51], s69 offen offset:3072 lds\n"
"v_mfma_f32_16x16x32_bf16 v[120:123], a[104:107], a[248:251], v[120:123]\n"
"v_mul_f32 v241, v246, v241\n"
"v_mul_f32 v242, v246, v242\n"
"v_mfma_f32_16x16x32_bf16 v[120:123], a[108:111], a[252:255], v[120:123]\n"
"v_mul_f32 v243, v246, v243\n"
"v_cvt_pk_bf16_f32 v240, v240, v241\n"
"v_mfma_f32_16x16x32_bf16 v[192:195], a[104:107], v[232:235], v[192:195]\n"
"v_mul_f32 v250, v246, v250\n"
"v_mul_f32 v251, v246, v251\n"
"v_mfma_f32_16x16x32_bf16 v[192:195], a[108:111], v[236:239], v[192:195]\n"
"v_mul_f32 v252, v246, v252\n"
"s_waitcnt lgkmcnt(2)\n"
"v_mfma_f32_16x16x32_bf16 v[64:67], a[112:115], a[216:219], v[64:67]\n"
"v_mul_f32 v253, v246, v253\n"
"v_cvt_pk_bf16_f32 v241, v242, v243\n"
"s_add_u32 s74, s57, s82\n"
"v_cvt_pk_bf16_f32 v242, v250, v251\n"
"s_waitcnt lgkmcnt(0)\n"
"v_mfma_f32_16x16x32_bf16 v[64:67], a[116:119], a[220:223], v[64:67]\n"
"v_cvt_pk_bf16_f32 v243, v252, v253\n"
"s_add_u32 s72, s59, 6144\n"
"s_add_u32 m0, s61, 32768\n"
"s_add_u32 s70, s74, 55296\n"
"v_mfma_f32_16x16x32_bf16 v[76:79], a[112:115], a[224:227], v[76:79]\n"
"buffer_store_dwordx4 v[240:243], v11, s[52:55], s72 offen offset:2048 sc1\n"
"buffer_load_dwordx4 v4, s[44:47], s70 offen offset:0 lds\n"
"s_nop 0\n"
"v_accvgpr_read_b32 v240, a120\n"
"v_accvgpr_read_b32 v241, a121\n"
"v_accvgpr_read_b32 v242, a122\n"
"v_accvgpr_read_b32 v243, a123\n"
"v_mfma_f32_16x16x32_bf16 v[76:79], a[116:119], a[228:231], v[76:79]\n"
"ds_read_b64_tr_b16 a[120:121], v9 offset:256\n"
"ds_read_b64_tr_b16 a[122:123], v9 offset:768\n"
"v_accvgpr_read_b32 v250, a124\n"
"v_accvgpr_read_b32 v251, a125\n"
"v_accvgpr_read_b32 v252, a126\n"
"v_accvgpr_read_b32 v253, a127\n"
"v_mfma_f32_16x16x32_bf16 v[92:95], a[112:115], a[232:235], v[92:95]\n"
"ds_read_b64_tr_b16 a[124:125], v9 offset:4352\n"
"ds_read_b64_tr_b16 a[126:127], v9 offset:4864\n"
"v_mfma_f32_16x16x32_bf16 v[92:95], a[116:119], a[236:239], v[92:95]\n"
"s_add_u32 s71, s74, 63488\n"
"s_add_u32 s64, s74, 71680\n"
"v_mfma_f32_16x16x32_bf16 v[108:111], a[112:115], a[240:243], v[108:111]\n"
"buffer_load_dwordx4 v5, s[44:47], s71 offen offset:1024 lds\n"
"v_mfma_f32_16x16x32_bf16 v[108:111], a[116:119], a[244:247], v[108:111]\n"
"buffer_load_dwordx4 v4, s[44:47], s64 offen offset:2048 lds\n"
"v_mfma_f32_16x16x32_bf16 v[124:127], a[112:115], a[248:251], v[124:127]\n"
"v_mul_f32 v240, v247, v240\n"
"v_mul_f32 v241, v247, v241\n"
"v_mfma_f32_16x16x32_bf16 v[124:127], a[116:119], a[252:255], v[124:127]\n"
"v_mul_f32 v242, v247, v242\n"
"v_mul_f32 v243, v247, v243\n"
"v_mfma_f32_16x16x32_bf16 v[196:199], a[112:115], v[232:235], v[196:199]\n"
"v_cvt_pk_bf16_f32 v240, v240, v241\n"
"v_mul_f32 v250, v247, v250\n"
"v_mfma_f32_16x16x32_bf16 v[196:199], a[116:119], v[236:239], v[196:199]\n"
"v_mul_f32 v251, v247, v251\n"
"s_waitcnt lgkmcnt(2)\n"
"v_mfma_f32_16x16x32_bf16 v[188:191], a[120:123], a[216:219], v[188:191]\n"
"v_mul_f32 v252, v247, v252\n"
"v_mul_f32 v253, v247, v253\n"
"v_cvt_pk_bf16_f32 v241, v242, v243\n"
"v_cvt_pk_bf16_f32 v242, v250, v251\n"
"v_cvt_pk_bf16_f32 v243, v252, v253\n"
"s_waitcnt lgkmcnt(0)\n"
"v_mfma_f32_16x16x32_bf16 v[188:191], a[124:127], a[220:223], v[188:191]\n"
"s_add_u32 s73, s59, 399360\n"
"buffer_store_dwordx4 v[240:243], v11, s[52:55], s73 offen offset:0 sc1\n"
"v_mfma_f32_16x16x32_bf16 v[80:83], a[120:123], a[224:227], v[80:83]\n"
"s_nop 0\n"
"v_accvgpr_read_b32 v240, a128\n"
"v_accvgpr_read_b32 v241, a129\n"
"v_accvgpr_read_b32 v242, a130\n"
"v_accvgpr_read_b32 v243, a131\n"
"v_accvgpr_read_b32 v250, a132\n"
"v_accvgpr_read_b32 v251, a133\n"
"v_accvgpr_read_b32 v252, a134\n"
"v_accvgpr_read_b32 v253, a135\n"
"v_add_u32 v10, 0x10000, v10\n"
"ds_read_b128 a[24:27], v10 offset:0\n"
"ds_read_b128 a[28:31], v10 offset:512\n"
"ds_read_b128 a[32:35], v10 offset:2048\n"
"ds_read_b128 a[36:39], v10 offset:2560\n"
"ds_read_b128 a[40:43], v10 offset:4096\n"
"ds_read_b128 a[44:47], v10 offset:4608\n"
"ds_read_b64_tr_b16 a[128:129], v9 offset:264\n"
"ds_read_b64_tr_b16 a[130:131], v9 offset:776\n"
"ds_read_b64_tr_b16 a[132:133], v9 offset:4360\n"
"ds_read_b64_tr_b16 a[134:135], v9 offset:4872\n"
"v_mfma_f32_16x16x32_bf16 v[80:83], a[124:127], a[228:231], v[80:83]\n"
"s_add_u32 s83, s83, 524288\n"
"s_cmp_eq_u32 s83, 4718592\n"
"s_cselect_b32 s83, 0, s83\n"
"v_mfma_f32_16x16x32_bf16 v[96:99], a[120:123], a[232:235], v[96:99]\n"
"v_add_u32 v9, 0xffff0000, v9\n"
"v_add_u32 v10, 0xffff0000, v10\n"
"v_mfma_f32_16x16x32_bf16 v[96:99], a[124:127], a[236:239], v[96:99]\n"
"s_add_u32 s75, s58, s83\n"
"s_add_u32 m0, s60, 81920\n"
"v_mfma_f32_16x16x32_bf16 v[112:115], a[120:123], a[240:243], v[112:115]\n"
"s_add_u32 s65, s75, 0\n"
"v_add_u32 v6, 0x10000, v6\n"
"v_mfma_f32_16x16x32_bf16 v[112:115], a[124:127], a[244:247], v[112:115]\n"
"s_add_u32 s82, s82, 128\n"
"s_cmp_eq_u32 s82, 1152\n"
"s_cselect_b32 s82, 0, s82\n"
"v_mfma_f32_16x16x32_bf16 v[128:131], a[120:123], a[248:251], v[128:131]\n"
"v_mul_f32 v240, v248, v240\n"
"v_mul_f32 v241, v248, v241\n"
"v_mfma_f32_16x16x32_bf16 v[128:131], a[124:127], a[252:255], v[128:131]\n"
"v_mul_f32 v242, v248, v242\n"
"v_mul_f32 v243, v248, v243\n"
"v_mfma_f32_16x16x32_bf16 v[200:203], a[120:123], v[232:235], v[200:203]\n"
"s_add_u32 s66, s75, 64512\n"
"v_cvt_pk_bf16_f32 v240, v240, v241\n"
"v_mfma_f32_16x16x32_bf16 v[200:203], a[124:127], v[236:239], v[200:203]\n"
"v_mul_f32 v250, v248, v250\n"
"s_waitcnt lgkmcnt(1)\n"
"v_mfma_f32_16x16x32_bf16 v[68:71], a[128:131], a[216:219], v[68:71]\n"
"s_waitcnt vmcnt(20) lgkmcnt(0)\n"
"s_barrier\n"
"buffer_load_dwordx4 v3, s[48:51], s65 offen offset:0 lds\n"
"v_mfma_f32_16x16x32_bf16 v[68:71], a[132:135], a[220:223], v[68:71]\n"
"ds_read_b64_tr_b16 a[72:73], v9 offset:0\n"
"ds_read_b64_tr_b16 a[74:75], v9 offset:512\n"
"v_mfma_f32_16x16x32_bf16 v[84:87], a[128:131], a[224:227], v[84:87]\n"
"ds_read_b64_tr_b16 a[76:77], v9 offset:4096\n"
"ds_read_b64_tr_b16 a[78:79], v9 offset:4608\n"
"v_mfma_f32_16x16x32_bf16 v[84:87], a[132:135], a[228:231], v[84:87]\n"
"buffer_load_dwordx4 v3, s[48:51], s66 offen offset:1024 lds\n"
"v_mfma_f32_16x16x32_bf16 v[100:103], a[128:131], a[232:235], v[100:103]\n"
"s_add_u32 s67, s75, 129024\n"
"ds_read_b64_tr_b16 a[80:81], v9 offset:8\n"
"v_mfma_f32_16x16x32_bf16 v[100:103], a[132:135], a[236:239], v[100:103]\n"
"buffer_load_dwordx4 v3, s[48:51], s67 offen offset:2048 lds\n"
"v_mfma_f32_16x16x32_bf16 v[116:119], a[128:131], a[240:243], v[116:119]\n"
"ds_read_b64_tr_b16 a[82:83], v9 offset:520\n"
"ds_read_b64_tr_b16 a[84:85], v9 offset:4104\n"
"v_mfma_f32_16x16x32_bf16 v[116:119], a[132:135], a[244:247], v[116:119]\n"
"ds_read_b64_tr_b16 a[86:87], v9 offset:4616\n"
"ds_read_b64_tr_b16 a[88:89], v9 offset:256\n"
"v_mfma_f32_16x16x32_bf16 v[132:135], a[128:131], a[248:251], v[132:135]\n"
"ds_read_b64_tr_b16 a[90:91], v9 offset:768\n"
"ds_read_b64_tr_b16 a[92:93], v9 offset:4352\n"
"v_mfma_f32_16x16x32_bf16 v[132:135], a[132:135], a[252:255], v[132:135]\n"
"s_add_u32 s68, s75, 193536\n"
"ds_read_b64_tr_b16 a[94:95], v9 offset:4864\n"
"v_mfma_f32_16x16x32_bf16 v[204:207], a[128:131], v[232:235], v[204:207]\n"
"buffer_load_dwordx4 v3, s[48:51], s68 offen offset:3072 lds\n"
"v_mfma_f32_16x16x32_bf16 v[204:207], a[132:135], v[236:239], v[204:207]\n"
"ds_read_b128 a[216:219], v10 offset:0\n"
"s_waitcnt lgkmcnt(10)\n"
"v_mfma_f32_16x16x32_bf16 v[136:139], a[72:75], a[0:3], v[136:139]\n"
"ds_read_b128 a[220:223], v10 offset:512\n"
"s_waitcnt lgkmcnt(10)\n"
"v_mfma_f32_16x16x32_bf16 v[136:139], a[76:79], a[4:7], v[136:139]\n"
"ds_read_b128 a[224:227], v10 offset:2048\n"
"ds_read_b128 a[228:231], v10 offset:2560\n"
"v_mfma_f32_16x16x32_bf16 v[140:143], a[72:75], a[8:11], v[140:143]\n"
"ds_read_b128 a[232:235], v10 offset:4096\n"
"ds_read_b128 a[236:239], v10 offset:4608\n"
"v_mfma_f32_16x16x32_bf16 v[140:143], a[76:79], a[12:15], v[140:143]\n"
"s_add_u32 s74, s57, s82\n"
"s_add_u32 m0, s61, 65536\n"
"v_mfma_f32_16x16x32_bf16 v[144:147], a[72:75], a[16:19], v[144:147]\n"
"s_add_u32 s69, s74, 0\n"
"ds_read_b64_tr_b16 a[96:97], v9 offset:264\n"
"v_mfma_f32_16x16x32_bf16 v[144:147], a[76:79], a[20:23], v[144:147]\n"
"buffer_load_dwordx4 v4, s[44:47], s69 offen offset:0 lds\n"
"v_mfma_f32_16x16x32_bf16 v[148:151], a[72:75], a[24:27], v[148:151]\n"
"ds_read_b64_tr_b16 a[98:99], v9 offset:776\n"
"ds_read_b64_tr_b16 a[100:101], v9 offset:4360\n"
"v_mfma_f32_16x16x32_bf16 v[148:151], a[76:79], a[28:31], v[148:151]\n"
"ds_read_b64_tr_b16 a[102:103], v9 offset:4872\n"
"v_mul_f32 v251, v248, v251\n"
"v_mfma_f32_16x16x32_bf16 v[152:155], a[72:75], a[32:35], v[152:155]\n"
"s_add_u32 s70, s74, 8192\n"
"v_mul_f32 v252, v248, v252\n"
"v_mfma_f32_16x16x32_bf16 v[152:155], a[76:79], a[36:39], v[152:155]\n"
"buffer_load_dwordx4 v5, s[44:47], s70 offen offset:1024 lds\n"
"v_mfma_f32_16x16x32_bf16 v[156:159], a[72:75], a[40:43], v[156:159]\n"
"v_mul_f32 v253, v248, v253\n"
"v_cvt_pk_bf16_f32 v241, v242, v243\n"
"v_mfma_f32_16x16x32_bf16 v[156:159], a[76:79], a[44:47], v[156:159]\n"
"v_cvt_pk_bf16_f32 v242, v250, v251\n"
"s_waitcnt lgkmcnt(5)\n"
"v_mfma_f32_16x16x32_bf16 v[16:19], a[80:83], a[0:3], v[16:19]\n"
"v_cvt_pk_bf16_f32 v243, v252, v253\n"
"s_add_u32 s72, s59, 399360\n"
"v_mfma_f32_16x16x32_bf16 v[16:19], a[84:87], a[4:7], v[16:19]\n"
"buffer_store_dwordx4 v[240:243], v11, s[52:55], s72 offen offset:1024 sc1\n"
"v_mfma_f32_16x16x32_bf16 v[20:23], a[80:83], a[8:11], v[20:23]\n"
"s_add_u32 s71, s74, 16384\n"
"v_accvgpr_read_b32 v240, a136\n"
"v_mfma_f32_16x16x32_bf16 v[20:23], a[84:87], a[12:15], v[20:23]\n"
"buffer_load_dwordx4 v4, s[44:47], s71 offen offset:2048 lds\n"
"v_mfma_f32_16x16x32_bf16 v[24:27], a[80:83], a[16:19], v[24:27]\n"
"v_accvgpr_read_b32 v241, a137\n"
"v_accvgpr_read_b32 v242, a138\n"
"v_mfma_f32_16x16x32_bf16 v[24:27], a[84:87], a[20:23], v[24:27]\n"
"v_accvgpr_read_b32 v243, a139\n"
"v_mul_f32 v240, v249, v240\n"
"v_mfma_f32_16x16x32_bf16 v[28:31], a[80:83], a[24:27], v[28:31]\n"
"v_mul_f32 v241, v249, v241\n"
"v_accvgpr_read_b32 v250, a140\n"
"v_mfma_f32_16x16x32_bf16 v[28:31], a[84:87], a[28:31], v[28:31]\n"
"v_accvgpr_read_b32 v251, a141\n"
"v_accvgpr_read_b32 v252, a142\n"
"v_mfma_f32_16x16x32_bf16 v[32:35], a[80:83], a[32:35], v[32:35]\n"
"v_accvgpr_read_b32 v253, a143\n"
"v_mul_f32 v242, v249, v242\n"
"v_mfma_f32_16x16x32_bf16 v[32:35], a[84:87], a[36:39], v[32:35]\n"
"s_waitcnt vmcnt(9) lgkmcnt(0)\n"
"s_barrier\n"
"v_mfma_f32_16x16x32_bf16 v[36:39], a[80:83], a[40:43], v[36:39]\n"
"ds_read_b64_tr_b16 a[104:105], v9 offset:32768\n"
"ds_read_b64_tr_b16 a[106:107], v9 offset:33280\n"
"v_mfma_f32_16x16x32_bf16 v[36:39], a[84:87], a[44:47], v[36:39]\n"
"ds_read_b64_tr_b16 a[108:109], v9 offset:36864\n"
"ds_read_b64_tr_b16 a[110:111], v9 offset:37376\n"
"v_mfma_f32_16x16x32_bf16 v[160:163], a[88:91], a[0:3], v[160:163]\n"
"ds_read_b64_tr_b16 a[112:113], v9 offset:32776\n"
"ds_read_b64_tr_b16 a[114:115], v9 offset:33288\n"
"v_mfma_f32_16x16x32_bf16 v[160:163], a[92:95], a[4:7], v[160:163]\n"
"ds_read_b64_tr_b16 a[116:117], v9 offset:36872\n"
"ds_read_b64_tr_b16 a[118:119], v9 offset:37384\n"
"v_mfma_f32_16x16x32_bf16 v[164:167], a[88:91], a[8:11], v[164:167]\n"
"ds_read_b64_tr_b16 a[120:121], v9 offset:33024\n"
"ds_read_b64_tr_b16 a[122:123], v9 offset:33536\n"
"v_mfma_f32_16x16x32_bf16 v[164:167], a[92:95], a[12:15], v[164:167]\n"
"ds_read_b64_tr_b16 a[124:125], v9 offset:37120\n"
"ds_read_b64_tr_b16 a[126:127], v9 offset:37632\n"
"v_mfma_f32_16x16x32_bf16 v[168:171], a[88:91], a[16:19], v[168:171]\n"
"ds_read_b128 a[240:243], v10 offset:32768\n"
"ds_read_b128 a[244:247], v10 offset:33280\n"
"v_mfma_f32_16x16x32_bf16 v[168:171], a[92:95], a[20:23], v[168:171]\n"
"s_add_u32 s75, s58, s83\n"
"s_add_u32 m0, s60, 16384\n"
"v_mfma_f32_16x16x32_bf16 v[172:175], a[88:91], a[24:27], v[172:175]\n"
"s_add_u32 s64, s75, 128\n"
"ds_read_b128 a[248:251], v10 offset:34816\n"
"v_mfma_f32_16x16x32_bf16 v[172:175], a[92:95], a[28:31], v[172:175]\n"
"buffer_load_dwordx4 v3, s[48:51], s64 offen offset:0 lds\n"
"v_mfma_f32_16x16x32_bf16 v[176:179], a[88:91], a[32:35], v[176:179]\n"
"ds_read_b128 a[252:255], v10 offset:35328\n"
"ds_read_b128 v[232:235], v10 offset:36864\n"
"v_mfma_f32_16x16x32_bf16 v[176:179], a[92:95], a[36:39], v[176:179]\n"
"ds_read_b128 v[236:239], v10 offset:37376\n"
"ds_read_b64_tr_b16 a[128:129], v9 offset:33032\n"
"v_mfma_f32_16x16x32_bf16 v[180:183], a[88:91], a[40:43], v[180:183]\n"
"s_add_u32 s65, s75, 64640\n"
"ds_read_b64_tr_b16 a[130:131], v9 offset:33544\n"
"v_mfma_f32_16x16x32_bf16 v[180:183], a[92:95], a[44:47], v[180:183]\n"
"buffer_load_dwordx4 v3, s[48:51], s65 offen offset:1024 lds\n"
"v_mfma_f32_16x16x32_bf16 v[40:43], a[96:99], a[0:3], v[40:43]\n"
"ds_read_b64_tr_b16 a[132:133], v9 offset:37128\n"
"ds_read_b64_tr_b16 a[134:135], v9 offset:37640\n"
"v_mfma_f32_16x16x32_bf16 v[40:43], a[100:103], a[4:7], v[40:43]\n"
"s_add_u32 s83, s83, 524288\n"
"s_cmp_eq_u32 s83, 4718592\n"
"s_cselect_b32 s83, 0, s83\n"
"v_mfma_f32_16x16x32_bf16 v[44:47], a[96:99], a[8:11], v[44:47]\n"
"v_add_u32 v9, 0x10000, v9\n"
"v_add_u32 v10, 0x10000, v10\n"
"v_mfma_f32_16x16x32_bf16 v[44:47], a[100:103], a[12:15], v[44:47]\n"
"v_mul_f32 v243, v249, v243\n"
"v_cvt_pk_bf16_f32 v240, v240, v241\n"
"v_mfma_f32_16x16x32_bf16 v[48:51], a[96:99], a[16:19], v[48:51]\n"
"s_add_u32 s66, s75, 129152\n"
"v_mul_f32 v250, v249, v250\n"
"v_mfma_f32_16x16x32_bf16 v[48:51], a[100:103], a[20:23], v[48:51]\n"
"buffer_load_dwordx4 v3, s[48:51], s66 offen offset:2048 lds\n"
"v_mfma_f32_16x16x32_bf16 v[52:55], a[96:99], a[24:27], v[52:55]\n"
"v_mul_f32 v251, v249, v251\n"
"v_mul_f32 v252, v249, v252\n"
"v_mfma_f32_16x16x32_bf16 v[52:55], a[100:103], a[28:31], v[52:55]\n"
"v_mul_f32 v253, v249, v253\n"
"v_cvt_pk_bf16_f32 v241, v242, v243\n"
"v_mfma_f32_16x16x32_bf16 v[56:59], a[96:99], a[32:35], v[56:59]\n"
"v_cvt_pk_bf16_f32 v242, v250, v251\n"
"v_cvt_pk_bf16_f32 v243, v252, v253\n"
"v_mfma_f32_16x16x32_bf16 v[56:59], a[100:103], a[36:39], v[56:59]\n"
"s_add_u32 s67, s75, 193664\n"
"s_add_u32 s73, s59, 399360\n"
"v_mfma_f32_16x16x32_bf16 v[60:63], a[96:99], a[40:43], v[60:63]\n"
"buffer_load_dwordx4 v3, s[48:51], s67 offen offset:3072 lds\n"
"v_mfma_f32_16x16x32_bf16 v[60:63], a[100:103], a[44:47], v[60:63]\n"
"v_accvgpr_read_b32 v250, a148\n"
"s_waitcnt lgkmcnt(0)\n"
"v_mfma_f32_16x16x32_bf16 v[184:187], a[104:107], a[0:3], v[184:187]\n"
"buffer_store_dwordx4 v[240:243], v11, s[52:55], s73 offen offset:2048 sc1\n"
"v_mfma_f32_16x16x32_bf16 v[184:187], a[108:111], a[4:7], v[184:187]\n"
"s_nop 0\n"
"v_accvgpr_read_b32 v240, a144\n"
"v_accvgpr_read_b32 v241, a145\n"
"v_mfma_f32_16x16x32_bf16 v[72:75], a[104:107], a[8:11], v[72:75]\n"
"v_accvgpr_read_b32 v242, a146\n"
"v_accvgpr_read_b32 v243, a147\n"
"v_mfma_f32_16x16x32_bf16 v[72:75], a[108:111], a[12:15], v[72:75]\n"
"s_add_u32 s74, s57, s82\n"
"s_add_u32 m0, s61, 0\n"
"v_mfma_f32_16x16x32_bf16 v[88:91], a[104:107], a[16:19], v[88:91]\n"
"s_add_u32 s68, s74, 55296\n"
"v_mul_f32 v240, v244, v240\n"
"v_mfma_f32_16x16x32_bf16 v[88:91], a[108:111], a[20:23], v[88:91]\n"
"buffer_load_dwordx4 v4, s[44:47], s68 offen offset:0 lds\n"
"v_mfma_f32_16x16x32_bf16 v[104:107], a[104:107], a[24:27], v[104:107]\n"
"s_add_u32 s82, s82, 128\n"
"s_cmp_eq_u32 s82, 1152\n"
"s_cselect_b32 s82, 0, s82\n"
"v_mfma_f32_16x16x32_bf16 v[104:107], a[108:111], a[28:31], v[104:107]\n"
"v_mul_f32 v241, v244, v241\n"
"v_accvgpr_read_b32 v251, a149\n"
"v_mfma_f32_16x16x32_bf16 v[120:123], a[104:107], a[32:35], v[120:123]\n"
"s_add_u32 s69, s74, 63488\n"
"v_accvgpr_read_b32 v252, a150\n"
"v_mfma_f32_16x16x32_bf16 v[120:123], a[108:111], a[36:39], v[120:123]\n"
"buffer_load_dwordx4 v5, s[44:47], s69 offen offset:1024 lds\n"
"v_mfma_f32_16x16x32_bf16 v[192:195], a[104:107], a[40:43], v[192:195]\n"
"v_accvgpr_read_b32 v253, a151\n"
"v_mul_f32 v242, v244, v242\n"
"v_mfma_f32_16x16x32_bf16 v[192:195], a[108:111], a[44:47], v[192:195]\n"
"v_mul_f32 v243, v244, v243\n"
"v_cvt_pk_bf16_f32 v240, v240, v241\n"
"v_mfma_f32_16x16x32_bf16 v[64:67], a[112:115], a[0:3], v[64:67]\n"
"v_mul_f32 v250, v244, v250\n"
"v_mul_f32 v251, v244, v251\n"
"v_mfma_f32_16x16x32_bf16 v[64:67], a[116:119], a[4:7], v[64:67]\n"
"v_mul_f32 v252, v244, v252\n"
"v_mul_f32 v253, v244, v253\n"
"v_mfma_f32_16x16x32_bf16 v[76:79], a[112:115], a[8:11], v[76:79]\n"
"s_add_u32 s70, s74, 71680\n"
"v_cvt_pk_bf16_f32 v241, v242, v243\n"
"v_mfma_f32_16x16x32_bf16 v[76:79], a[116:119], a[12:15], v[76:79]\n"
"buffer_load_dwordx4 v4, s[44:47], s70 offen offset:2048 lds\n"
"v_mfma_f32_16x16x32_bf16 v[92:95], a[112:115], a[16:19], v[92:95]\n"
"v_cvt_pk_bf16_f32 v242, v250, v251\n"
"v_cvt_pk_bf16_f32 v243, v252, v253\n"
"v_mfma_f32_16x16x32_bf16 v[92:95], a[116:119], a[20:23], v[92:95]\n"
"s_add_u32 s72, s59, 9216\n"
"v_accvgpr_read_b32 v250, a156\n"
"v_mfma_f32_16x16x32_bf16 v[108:111], a[112:115], a[24:27], v[108:111]\n"
"buffer_store_dwordx4 v[240:243], v11, s[52:55], s72 offen offset:0 sc1\n"
"v_mfma_f32_16x16x32_bf16 v[108:111], a[116:119], a[28:31], v[108:111]\n"
"s_nop 0\n"
"v_accvgpr_read_b32 v240, a152\n"
"v_accvgpr_read_b32 v241, a153\n"
"v_mfma_f32_16x16x32_bf16 v[124:127], a[112:115], a[32:35], v[124:127]\n"
"v_accvgpr_read_b32 v242, a154\n"
"v_accvgpr_read_b32 v243, a155\n"
"v_mfma_f32_16x16x32_bf16 v[124:127], a[116:119], a[36:39], v[124:127]\n"
"s_waitcnt vmcnt(9)\n"
"s_barrier\n"
"v_mfma_f32_16x16x32_bf16 v[196:199], a[112:115], a[40:43], v[196:199]\n"
"ds_read_b64_tr_b16 a[72:73], v9 offset:0\n"
"ds_read_b64_tr_b16 a[74:75], v9 offset:512\n"
"v_mfma_f32_16x16x32_bf16 v[196:199], a[116:119], a[44:47], v[196:199]\n"
"ds_read_b64_tr_b16 a[76:77], v9 offset:4096\n"
"ds_read_b64_tr_b16 a[78:79], v9 offset:4608\n"
"v_mfma_f32_16x16x32_bf16 v[188:191], a[120:123], a[0:3], v[188:191]\n"
"ds_read_b64_tr_b16 a[80:81], v9 offset:8\n"
"ds_read_b64_tr_b16 a[82:83], v9 offset:520\n"
"v_mfma_f32_16x16x32_bf16 v[188:191], a[124:127], a[4:7], v[188:191]\n"
"ds_read_b64_tr_b16 a[84:85], v9 offset:4104\n"
"ds_read_b64_tr_b16 a[86:87], v9 offset:4616\n"
"v_mfma_f32_16x16x32_bf16 v[80:83], a[120:123], a[8:11], v[80:83]\n"
"ds_read_b64_tr_b16 a[88:89], v9 offset:256\n"
"ds_read_b64_tr_b16 a[90:91], v9 offset:768\n"
"v_mfma_f32_16x16x32_bf16 v[80:83], a[124:127], a[12:15], v[80:83]\n"
"ds_read_b64_tr_b16 a[92:93], v9 offset:4352\n"
"ds_read_b64_tr_b16 a[94:95], v9 offset:4864\n"
"v_mfma_f32_16x16x32_bf16 v[96:99], a[120:123], a[16:19], v[96:99]\n"
"ds_read_b64_tr_b16 a[96:97], v9 offset:264\n"
"ds_read_b64_tr_b16 a[98:99], v9 offset:776\n"
"v_mfma_f32_16x16x32_bf16 v[96:99], a[124:127], a[20:23], v[96:99]\n"
"s_add_u32 s75, s58, s83\n"
"s_add_u32 m0, s60, 49152\n"
"v_mfma_f32_16x16x32_bf16 v[112:115], a[120:123], a[24:27], v[112:115]\n"
"s_add_u32 s71, s75, 0\n"
"ds_read_b64_tr_b16 a[100:101], v9 offset:4360\n"
"v_mfma_f32_16x16x32_bf16 v[112:115], a[124:127], a[28:31], v[112:115]\n"
"buffer_load_dwordx4 v3, s[48:51], s71 offen offset:0 lds\n"
"v_mfma_f32_16x16x32_bf16 v[128:131], a[120:123], a[32:35], v[128:131]\n"
"ds_read_b64_tr_b16 a[102:103], v9 offset:4872\n"
"v_add_u32 v9, 0xffff0000, v9\n"
"v_mfma_f32_16x16x32_bf16 v[128:131], a[124:127], a[36:39], v[128:131]\n"
"v_mul_f32 v240, v245, v240\n"
"v_mul_f32 v241, v245, v241\n"
"v_mfma_f32_16x16x32_bf16 v[200:203], a[120:123], a[40:43], v[200:203]\n"
"s_add_u32 s64, s75, 64512\n"
"v_accvgpr_read_b32 v251, a157\n"
"v_mfma_f32_16x16x32_bf16 v[200:203], a[124:127], a[44:47], v[200:203]\n"
"buffer_load_dwordx4 v3, s[48:51], s64 offen offset:1024 lds\n"
"v_mfma_f32_16x16x32_bf16 v[68:71], a[128:131], a[0:3], v[68:71]\n"
"ds_read_b128 a[0:3], v10 offset:0\n"
"v_accvgpr_read_b32 v252, a158\n"
"v_mfma_f32_16x16x32_bf16 v[68:71], a[132:135], a[4:7], v[68:71]\n"
"ds_read_b128 a[4:7], v10 offset:512\n"
"v_accvgpr_read_b32 v253, a159\n"
"v_mfma_f32_16x16x32_bf16 v[84:87], a[128:131], a[8:11], v[84:87]\n"
"ds_read_b128 a[8:11], v10 offset:2048\n"
"v_mul_f32 v242, v245, v242\n"
"v_mfma_f32_16x16x32_bf16 v[84:87], a[132:135], a[12:15], v[84:87]\n"
"ds_read_b128 a[12:15], v10 offset:2560\n"
"v_mul_f32 v243, v245, v243\n"
"v_mfma_f32_16x16x32_bf16 v[100:103], a[128:131], a[16:19], v[100:103]\n"
"s_add_u32 s65, s75, 129024\n"
"ds_read_b128 a[16:19], v10 offset:4096\n"
"v_mfma_f32_16x16x32_bf16 v[100:103], a[132:135], a[20:23], v[100:103]\n"
"buffer_load_dwordx4 v3, s[48:51], s65 offen offset:2048 lds\n"
"v_mfma_f32_16x16x32_bf16 v[116:119], a[128:131], a[24:27], v[116:119]\n"
"ds_read_b128 a[20:23], v10 offset:4608\n"
"v_add_u32 v10, 0xffff0000, v10\n"
"v_mfma_f32_16x16x32_bf16 v[116:119], a[132:135], a[28:31], v[116:119]\n"
"v_cvt_pk_bf16_f32 v240, v240, v241\n"
"v_mul_f32 v250, v245, v250\n"
"v_mfma_f32_16x16x32_bf16 v[132:135], a[128:131], a[32:35], v[132:135]\n"
"v_mul_f32 v251, v245, v251\n"
"v_mul_f32 v252, v245, v252\n"
"v_mfma_f32_16x16x32_bf16 v[132:135], a[132:135], a[36:39], v[132:135]\n"
"s_add_u32 s66, s75, 193536\n"
"v_mul_f32 v253, v245, v253\n"
"v_mfma_f32_16x16x32_bf16 v[204:207], a[128:131], a[40:43], v[204:207]\n"
"buffer_load_dwordx4 v3, s[48:51], s66 offen offset:3072 lds\n"
"v_mfma_f32_16x16x32_bf16 v[204:207], a[132:135], a[44:47], v[204:207]\n"
"v_cvt_pk_bf16_f32 v241, v242, v243\n"
"s_waitcnt lgkmcnt(3)\n"
"v_mfma_f32_16x16x32_bf16 v[136:139], a[72:75], a[216:219], v[136:139]\n"
"v_cvt_pk_bf16_f32 v242, v250, v251\n"
"v_cvt_pk_bf16_f32 v243, v252, v253\n"
"v_mfma_f32_16x16x32_bf16 v[136:139], a[76:79], a[220:223], v[136:139]\n"
"s_add_u32 s73, s59, 9216\n"
"v_accvgpr_read_b32 v250, a164\n"
"v_mfma_f32_16x16x32_bf16 v[140:143], a[72:75], a[224:227], v[140:143]\n"
"buffer_store_dwordx4 v[240:243], v11, s[52:55], s73 offen offset:1024 sc1\n"
"v_mfma_f32_16x16x32_bf16 v[140:143], a[76:79], a[228:231], v[140:143]\n"
"s_add_u32 s74, s57, s82\n"
"s_add_u32 m0, s61, 32768\n"
"v_mfma_f32_16x16x32_bf16 v[144:147], a[72:75], a[232:235], v[144:147]\n"
"s_add_u32 s67, s74, 0\n"
"v_accvgpr_read_b32 v240, a160\n"
"v_mfma_f32_16x16x32_bf16 v[144:147], a[76:79], a[236:239], v[144:147]\n"
"buffer_load_dwordx4 v4, s[44:47], s67 offen offset:0 lds\n"
"v_mfma_f32_16x16x32_bf16 v[148:151], a[72:75], a[240:243], v[148:151]\n"
"v_accvgpr_read_b32 v241, a161\n"
"v_accvgpr_read_b32 v242, a162\n"
"v_mfma_f32_16x16x32_bf16 v[148:151], a[76:79], a[244:247], v[148:151]\n"
"v_accvgpr_read_b32 v243, a163\n"
"v_mul_f32 v240, v246, v240\n"
"v_mfma_f32_16x16x32_bf16 v[152:155], a[72:75], a[248:251], v[152:155]\n"
"s_add_u32 s68, s74, 8192\n"
"v_mul_f32 v241, v246, v241\n"
"v_mfma_f32_16x16x32_bf16 v[152:155], a[76:79], a[252:255], v[152:155]\n"
"buffer_load_dwordx4 v5, s[44:47], s68 offen offset:1024 lds\n"
"v_mfma_f32_16x16x32_bf16 v[156:159], a[72:75], v[232:235], v[156:159]\n"
"v_accvgpr_read_b32 v251, a165\n"
"v_accvgpr_read_b32 v252, a166\n"
"v_mfma_f32_16x16x32_bf16 v[156:159], a[76:79], v[236:239], v[156:159]\n"
"v_accvgpr_read_b32 v253, a167\n"
"v_mul_f32 v242, v246, v242\n"
"v_mfma_f32_16x16x32_bf16 v[16:19], a[80:83], a[216:219], v[16:19]\n"
"v_mul_f32 v243, v246, v243\n"
"v_cvt_pk_bf16_f32 v240, v240, v241\n"
"v_mfma_f32_16x16x32_bf16 v[16:19], a[84:87], a[220:223], v[16:19]\n"
"v_mul_f32 v250, v246, v250\n"
"v_mul_f32 v251, v246, v251\n"
"v_mfma_f32_16x16x32_bf16 v[20:23], a[80:83], a[224:227], v[20:23]\n"
"s_add_u32 s69, s74, 16384\n"
"v_mul_f32 v252, v246, v252\n"
"v_mfma_f32_16x16x32_bf16 v[20:23], a[84:87], a[228:231], v[20:23]\n"
"buffer_load_dwordx4 v4, s[44:47], s69 offen offset:2048 lds\n"
"v_mfma_f32_16x16x32_bf16 v[24:27], a[80:83], a[232:235], v[24:27]\n"
"v_mul_f32 v253, v246, v253\n"
"v_cvt_pk_bf16_f32 v241, v242, v243\n"
"v_mfma_f32_16x16x32_bf16 v[24:27], a[84:87], a[236:239], v[24:27]\n"
"v_cvt_pk_bf16_f32 v242, v250, v251\n"
"v_cvt_pk_bf16_f32 v243, v252, v253\n"
"v_mfma_f32_16x16x32_bf16 v[28:31], a[80:83], a[240:243], v[28:31]\n"
"s_add_u32 s72, s59, 9216\n"
"v_accvgpr_read_b32 v250, a172\n"
"v_mfma_f32_16x16x32_bf16 v[28:31], a[84:87], a[244:247], v[28:31]\n"
"buffer_store_dwordx4 v[240:243], v11, s[52:55], s72 offen offset:2048 sc1\n"
"v_mfma_f32_16x16x32_bf16 v[32:35], a[80:83], a[248:251], v[32:35]\n"
"s_nop 0\n"
"v_accvgpr_read_b32 v240, a168\n"
"v_accvgpr_read_b32 v241, a169\n"
"v_mfma_f32_16x16x32_bf16 v[32:35], a[84:87], a[252:255], v[32:35]\n"
"s_waitcnt vmcnt(10) lgkmcnt(0)\n"
"s_barrier\n"
"v_mfma_f32_16x16x32_bf16 v[36:39], a[80:83], v[232:235], v[36:39]\n"
"ds_read_b64_tr_b16 a[104:105], v9 offset:0\n"
"ds_read_b64_tr_b16 a[106:107], v9 offset:512\n"
"v_mfma_f32_16x16x32_bf16 v[36:39], a[84:87], v[236:239], v[36:39]\n"
"ds_read_b64_tr_b16 a[108:109], v9 offset:4096\n"
"ds_read_b64_tr_b16 a[110:111], v9 offset:4608\n"
"v_mfma_f32_16x16x32_bf16 v[160:163], a[88:91], a[216:219], v[160:163]\n"
"ds_read_b64_tr_b16 a[112:113], v9 offset:8\n"
"ds_read_b64_tr_b16 a[114:115], v9 offset:520\n"
"v_mfma_f32_16x16x32_bf16 v[160:163], a[92:95], a[220:223], v[160:163]\n"
"ds_read_b64_tr_b16 a[116:117], v9 offset:4104\n"
"ds_read_b64_tr_b16 a[118:119], v9 offset:4616\n"
"v_mfma_f32_16x16x32_bf16 v[164:167], a[88:91], a[224:227], v[164:167]\n"
"ds_read_b64_tr_b16 a[120:121], v9 offset:256\n"
"ds_read_b64_tr_b16 a[122:123], v9 offset:768\n"
"v_mfma_f32_16x16x32_bf16 v[164:167], a[92:95], a[228:231], v[164:167]\n"
"ds_read_b64_tr_b16 a[124:125], v9 offset:4352\n"
"ds_read_b64_tr_b16 a[126:127], v9 offset:4864\n"
"v_mfma_f32_16x16x32_bf16 v[168:171], a[88:91], a[232:235], v[168:171]\n"
"ds_read_b128 a[24:27], v10 offset:0\n"
"ds_read_b128 a[28:31], v10 offset:512\n"
"v_mfma_f32_16x16x32_bf16 v[168:171], a[92:95], a[236:239], v[168:171]\n"
"s_add_u32 s75, s58, s83\n"
"s_add_u32 m0, s60, 81920\n"
"v_mfma_f32_16x16x32_bf16 v[172:175], a[88:91], a[240:243], v[172:175]\n"
"s_add_u32 s70, s75, 128\n"
"ds_read_b128 a[32:35], v10 offset:2048\n"
"v_mfma_f32_16x16x32_bf16 v[172:175], a[92:95], a[244:247], v[172:175]\n"
"buffer_load_dwordx4 v3, s[48:51], s70 offen offset:0 lds\n"
"v_mfma_f32_16x16x32_bf16 v[176:179], a[88:91], a[248:251], v[176:179]\n"
"ds_read_b128 a[36:39], v10 offset:2560\n"
"ds_read_b128 a[40:43], v10 offset:4096\n"
"v_mfma_f32_16x16x32_bf16 v[176:179], a[92:95], a[252:255], v[176:179]\n"
"ds_read_b128 a[44:47], v10 offset:4608\n"
"ds_read_b64_tr_b16 a[128:129], v9 offset:264\n"
"v_mfma_f32_16x16x32_bf16 v[180:183], a[88:91], v[232:235], v[180:183]\n"
"s_add_u32 s71, s75, 64640\n"
"ds_read_b64_tr_b16 a[130:131], v9 offset:776\n"
"v_mfma_f32_16x16x32_bf16 v[180:183], a[92:95], v[236:239], v[180:183]\n"
"buffer_load_dwordx4 v3, s[48:51], s71 offen offset:1024 lds\n"
"v_mfma_f32_16x16x32_bf16 v[40:43], a[96:99], a[216:219], v[40:43]\n"
"ds_read_b64_tr_b16 a[132:133], v9 offset:4360\n"
"ds_read_b64_tr_b16 a[134:135], v9 offset:4872\n"
"v_mfma_f32_16x16x32_bf16 v[40:43], a[100:103], a[220:223], v[40:43]\n"
"s_add_u32 s83, s83, 524288\n"
"s_cmp_eq_u32 s83, 4718592\n"
"s_cselect_b32 s83, 0, s83\n"
"v_mfma_f32_16x16x32_bf16 v[44:47], a[96:99], a[224:227], v[44:47]\n"
"v_accvgpr_read_b32 v242, a170\n"
"v_accvgpr_read_b32 v243, a171\n"
"v_mfma_f32_16x16x32_bf16 v[44:47], a[100:103], a[228:231], v[44:47]\n"
"v_mul_f32 v240, v247, v240\n"
"v_mul_f32 v241, v247, v241\n"
"v_mfma_f32_16x16x32_bf16 v[48:51], a[96:99], a[232:235], v[48:51]\n"
"s_add_u32 s64, s75, 129152\n"
"v_accvgpr_read_b32 v251, a173\n"
"v_mfma_f32_16x16x32_bf16 v[48:51], a[100:103], a[236:239], v[48:51]\n"
"buffer_load_dwordx4 v3, s[48:51], s64 offen offset:2048 lds\n"
"v_mfma_f32_16x16x32_bf16 v[52:55], a[96:99], a[240:243], v[52:55]\n"
"v_accvgpr_read_b32 v252, a174\n"
"v_accvgpr_read_b32 v253, a175\n"
"v_mfma_f32_16x16x32_bf16 v[52:55], a[100:103], a[244:247], v[52:55]\n"
"v_mul_f32 v242, v247, v242\n"
"v_mul_f32 v243, v247, v243\n"
"v_mfma_f32_16x16x32_bf16 v[56:59], a[96:99], a[248:251], v[56:59]\n"
"v_cvt_pk_bf16_f32 v240, v240, v241\n"
"v_mul_f32 v250, v247, v250\n"
"v_mfma_f32_16x16x32_bf16 v[56:59], a[100:103], a[252:255], v[56:59]\n"
"s_add_u32 s65, s75, 193664\n"
"v_mul_f32 v251, v247, v251\n"
"v_mfma_f32_16x16x32_bf16 v[60:63], a[96:99], v[232:235], v[60:63]\n"
"buffer_load_dwordx4 v3, s[48:51], s65 offen offset:3072 lds\n"
"v_mfma_f32_16x16x32_bf16 v[60:63], a[100:103], v[236:239], v[60:63]\n"
"v_mul_f32 v252, v247, v252\n"
"s_waitcnt lgkmcnt(0)\n"
"v_mfma_f32_16x16x32_bf16 v[184:187], a[104:107], a[216:219], v[184:187]\n"
"v_mul_f32 v253, v247, v253\n"
"v_cvt_pk_bf16_f32 v241, v242, v243\n"
"v_mfma_f32_16x16x32_bf16 v[184:187], a[108:111], a[220:223], v[184:187]\n"
"v_cvt_pk_bf16_f32 v242, v250, v251\n"
"v_cvt_pk_bf16_f32 v243, v252, v253\n"
"v_mfma_f32_16x16x32_bf16 v[72:75], a[104:107], a[224:227], v[72:75]\n"
"s_add_u32 s73, s59, 402432\n"
"v_accvgpr_read_b32 v250, a180\n"
"v_mfma_f32_16x16x32_bf16 v[72:75], a[108:111], a[228:231], v[72:75]\n"
"s_add_u32 s74, s57, s82\n"
"s_add_u32 m0, s61, 65536\n"
"v_mfma_f32_16x16x32_bf16 v[88:91], a[104:107], a[232:235], v[88:91]\n"
"s_add_u32 s66, s74, 55296\n"
"v_accvgpr_read_b32 v251, a181\n"
"v_mfma_f32_16x16x32_bf16 v[88:91], a[108:111], a[236:239], v[88:91]\n"
"buffer_load_dwordx4 v4, s[44:47], s66 offen offset:0 lds\n"
"v_mfma_f32_16x16x32_bf16 v[104:107], a[104:107], a[240:243], v[104:107]\n"
"s_add_u32 s82, s82, 128\n"
"s_cmp_eq_u32 s82, 1152\n"
"s_cselect_b32 s82, 0, s82\n"
"v_mfma_f32_16x16x32_bf16 v[104:107], a[108:111], a[244:247], v[104:107]\n"
"buffer_store_dwordx4 v[240:243], v11, s[52:55], s73 offen offset:0 sc1\n"
"v_mfma_f32_16x16x32_bf16 v[120:123], a[104:107], a[248:251], v[120:123]\n"
"s_add_u32 s67, s74, 63488\n"
"v_accvgpr_read_b32 v240, a176\n"
"v_mfma_f32_16x16x32_bf16 v[120:123], a[108:111], a[252:255], v[120:123]\n"
"buffer_load_dwordx4 v5, s[44:47], s67 offen offset:1024 lds\n"
"v_mfma_f32_16x16x32_bf16 v[192:195], a[104:107], v[232:235], v[192:195]\n"
"v_accvgpr_read_b32 v241, a177\n"
"v_accvgpr_read_b32 v242, a178\n"
"v_mfma_f32_16x16x32_bf16 v[192:195], a[108:111], v[236:239], v[192:195]\n"
"v_accvgpr_read_b32 v243, a179\n"
"v_mul_f32 v240, v248, v240\n"
"v_mfma_f32_16x16x32_bf16 v[64:67], a[112:115], a[216:219], v[64:67]\n"
"v_mul_f32 v241, v248, v241\n"
"v_accvgpr_read_b32 v252, a182\n"
"v_mfma_f32_16x16x32_bf16 v[64:67], a[116:119], a[220:223], v[64:67]\n"
"v_accvgpr_read_b32 v253, a183\n"
"v_mul_f32 v242, v248, v242\n"
"v_mfma_f32_16x16x32_bf16 v[76:79], a[112:115], a[224:227], v[76:79]\n"
"s_add_u32 s68, s74, 71680\n"
"v_mul_f32 v243, v248, v243\n"
"v_mfma_f32_16x16x32_bf16 v[76:79], a[116:119], a[228:231], v[76:79]\n"
"buffer_load_dwordx4 v4, s[44:47], s68 offen offset:2048 lds\n"
"v_mfma_f32_16x16x32_bf16 v[92:95], a[112:115], a[232:235], v[92:95]\n"
"v_cvt_pk_bf16_f32 v240, v240, v241\n"
"v_mul_f32 v250, v248, v250\n"
"v_mfma_f32_16x16x32_bf16 v[92:95], a[116:119], a[236:239], v[92:95]\n"
"v_mul_f32 v251, v248, v251\n"
"v_mul_f32 v252, v248, v252\n"
"v_mfma_f32_16x16x32_bf16 v[108:111], a[112:115], a[240:243], v[108:111]\n"
"v_mul_f32 v253, v248, v253\n"
"v_cvt_pk_bf16_f32 v241, v242, v243\n"
"v_mfma_f32_16x16x32_bf16 v[108:111], a[116:119], a[244:247], v[108:111]\n"
"v_cvt_pk_bf16_f32 v242, v250, v251\n"
"v_cvt_pk_bf16_f32 v243, v252, v253\n"
"v_mfma_f32_16x16x32_bf16 v[124:127], a[112:115], a[248:251], v[124:127]\n"
"s_add_u32 s72, s59, 402432\n"
"v_accvgpr_read_b32 v250, a188\n"
"v_mfma_f32_16x16x32_bf16 v[124:127], a[116:119], a[252:255], v[124:127]\n"
"s_waitcnt vmcnt(9)\n"
"s_barrier\n"
"v_mfma_f32_16x16x32_bf16 v[196:199], a[112:115], v[232:235], v[196:199]\n"
"ds_read_b64_tr_b16 a[72:73], v9 offset:32768\n"
"ds_read_b64_tr_b16 a[74:75], v9 offset:33280\n"
"v_mfma_f32_16x16x32_bf16 v[196:199], a[116:119], v[236:239], v[196:199]\n"
"ds_read_b64_tr_b16 a[76:77], v9 offset:36864\n"
"ds_read_b64_tr_b16 a[78:79], v9 offset:37376\n"
"v_mfma_f32_16x16x32_bf16 v[188:191], a[120:123], a[216:219], v[188:191]\n"
"ds_read_b64_tr_b16 a[80:81], v9 offset:32776\n"
"ds_read_b64_tr_b16 a[82:83], v9 offset:33288\n"
"v_mfma_f32_16x16x32_bf16 v[188:191], a[124:127], a[220:223], v[188:191]\n"
"ds_read_b64_tr_b16 a[84:85], v9 offset:36872\n"
"ds_read_b64_tr_b16 a[86:87], v9 offset:37384\n"
"v_mfma_f32_16x16x32_bf16 v[80:83], a[120:123], a[224:227], v[80:83]\n"
"ds_read_b64_tr_b16 a[88:89], v9 offset:33024\n"
"ds_read_b64_tr_b16 a[90:91], v9 offset:33536\n"
"v_mfma_f32_16x16x32_bf16 v[80:83], a[124:127], a[228:231], v[80:83]\n"
"ds_read_b64_tr_b16 a[92:93], v9 offset:37120\n"
"ds_read_b64_tr_b16 a[94:95], v9 offset:37632\n"
"v_mfma_f32_16x16x32_bf16 v[96:99], a[120:123], a[232:235], v[96:99]\n"
"s_add_u32 m0, s60, 147456\n"
"s_add_u32 s69, s56, 0\n"
"v_mfma_f32_16x16x32_bf16 v[96:99], a[124:127], a[236:239], v[96:99]\n"
"buffer_load_dwordx4 v1, s[40:43], s69 offen offset:0 nt lds\n"
"v_mfma_f32_16x16x32_bf16 v[112:115], a[120:123], a[240:243], v[112:115]\n"
"buffer_load_dwordx4 v2, s[40:43], s69 offen offset:1024 nt lds\n"
"v_mfma_f32_16x16x32_bf16 v[112:115], a[124:127], a[244:247], v[112:115]\n"
"buffer_load_dwordx4 v1, s[40:43], s69 offen offset:2048 nt lds\n"
"v_mfma_f32_16x16x32_bf16 v[128:131], a[120:123], a[248:251], v[128:131]\n"
"buffer_load_dwordx4 v2, s[40:43], s69 offen offset:3072 nt lds\n"
"v_mfma_f32_16x16x32_bf16 v[128:131], a[124:127], a[252:255], v[128:131]\n"
"s_add_u32 m0, s60, 131072\n"
"s_add_u32 s70, s56, 16384\n"
"v_mfma_f32_16x16x32_bf16 v[200:203], a[120:123], v[232:235], v[200:203]\n"
"buffer_load_dwordx4 v1, s[36:39], s70 offen offset:0 nt lds\n"
"v_mfma_f32_16x16x32_bf16 v[200:203], a[124:127], v[236:239], v[200:203]\n"
"buffer_load_dwordx4 v2, s[36:39], s70 offen offset:1024 nt lds\n"
"v_mfma_f32_16x16x32_bf16 v[68:71], a[128:131], a[216:219], v[68:71]\n"
"buffer_load_dwordx4 v1, s[36:39], s70 offen offset:2048 nt lds\n"
"v_mfma_f32_16x16x32_bf16 v[68:71], a[132:135], a[220:223], v[68:71]\n"
"buffer_load_dwordx4 v2, s[36:39], s70 offen offset:3072 nt lds\n"
"v_mfma_f32_16x16x32_bf16 v[84:87], a[128:131], a[224:227], v[84:87]\n"
"s_add_u32 s75, s58, s83\n"
"s_add_u32 m0, s60, 16384\n"
"v_mfma_f32_16x16x32_bf16 v[84:87], a[132:135], a[228:231], v[84:87]\n"
"s_add_u32 s71, s75, 0\n"
"ds_read_b128 a[216:219], v10 offset:32768\n"
"v_mfma_f32_16x16x32_bf16 v[100:103], a[128:131], a[232:235], v[100:103]\n"
"buffer_load_dwordx4 v3, s[48:51], s71 offen offset:0 lds\n"
"v_mfma_f32_16x16x32_bf16 v[100:103], a[132:135], a[236:239], v[100:103]\n"
"ds_read_b128 a[220:223], v10 offset:33280\n"
"ds_read_b128 a[224:227], v10 offset:34816\n"
"v_mfma_f32_16x16x32_bf16 v[116:119], a[128:131], a[240:243], v[116:119]\n"
"s_add_u32 s64, s75, 64512\n"
"ds_read_b128 a[228:231], v10 offset:35328\n"
"v_mfma_f32_16x16x32_bf16 v[116:119], a[132:135], a[244:247], v[116:119]\n"
"buffer_load_dwordx4 v3, s[48:51], s64 offen offset:1024 lds\n"
"v_mfma_f32_16x16x32_bf16 v[132:135], a[128:131], a[248:251], v[132:135]\n"
"ds_read_b128 a[232:235], v10 offset:36864\n"
"ds_read_b128 a[236:239], v10 offset:37376\n"
"v_mfma_f32_16x16x32_bf16 v[132:135], a[132:135], a[252:255], v[132:135]\n"
"ds_read_b64_tr_b16 a[96:97], v9 offset:33032\n"
"ds_read_b64_tr_b16 a[98:99], v9 offset:33544\n"
"v_mfma_f32_16x16x32_bf16 v[204:207], a[128:131], v[232:235], v[204:207]\n"
"s_add_u32 s65, s75, 129024\n"
"ds_read_b64_tr_b16 a[100:101], v9 offset:37128\n"
"v_mfma_f32_16x16x32_bf16 v[204:207], a[132:135], v[236:239], v[204:207]\n"
"ds_read_b64_tr_b16 a[102:103], v9 offset:37640\n"
"s_waitcnt lgkmcnt(10)\n"
"v_mfma_f32_16x16x32_bf16 v[136:139], a[72:75], a[0:3], v[136:139]\n"
"buffer_load_dwordx4 v3, s[48:51], s65 offen offset:2048 lds\n"
"v_mfma_f32_16x16x32_bf16 v[136:139], a[76:79], a[4:7], v[136:139]\n"
"v_add_u32 v9, 0x10000, v9\n"
"v_add_u32 v10, 0x10000, v10\n"
"v_mfma_f32_16x16x32_bf16 v[140:143], a[72:75], a[8:11], v[140:143]\n"
"buffer_store_dwordx4 v[240:243], v11, s[52:55], s72 offen offset:1024 sc1\n"
"v_mfma_f32_16x16x32_bf16 v[140:143], a[76:79], a[12:15], v[140:143]\n"
"s_add_u32 s66, s75, 193536\n"
"v_accvgpr_read_b32 v240, a184\n"
"v_mfma_f32_16x16x32_bf16 v[144:147], a[72:75], a[16:19], v[144:147]\n"
"buffer_load_dwordx4 v3, s[48:51], s66 offen offset:3072 lds\n"
"v_mfma_f32_16x16x32_bf16 v[144:147], a[76:79], a[20:23], v[144:147]\n"
"v_accvgpr_read_b32 v241, a185\n"
"v_accvgpr_read_b32 v242, a186\n"
"v_mfma_f32_16x16x32_bf16 v[148:151], a[72:75], a[24:27], v[148:151]\n"
"v_accvgpr_read_b32 v243, a187\n"
"v_mul_f32 v240, v249, v240\n"
"v_mfma_f32_16x16x32_bf16 v[148:151], a[76:79], a[28:31], v[148:151]\n"
"s_add_u32 s74, s57, s82\n"
"s_add_u32 m0, s61, 0\n"
"v_mfma_f32_16x16x32_bf16 v[152:155], a[72:75], a[32:35], v[152:155]\n"
"s_add_u32 s67, s74, 0\n"
"v_mul_f32 v241, v249, v241\n"
"v_mfma_f32_16x16x32_bf16 v[152:155], a[76:79], a[36:39], v[152:155]\n"
"buffer_load_dwordx4 v4, s[44:47], s67 offen offset:0 lds\n"
"v_mfma_f32_16x16x32_bf16 v[156:159], a[72:75], a[40:43], v[156:159]\n"
"v_accvgpr_read_b32 v251, a189\n"
"v_mul_f32 v242, v249, v242\n"
"v_mfma_f32_16x16x32_bf16 v[156:159], a[76:79], a[44:47], v[156:159]\n"
"s_add_u32 s68, s74, 8192\n"
"v_mul_f32 v243, v249, v243\n"
"v_mfma_f32_16x16x32_bf16 v[16:19], a[80:83], a[0:3], v[16:19]\n"
"buffer_load_dwordx4 v5, s[44:47], s68 offen offset:1024 lds\n"
"v_mfma_f32_16x16x32_bf16 v[16:19], a[84:87], a[4:7], v[16:19]\n"
"v_cvt_pk_bf16_f32 v240, v240, v241\n"
"v_mul_f32 v250, v249, v250\n"
"v_mfma_f32_16x16x32_bf16 v[20:23], a[80:83], a[8:11], v[20:23]\n"
"v_mul_f32 v251, v249, v251\n"
"v_cvt_pk_bf16_f32 v241, v242, v243\n"
"v_mfma_f32_16x16x32_bf16 v[20:23], a[84:87], a[12:15], v[20:23]\n"
"v_cvt_pk_bf16_f32 v242, v250, v251\n"
"v_accvgpr_read_b32 v252, a190\n"
"v_mfma_f32_16x16x32_bf16 v[24:27], a[80:83], a[16:19], v[24:27]\n"
"s_add_u32 s69, s74, 16384\n"
"v_accvgpr_read_b32 v253, a191\n"
"v_mfma_f32_16x16x32_bf16 v[24:27], a[84:87], a[20:23], v[24:27]\n"
"buffer_load_dwordx4 v4, s[44:47], s69 offen offset:2048 lds\n"
"v_mfma_f32_16x16x32_bf16 v[28:31], a[80:83], a[24:27], v[28:31]\n"
"v_mul_f32 v252, v249, v252\n"
"v_mul_f32 v253, v249, v253\n"
"v_mfma_f32_16x16x32_bf16 v[28:31], a[84:87], a[28:31], v[28:31]\n"
"v_cvt_pk_bf16_f32 v243, v252, v253\n"
"v_add_u32 v7, 0x20000, v7\n"
"v_mfma_f32_16x16x32_bf16 v[32:35], a[80:83], a[32:35], v[32:35]\n"
"v_add_u32 v8, 0x20000, v8\n"
"s_add_u32 s73, s59, 402432\n"
"v_mfma_f32_16x16x32_bf16 v[32:35], a[84:87], a[36:39], v[32:35]\n"
"s_waitcnt vmcnt(16) lgkmcnt(0)\n"
"s_barrier\n"
"v_mfma_f32_16x16x32_bf16 v[36:39], a[80:83], a[40:43], v[36:39]\n"
"ds_read_b64_tr_b16 a[104:105], v9 offset:0\n"
"ds_read_b64_tr_b16 a[106:107], v9 offset:512\n"
"v_mfma_f32_16x16x32_bf16 v[36:39], a[84:87], a[44:47], v[36:39]\n"
"ds_read_b64_tr_b16 a[108:109], v9 offset:4096\n"
"ds_read_b64_tr_b16 a[110:111], v9 offset:4608\n"
"v_mfma_f32_16x16x32_bf16 v[160:163], a[88:91], a[0:3], v[160:163]\n"
"ds_read_b64_tr_b16 a[112:113], v9 offset:8\n"
"ds_read_b64_tr_b16 a[114:115], v9 offset:520\n"
"v_mfma_f32_16x16x32_bf16 v[160:163], a[92:95], a[4:7], v[160:163]\n"
"ds_read_b64_tr_b16 a[116:117], v9 offset:4104\n"
"ds_read_b64_tr_b16 a[118:119], v9 offset:4616\n"
"v_mfma_f32_16x16x32_bf16 v[164:167], a[88:91], a[8:11], v[164:167]\n"
"ds_read_b64_tr_b16 a[120:121], v9 offset:256\n"
"ds_read_b64_tr_b16 a[122:123], v9 offset:768\n"
"v_mfma_f32_16x16x32_bf16 v[164:167], a[92:95], a[12:15], v[164:167]\n"
"ds_read_b64_tr_b16 a[124:125], v9 offset:4352\n"
"ds_read_b64_tr_b16 a[126:127], v9 offset:4864\n"
"v_mfma_f32_16x16x32_bf16 v[168:171], a[88:91], a[16:19], v[168:171]\n"
"ds_read_b128 a[240:243], v10 offset:0\n"
"ds_read_b128 a[244:247], v10 offset:512\n"
"v_mfma_f32_16x16x32_bf16 v[168:171], a[92:95], a[20:23], v[168:171]\n"
"s_add_u32 s75, s58, s83\n"
"s_add_u32 m0, s60, 49152\n"
"v_mfma_f32_16x16x32_bf16 v[172:175], a[88:91], a[24:27], v[172:175]\n"
"s_add_u32 s70, s75, 128\n"
"ds_read_b128 a[248:251], v10 offset:2048\n"
"v_mfma_f32_16x16x32_bf16 v[172:175], a[92:95], a[28:31], v[172:175]\n"
"buffer_load_dwordx4 v3, s[48:51], s70 offen offset:0 lds\n"
"v_mfma_f32_16x16x32_bf16 v[176:179], a[88:91], a[32:35], v[176:179]\n"
"ds_read_b128 a[252:255], v10 offset:2560\n"
"ds_read_b128 v[232:235], v10 offset:4096\n"
"v_mfma_f32_16x16x32_bf16 v[176:179], a[92:95], a[36:39], v[176:179]\n"
"ds_read_b128 v[236:239], v10 offset:4608\n"
"ds_read_b64_tr_b16 a[128:129], v9 offset:264\n"
"v_mfma_f32_16x16x32_bf16 v[180:183], a[88:91], a[40:43], v[180:183]\n"
"s_add_u32 s71, s75, 64640\n"
"ds_read_b64_tr_b16 a[130:131], v9 offset:776\n"
"v_mfma_f32_16x16x32_bf16 v[180:183], a[92:95], a[44:47], v[180:183]\n"
"buffer_load_dwordx4 v3, s[48:51], s71 offen offset:1024 lds\n"
"v_mfma_f32_16x16x32_bf16 v[40:43], a[96:99], a[0:3], v[40:43]\n"
"ds_read_b64_tr_b16 a[132:133], v9 offset:4360\n"
"ds_read_b64_tr_b16 a[134:135], v9 offset:4872\n"
"v_mfma_f32_16x16x32_bf16 v[40:43], a[100:103], a[4:7], v[40:43]\n"
"s_add_u32 s83, s83, 524288\n"
"s_cmp_eq_u32 s83, 4718592\n"
"s_cselect_b32 s83, 0, s83\n"
"v_mfma_f32_16x16x32_bf16 v[44:47], a[96:99], a[8:11], v[44:47]\n"
"v_add_u32 v9, 0xffff0000, v9\n"
"v_add_u32 v10, 0xffff0000, v10\n"
"v_mfma_f32_16x16x32_bf16 v[44:47], a[100:103], a[12:15], v[44:47]\n"
"buffer_store_dwordx4 a[48:51], v11, s[96:99], s89 offen offset:0 sc1\n"
"v_mfma_f32_16x16x32_bf16 v[48:51], a[96:99], a[16:19], v[48:51]\n"
"s_add_u32 s64, s75, 129152\n"
"s_add_u32 s89, s89, 4096\n"
"v_mfma_f32_16x16x32_bf16 v[48:51], a[100:103], a[20:23], v[48:51]\n"
"buffer_load_dwordx4 v3, s[48:51], s64 offen offset:2048 lds\n"
"v_mfma_f32_16x16x32_bf16 v[52:55], a[96:99], a[24:27], v[52:55]\n"
"buffer_store_dwordx4 a[52:55], v11, s[96:99], s89 offen offset:0 sc1\n"
"v_mfma_f32_16x16x32_bf16 v[52:55], a[100:103], a[28:31], v[52:55]\n"
"s_add_u32 s89, s89, 4096\n"
"s_mul_i32 s72, s91, 12288\n"
"v_mfma_f32_16x16x32_bf16 v[56:59], a[96:99], a[32:35], v[56:59]\n"
"buffer_store_dwordx4 a[56:59], v11, s[96:99], s89 offen offset:0 sc1\n"
"v_mfma_f32_16x16x32_bf16 v[56:59], a[100:103], a[36:39], v[56:59]\n"
"s_add_u32 s65, s75, 193664\n"
"s_add_u32 s89, s89, 4096\n"
"v_mfma_f32_16x16x32_bf16 v[60:63], a[96:99], a[40:43], v[60:63]\n"
"buffer_load_dwordx4 v3, s[48:51], s65 offen offset:3072 lds\n"
"v_mfma_f32_16x16x32_bf16 v[60:63], a[100:103], a[44:47], v[60:63]\n"
"s_add_u32 s59, s59, s72\n"
"s_waitcnt lgkmcnt(0)\n"
"v_mfma_f32_16x16x32_bf16 v[184:187], a[104:107], a[0:3], v[184:187]\n"
"buffer_store_dwordx4 a[60:63], v11, s[96:99], s89 offen offset:0 sc1\n"
"v_mfma_f32_16x16x32_bf16 v[184:187], a[108:111], a[4:7], v[184:187]\n"
"s_add_u32 s89, s89, 4096\n"
"s_add_u32 s72, s59, 0\n"
"v_mfma_f32_16x16x32_bf16 v[72:75], a[104:107], a[8:11], v[72:75]\n"
"buffer_store_dwordx4 a[64:67], v11, s[96:99], s89 offen offset:0 sc1\n"
"v_mfma_f32_16x16x32_bf16 v[72:75], a[108:111], a[12:15], v[72:75]\n"
"s_add_u32 s74, s57, s82\n"
"s_add_u32 m0, s61, 32768\n"
"v_mfma_f32_16x16x32_bf16 v[88:91], a[104:107], a[16:19], v[88:91]\n"
"s_add_u32 s66, s74, 55296\n"
"s_add_u32 s88, s88, 512\n"
"v_mfma_f32_16x16x32_bf16 v[88:91], a[108:111], a[20:23], v[88:91]\n"
"buffer_load_dwordx4 v4, s[44:47], s66 offen offset:0 lds\n"
"v_mfma_f32_16x16x32_bf16 v[104:107], a[104:107], a[24:27], v[104:107]\n"
"s_add_u32 s82, s82, 128\n"
"s_cmp_eq_u32 s82, 1152\n"
"s_cselect_b32 s82, 0, s82\n"
"v_mfma_f32_16x16x32_bf16 v[104:107], a[108:111], a[28:31], v[104:107]\n"
"buffer_store_dwordx4 v[240:243], v11, s[52:55], s73 offen offset:2048 sc1\n"
"v_mfma_f32_16x16x32_bf16 v[120:123], a[104:107], a[32:35], v[120:123]\n"
"s_add_u32 s67, s74, 63488\n"
"v_mov_b32 v240, 0x3f803f80\n"
"v_mfma_f32_16x16x32_bf16 v[120:123], a[108:111], a[36:39], v[120:123]\n"
"buffer_load_dwordx4 v5, s[44:47], s67 offen offset:1024 lds\n"
"v_mfma_f32_16x16x32_bf16 v[192:195], a[104:107], a[40:43], v[192:195]\n"
"v_mov_b32 v241, 0x3f803f80\n"
"v_mov_b32 v242, 0x3f803f80\n"
"v_mfma_f32_16x16x32_bf16 v[192:195], a[108:111], a[44:47], v[192:195]\n"
"v_mov_b32 v243, 0x3f803f80\n"
"s_mov_b32 s54, s80\n"
"v_mfma_f32_16x16x32_bf16 v[64:67], a[112:115], a[0:3], v[64:67]\n"
"s_add_u32 s73, s59, 0\n"
"v_mfma_f32_16x16x32_bf16 v[64:67], a[116:119], a[4:7], v[64:67]\n"
"v_mfma_f32_16x16x32_bf16 v[76:79], a[112:115], a[8:11], v[76:79]\n"
"s_add_u32 s68, s74, 71680\n"
"v_mfma_f32_16x16x32_bf16 v[76:79], a[116:119], a[12:15], v[76:79]\n"
"buffer_load_dwordx4 v4, s[44:47], s68 offen offset:2048 lds\n"
"v_mfma_f32_16x16x32_bf16 v[92:95], a[112:115], a[16:19], v[92:95]\n"
"v_mfma_f32_16x16x32_bf16 v[92:95], a[116:119], a[20:23], v[92:95]\n"
"v_mfma_f32_16x16x32_bf16 v[108:111], a[112:115], a[24:27], v[108:111]\n"
"v_mfma_f32_16x16x32_bf16 v[108:111], a[116:119], a[28:31], v[108:111]\n"
"v_mfma_f32_16x16x32_bf16 v[124:127], a[112:115], a[32:35], v[124:127]\n"
"v_mfma_f32_16x16x32_bf16 v[124:127], a[116:119], a[36:39], v[124:127]\n"
"s_waitcnt vmcnt(13)\n"
"s_barrier\n"
"v_mfma_f32_16x16x32_bf16 v[196:199], a[112:115], a[40:43], v[196:199]\n"
"ds_read_b64_tr_b16 a[72:73], v9 offset:0\n"
"ds_read_b64_tr_b16 a[74:75], v9 offset:512\n"
"v_mfma_f32_16x16x32_bf16 v[196:199], a[116:119], a[44:47], v[196:199]\n"
"ds_read_b64_tr_b16 a[76:77], v9 offset:4096\n"
"ds_read_b64_tr_b16 a[78:79], v9 offset:4608\n"
"v_mfma_f32_16x16x32_bf16 v[188:191], a[120:123], a[0:3], v[188:191]\n"
"ds_read_b64_tr_b16 a[80:81], v9 offset:8\n"
"ds_read_b64_tr_b16 a[82:83], v9 offset:520\n"
"v_mfma_f32_16x16x32_bf16 v[188:191], a[124:127], a[4:7], v[188:191]\n"
"ds_read_b64_tr_b16 a[84:85], v9 offset:4104\n"
"ds_read_b64_tr_b16 a[86:87], v9 offset:4616\n"
"v_mfma_f32_16x16x32_bf16 v[80:83], a[120:123], a[8:11], v[80:83]\n"
"ds_read_b64_tr_b16 a[88:89], v9 offset:256\n"
"ds_read_b64_tr_b16 a[90:91], v9 offset:768\n"
"v_mfma_f32_16x16x32_bf16 v[80:83], a[124:127], a[12:15], v[80:83]\n"
"ds_read_b64_tr_b16 a[92:93], v9 offset:4352\n"
"ds_read_b64_tr_b16 a[94:95], v9 offset:4864\n"
"v_mfma_f32_16x16x32_bf16 v[96:99], a[120:123], a[16:19], v[96:99]\n"
"ds_read_b64_tr_b16 a[96:97], v9 offset:264\n"
"ds_read_b64_tr_b16 a[98:99], v9 offset:776\n"
"v_mfma_f32_16x16x32_bf16 v[96:99], a[124:127], a[20:23], v[96:99]\n"
"s_add_u32 s75, s58, s83\n"
"s_add_u32 m0, s60, 81920\n"
"v_mfma_f32_16x16x32_bf16 v[112:115], a[120:123], a[24:27], v[112:115]\n"
"s_add_u32 s69, s75, 0\n"
"ds_read_b64_tr_b16 a[100:101], v9 offset:4360\n"
"v_mfma_f32_16x16x32_bf16 v[112:115], a[124:127], a[28:31], v[112:115]\n"
"buffer_load_dwordx4 v3, s[48:51], s69 offen offset:0 lds\n"
"v_mfma_f32_16x16x32_bf16 v[128:131], a[120:123], a[32:35], v[128:131]\n"
"ds_read_b64_tr_b16 a[102:103], v9 offset:4872\n"
"v_mfma_f32_16x16x32_bf16 v[128:131], a[124:127], a[36:39], v[128:131]\n"
"v_mfma_f32_16x16x32_bf16 v[200:203], a[120:123], a[40:43], v[200:203]\n"
"s_add_u32 s70, s75, 64512\n"
"v_mfma_f32_16x16x32_bf16 v[200:203], a[124:127], a[44:47], v[200:203]\n"
"buffer_load_dwordx4 v3, s[48:51], s70 offen offset:1024 lds\n"
"v_mfma_f32_16x16x32_bf16 v[68:71], a[128:131], a[0:3], v[68:71]\n"
"ds_read_b128 a[0:3], v10 offset:0\n"
"v_mfma_f32_16x16x32_bf16 v[68:71], a[132:135], a[4:7], v[68:71]\n"
"ds_read_b128 a[4:7], v10 offset:512\n"
"v_mfma_f32_16x16x32_bf16 v[84:87], a[128:131], a[8:11], v[84:87]\n"
"ds_read_b128 a[8:11], v10 offset:2048\n"
"v_mfma_f32_16x16x32_bf16 v[84:87], a[132:135], a[12:15], v[84:87]\n"
"ds_read_b128 a[12:15], v10 offset:2560\n"
"v_mfma_f32_16x16x32_bf16 v[100:103], a[128:131], a[16:19], v[100:103]\n"
"s_add_u32 s71, s75, 129024\n"
"ds_read_b128 a[16:19], v10 offset:4096\n"
"v_mfma_f32_16x16x32_bf16 v[100:103], a[132:135], a[20:23], v[100:103]\n"
"buffer_load_dwordx4 v3, s[48:51], s71 offen offset:2048 lds\n"
"v_mfma_f32_16x16x32_bf16 v[116:119], a[128:131], a[24:27], v[116:119]\n"
"ds_read_b128 a[20:23], v10 offset:4608\n"
"v_mfma_f32_16x16x32_bf16 v[116:119], a[132:135], a[28:31], v[116:119]\n"
"v_mfma_f32_16x16x32_bf16 v[132:135], a[128:131], a[32:35], v[132:135]\n"
"v_mfma_f32_16x16x32_bf16 v[132:135], a[132:135], a[36:39], v[132:135]\n"
"s_add_u32 s64, s75, 193536\n"
"v_mfma_f32_16x16x32_bf16 v[204:207], a[128:131], a[40:43], v[204:207]\n"
"buffer_load_dwordx4 v3, s[48:51], s64 offen offset:3072 lds\n"
"v_mfma_f32_16x16x32_bf16 v[204:207], a[132:135], a[44:47], v[204:207]\n"
"s_waitcnt lgkmcnt(3)\n"
"v_mfma_f32_16x16x32_bf16 v[136:139], a[72:75], a[216:219], v[136:139]\n"
"v_mfma_f32_16x16x32_bf16 v[136:139], a[76:79], a[220:223], v[136:139]\n"
"v_mfma_f32_16x16x32_bf16 v[140:143], a[72:75], a[224:227], v[140:143]\n"
"v_mfma_f32_16x16x32_bf16 v[140:143], a[76:79], a[228:231], v[140:143]\n"
"s_add_u32 s74, s57, s82\n"
"s_add_u32 m0, s61, 65536\n"
"v_mfma_f32_16x16x32_bf16 v[144:147], a[72:75], a[232:235], v[144:147]\n"
"s_add_u32 s65, s74, 0\n"
"v_mfma_f32_16x16x32_bf16 v[144:147], a[76:79], a[236:239], v[144:147]\n"
"buffer_load_dwordx4 v4, s[44:47], s65 offen offset:0 lds\n"
"v_mfma_f32_16x16x32_bf16 v[148:151], a[72:75], a[240:243], v[148:151]\n"
"v_mfma_f32_16x16x32_bf16 v[148:151], a[76:79], a[244:247], v[148:151]\n"
"v_mfma_f32_16x16x32_bf16 v[152:155], a[72:75], a[248:251], v[152:155]\n"
"s_add_u32 s66, s74, 8192\n"
"v_mfma_f32_16x16x32_bf16 v[152:155], a[76:79], a[252:255], v[152:155]\n"
"buffer_load_dwordx4 v5, s[44:47], s66 offen offset:1024 lds\n"
"v_mfma_f32_16x16x32_bf16 v[156:159], a[72:75], v[232:235], v[156:159]\n"
"v_mfma_f32_16x16x32_bf16 v[156:159], a[76:79], v[236:239], v[156:159]\n"
"v_mfma_f32_16x16x32_bf16 v[16:19], a[80:83], a[216:219], v[16:19]\n"
"v_mfma_f32_16x16x32_bf16 v[16:19], a[84:87], a[220:223], v[16:19]\n"
"v_mfma_f32_16x16x32_bf16 v[20:23], a[80:83], a[224:227], v[20:23]\n"
"s_add_u32 s67, s74, 16384\n"
"v_mfma_f32_16x16x32_bf16 v[20:23], a[84:87], a[228:231], v[20:23]\n"
"buffer_load_dwordx4 v4, s[44:47], s67 offen offset:2048 lds\n"
"v_mfma_f32_16x16x32_bf16 v[24:27], a[80:83], a[232:235], v[24:27]\n"
"v_mfma_f32_16x16x32_bf16 v[24:27], a[84:87], a[236:239], v[24:27]\n"
"v_mfma_f32_16x16x32_bf16 v[28:31], a[80:83], a[240:243], v[28:31]\n"
"v_mfma_f32_16x16x32_bf16 v[28:31], a[84:87], a[244:247], v[28:31]\n"
"v_mfma_f32_16x16x32_bf16 v[32:35], a[80:83], a[248:251], v[32:35]\n"
"v_mfma_f32_16x16x32_bf16 v[32:35], a[84:87], a[252:255], v[32:35]\n"
"s_waitcnt vmcnt(7) lgkmcnt(0)\n"
"s_barrier\n"
"v_mfma_f32_16x16x32_bf16 v[36:39], a[80:83], v[232:235], v[36:39]\n"
"ds_read_b64_tr_b16 a[104:105], v9 offset:32768\n"
"ds_read_b64_tr_b16 a[106:107], v9 offset:33280\n"
"v_mfma_f32_16x16x32_bf16 v[36:39], a[84:87], v[236:239], v[36:39]\n"
"ds_read_b64_tr_b16 a[108:109], v9 offset:36864\n"
"ds_read_b64_tr_b16 a[110:111], v9 offset:37376\n"
"v_mfma_f32_16x16x32_bf16 v[160:163], a[88:91], a[216:219], v[160:163]\n"
"ds_read_b64_tr_b16 a[112:113], v9 offset:32776\n"
"ds_read_b64_tr_b16 a[114:115], v9 offset:33288\n"
"v_mfma_f32_16x16x32_bf16 v[160:163], a[92:95], a[220:223], v[160:163]\n"
"ds_read_b64_tr_b16 a[116:117], v9 offset:36872\n"
"ds_read_b64_tr_b16 a[118:119], v9 offset:37384\n"
"v_mfma_f32_16x16x32_bf16 v[164:167], a[88:91], a[224:227], v[164:167]\n"
"ds_read_b64_tr_b16 a[120:121], v9 offset:33024\n"
"ds_read_b64_tr_b16 a[122:123], v9 offset:33536\n"
"v_mfma_f32_16x16x32_bf16 v[164:167], a[92:95], a[228:231], v[164:167]\n"
"ds_read_b64_tr_b16 a[124:125], v9 offset:37120\n"
"ds_read_b64_tr_b16 a[126:127], v9 offset:37632\n"
"v_mfma_f32_16x16x32_bf16 v[168:171], a[88:91], a[232:235], v[168:171]\n"
"ds_read_b128 a[24:27], v10 offset:32768\n"
"ds_read_b128 a[28:31], v10 offset:33280\n"
"v_mfma_f32_16x16x32_bf16 v[168:171], a[92:95], a[236:239], v[168:171]\n"
"s_add_u32 s75, s58, s83\n"
"s_add_u32 m0, s60, 16384\n"
"v_mfma_f32_16x16x32_bf16 v[172:175], a[88:91], a[240:243], v[172:175]\n"
"s_add_u32 s68, s75, 128\n"
"ds_read_b128 a[32:35], v10 offset:34816\n"
"v_mfma_f32_16x16x32_bf16 v[172:175], a[92:95], a[244:247], v[172:175]\n"
"buffer_load_dwordx4 v3, s[48:51], s68 offen offset:0 lds\n"
"v_mfma_f32_16x16x32_bf16 v[176:179], a[88:91], a[248:251], v[176:179]\n"
"ds_read_b128 a[36:39], v10 offset:35328\n"
"ds_read_b128 a[40:43], v10 offset:36864\n"
"v_mfma_f32_16x16x32_bf16 v[176:179], a[92:95], a[252:255], v[176:179]\n"
"ds_read_b128 a[44:47], v10 offset:37376\n"
"ds_read_b64_tr_b16 a[128:129], v9 offset:33032\n"
"v_mfma_f32_16x16x32_bf16 v[180:183], a[88:91], v[232:235], v[180:183]\n"
"s_add_u32 s69, s75, 64640\n"
"ds_read_b64_tr_b16 a[130:131], v9 offset:33544\n"
"v_mfma_f32_16x16x32_bf16 v[180:183], a[92:95], v[236:239], v[180:183]\n"
"buffer_load_dwordx4 v3, s[48:51], s69 offen offset:1024 lds\n"
"v_mfma_f32_16x16x32_bf16 v[40:43], a[96:99], a[216:219], v[40:43]\n"
"ds_read_b64_tr_b16 a[132:133], v9 offset:37128\n"
"ds_read_b64_tr_b16 a[134:135], v9 offset:37640\n"
"v_mfma_f32_16x16x32_bf16 v[40:43], a[100:103], a[220:223], v[40:43]\n"
"s_add_u32 s83, s83, 524288\n"
"s_cmp_eq_u32 s83, 4718592\n"
"s_cselect_b32 s83, 0, s83\n"
"v_mfma_f32_16x16x32_bf16 v[44:47], a[96:99], a[224:227], v[44:47]\n"
"v_add_u32 v9, 0x10000, v9\n"
"v_add_u32 v10, 0x10000, v10\n"
"v_mfma_f32_16x16x32_bf16 v[44:47], a[100:103], a[228:231], v[44:47]\n"
"v_mfma_f32_16x16x32_bf16 v[48:51], a[96:99], a[232:235], v[48:51]\n"
"s_add_u32 s70, s75, 129152\n"
"v_mfma_f32_16x16x32_bf16 v[48:51], a[100:103], a[236:239], v[48:51]\n"
"buffer_load_dwordx4 v3, s[48:51], s70 offen offset:2048 lds\n"
"v_mfma_f32_16x16x32_bf16 v[52:55], a[96:99], a[240:243], v[52:55]\n"
"v_mfma_f32_16x16x32_bf16 v[52:55], a[100:103], a[244:247], v[52:55]\n"
"v_mfma_f32_16x16x32_bf16 v[56:59], a[96:99], a[248:251], v[56:59]\n"
"v_mfma_f32_16x16x32_bf16 v[56:59], a[100:103], a[252:255], v[56:59]\n"
"s_add_u32 s71, s75, 193664\n"
"v_mfma_f32_16x16x32_bf16 v[60:63], a[96:99], v[232:235], v[60:63]\n"
"buffer_load_dwordx4 v3, s[48:51], s71 offen offset:3072 lds\n"
"v_mfma_f32_16x16x32_bf16 v[60:63], a[100:103], v[236:239], v[60:63]\n"
"s_waitcnt lgkmcnt(0)\n"
"v_mfma_f32_16x16x32_bf16 v[184:187], a[104:107], a[216:219], v[184:187]\n"
"v_mfma_f32_16x16x32_bf16 v[184:187], a[108:111], a[220:223], v[184:187]\n"
"v_mfma_f32_16x16x32_bf16 v[72:75], a[104:107], a[224:227], v[72:75]\n"
"v_mfma_f32_16x16x32_bf16 v[72:75], a[108:111], a[228:231], v[72:75]\n"
"s_add_u32 s74, s57, s82\n"
"s_add_u32 m0, s61, 0\n"
"v_mfma_f32_16x16x32_bf16 v[88:91], a[104:107], a[232:235], v[88:91]\n"
"s_add_u32 s64, s74, 55296\n"
"v_mfma_f32_16x16x32_bf16 v[88:91], a[108:111], a[236:239], v[88:91]\n"
"buffer_load_dwordx4 v4, s[44:47], s64 offen offset:0 lds\n"
"v_mfma_f32_16x16x32_bf16 v[104:107], a[104:107], a[240:243], v[104:107]\n"
"s_add_u32 s82, s82, 128\n"
"s_cmp_eq_u32 s82, 1152\n"
"s_cselect_b32 s82, 0, s82\n"
"v_mfma_f32_16x16x32_bf16 v[104:107], a[108:111], a[244:247], v[104:107]\n"
"v_mfma_f32_16x16x32_bf16 v[120:123], a[104:107], a[248:251], v[120:123]\n"
"s_add_u32 s65, s74, 63488\n"
"v_mfma_f32_16x16x32_bf16 v[120:123], a[108:111], a[252:255], v[120:123]\n"
"buffer_load_dwordx4 v5, s[44:47], s65 offen offset:1024 lds\n"
"v_mfma_f32_16x16x32_bf16 v[192:195], a[104:107], v[232:235], v[192:195]\n"
"v_mfma_f32_16x16x32_bf16 v[192:195], a[108:111], v[236:239], v[192:195]\n"
"v_mfma_f32_16x16x32_bf16 v[64:67], a[112:115], a[216:219], v[64:67]\n"
"v_mfma_f32_16x16x32_bf16 v[64:67], a[116:119], a[220:223], v[64:67]\n"
"v_mfma_f32_16x16x32_bf16 v[76:79], a[112:115], a[224:227], v[76:79]\n"
"s_add_u32 s66, s74, 71680\n"
"v_mfma_f32_16x16x32_bf16 v[76:79], a[116:119], a[228:231], v[76:79]\n"
"buffer_load_dwordx4 v4, s[44:47], s66 offen offset:2048 lds\n"
"v_mfma_f32_16x16x32_bf16 v[92:95], a[112:115], a[232:235], v[92:95]\n"
"s_cmp_eq_u32 s62, 1\n"
"s_cselect_b32 s46, 0, s46\n"
"v_mfma_f32_16x16x32_bf16 v[92:95], a[116:119], a[236:239], v[92:95]\n"
"v_mfma_f32_16x16x32_bf16 v[108:111], a[112:115], a[240:243], v[108:111]\n"
"v_mfma_f32_16x16x32_bf16 v[108:111], a[116:119], a[244:247], v[108:111]\n"
"v_mfma_f32_16x16x32_bf16 v[124:127], a[112:115], a[248:251], v[124:127]\n"
"v_mfma_f32_16x16x32_bf16 v[124:127], a[116:119], a[252:255], v[124:127]\n"
"s_waitcnt vmcnt(7)\n"
"s_barrier\n"
"v_mfma_f32_16x16x32_bf16 v[196:199], a[112:115], v[232:235], v[196:199]\n"
"ds_read_b64_tr_b16 a[72:73], v9 offset:0\n"
"ds_read_b64_tr_b16 a[74:75], v9 offset:512\n"
"v_mfma_f32_16x16x32_bf16 v[196:199], a[116:119], v[236:239], v[196:199]\n"
"ds_read_b64_tr_b16 a[76:77], v9 offset:4096\n"
"ds_read_b64_tr_b16 a[78:79], v9 offset:4608\n"
"v_mfma_f32_16x16x32_bf16 v[188:191], a[120:123], a[216:219], v[188:191]\n"
"ds_read_b64_tr_b16 a[80:81], v9 offset:8\n"
"ds_read_b64_tr_b16 a[82:83], v9 offset:520\n"
"v_mfma_f32_16x16x32_bf16 v[188:191], a[124:127], a[220:223], v[188:191]\n"
"ds_read_b64_tr_b16 a[84:85], v9 offset:4104\n"
"ds_read_b64_tr_b16 a[86:87], v9 offset:4616\n"
"v_mfma_f32_16x16x32_bf16 v[80:83], a[120:123], a[224:227], v[80:83]\n"
"ds_read_b64_tr_b16 a[88:89], v9 offset:256\n"
"ds_read_b64_tr_b16 a[90:91], v9 offset:768\n"
"v_mfma_f32_16x16x32_bf16 v[80:83], a[124:127], a[228:231], v[80:83]\n"
"ds_read_b64_tr_b16 a[92:93], v9 offset:4352\n"
"ds_read_b64_tr_b16 a[94:95], v9 offset:4864\n"
"v_mfma_f32_16x16x32_bf16 v[96:99], a[120:123], a[232:235], v[96:99]\n"
"ds_read_b64_tr_b16 a[96:97], v9 offset:264\n"
"ds_read_b64_tr_b16 a[98:99], v9 offset:776\n"
"v_mfma_f32_16x16x32_bf16 v[96:99], a[124:127], a[236:239], v[96:99]\n"
"s_add_u32 s75, s58, s83\n"
"s_add_u32 m0, s60, 49152\n"
"v_mfma_f32_16x16x32_bf16 v[112:115], a[120:123], a[240:243], v[112:115]\n"
"s_add_u32 s67, s75, 0\n"
"ds_read_b64_tr_b16 a[100:101], v9 offset:4360\n"
"v_mfma_f32_16x16x32_bf16 v[112:115], a[124:127], a[244:247], v[112:115]\n"
"buffer_load_dwordx4 v3, s[48:51], s67 offen offset:0 lds\n"
"v_mfma_f32_16x16x32_bf16 v[128:131], a[120:123], a[248:251], v[128:131]\n"
"ds_read_b64_tr_b16 a[102:103], v9 offset:4872\n"
"v_add_u32 v9, 0xffff0000, v9\n"
"v_mfma_f32_16x16x32_bf16 v[128:131], a[124:127], a[252:255], v[128:131]\n"
"v_mfma_f32_16x16x32_bf16 v[200:203], a[120:123], v[232:235], v[200:203]\n"
"s_add_u32 s68, s75, 64512\n"
"v_mfma_f32_16x16x32_bf16 v[200:203], a[124:127], v[236:239], v[200:203]\n"
"buffer_load_dwordx4 v3, s[48:51], s68 offen offset:1024 lds\n"
"v_mfma_f32_16x16x32_bf16 v[68:71], a[128:131], a[216:219], v[68:71]\n"
"ds_read_b128 a[216:219], v10 offset:0\n"
"v_mfma_f32_16x16x32_bf16 v[68:71], a[132:135], a[220:223], v[68:71]\n"
"ds_read_b128 a[220:223], v10 offset:512\n"
"v_mfma_f32_16x16x32_bf16 v[84:87], a[128:131], a[224:227], v[84:87]\n"
"ds_read_b128 a[224:227], v10 offset:2048\n"
"v_mfma_f32_16x16x32_bf16 v[84:87], a[132:135], a[228:231], v[84:87]\n"
"ds_read_b128 a[228:231], v10 offset:2560\n"
"v_mfma_f32_16x16x32_bf16 v[100:103], a[128:131], a[232:235], v[100:103]\n"
"s_add_u32 s69, s75, 129024\n"
"ds_read_b128 a[232:235], v10 offset:4096\n"
"v_mfma_f32_16x16x32_bf16 v[100:103], a[132:135], a[236:239], v[100:103]\n"
"buffer_load_dwordx4 v3, s[48:51], s69 offen offset:2048 lds\n"
"v_mfma_f32_16x16x32_bf16 v[116:119], a[128:131], a[240:243], v[116:119]\n"
"ds_read_b128 a[236:239], v10 offset:4608\n"
"v_add_u32 v10, 0xffff0000, v10\n"
"v_mfma_f32_16x16x32_bf16 v[116:119], a[132:135], a[244:247], v[116:119]\n"
"v_mfma_f32_16x16x32_bf16 v[132:135], a[128:131], a[248:251], v[132:135]\n"
"v_mfma_f32_16x16x32_bf16 v[132:135], a[132:135], a[252:255], v[132:135]\n"
"s_add_u32 s70, s75, 193536\n"
"v_mfma_f32_16x16x32_bf16 v[204:207], a[128:131], v[232:235], v[204:207]\n"
"buffer_load_dwordx4 v3, s[48:51], s70 offen offset:3072 lds\n"
"v_mfma_f32_16x16x32_bf16 v[204:207], a[132:135], v[236:239], v[204:207]\n"
"s_waitcnt lgkmcnt(3)\n"
"v_mfma_f32_16x16x32_bf16 v[136:139], a[72:75], a[0:3], v[136:139]\n"
"v_mfma_f32_16x16x32_bf16 v[136:139], a[76:79], a[4:7], v[136:139]\n"
"v_mfma_f32_16x16x32_bf16 v[140:143], a[72:75], a[8:11], v[140:143]\n"
"v_mfma_f32_16x16x32_bf16 v[140:143], a[76:79], a[12:15], v[140:143]\n"
"v_mfma_f32_16x16x32_bf16 v[144:147], a[72:75], a[16:19], v[144:147]\n"
"v_mfma_f32_16x16x32_bf16 v[144:147], a[76:79], a[20:23], v[144:147]\n"
"v_mfma_f32_16x16x32_bf16 v[148:151], a[72:75], a[24:27], v[148:151]\n"
"v_mfma_f32_16x16x32_bf16 v[148:151], a[76:79], a[28:31], v[148:151]\n"
"v_mfma_f32_16x16x32_bf16 v[152:155], a[72:75], a[32:35], v[152:155]\n"
"v_mfma_f32_16x16x32_bf16 v[152:155], a[76:79], a[36:39], v[152:155]\n"
"v_mfma_f32_16x16x32_bf16 v[156:159], a[72:75], a[40:43], v[156:159]\n"
"v_mfma_f32_16x16x32_bf16 v[156:159], a[76:79], a[44:47], v[156:159]\n"
"v_mfma_f32_16x16x32_bf16 v[16:19], a[80:83], a[0:3], v[16:19]\n"
"v_mfma_f32_16x16x32_bf16 v[16:19], a[84:87], a[4:7], v[16:19]\n"
"v_mfma_f32_16x16x32_bf16 v[20:23], a[80:83], a[8:11], v[20:23]\n"
"v_mfma_f32_16x16x32_bf16 v[20:23], a[84:87], a[12:15], v[20:23]\n"
"v_mfma_f32_16x16x32_bf16 v[24:27], a[80:83], a[16:19], v[24:27]\n"
"v_mfma_f32_16x16x32_bf16 v[24:27], a[84:87], a[20:23], v[24:27]\n"
"v_mfma_f32_16x16x32_bf16 v[28:31], a[80:83], a[24:27], v[28:31]\n"
"v_mfma_f32_16x16x32_bf16 v[28:31], a[84:87], a[28:31], v[28:31]\n"
"v_mfma_f32_16x16x32_bf16 v[32:35], a[80:83], a[32:35], v[32:35]\n"
"v_mfma_f32_16x16x32_bf16 v[32:35], a[84:87], a[36:39], v[32:35]\n"
"s_waitcnt vmcnt(4) lgkmcnt(0)\n"
"s_barrier\n"
"v_mfma_f32_16x16x32_bf16 v[36:39], a[80:83], a[40:43], v[36:39]\n"
"ds_read_b64_tr_b16 a[104:105], v9 offset:0\n"
"ds_read_b64_tr_b16 a[106:107], v9 offset:512\n"
"v_mfma_f32_16x16x32_bf16 v[36:39], a[84:87], a[44:47], v[36:39]\n"
"ds_read_b64_tr_b16 a[108:109], v9 offset:4096\n"
"ds_read_b64_tr_b16 a[110:111], v9 offset:4608\n"
"v_mfma_f32_16x16x32_bf16 v[160:163], a[88:91], a[0:3], v[160:163]\n"
"ds_read_b64_tr_b16 a[112:113], v9 offset:8\n"
"ds_read_b64_tr_b16 a[114:115], v9 offset:520\n"
"v_mfma_f32_16x16x32_bf16 v[160:163], a[92:95], a[4:7], v[160:163]\n"
"ds_read_b64_tr_b16 a[116:117], v9 offset:4104\n"
"ds_read_b64_tr_b16 a[118:119], v9 offset:4616\n"
"v_mfma_f32_16x16x32_bf16 v[164:167], a[88:91], a[8:11], v[164:167]\n"
"ds_read_b64_tr_b16 a[120:121], v9 offset:256\n"
"ds_read_b64_tr_b16 a[122:123], v9 offset:768\n"
"v_mfma_f32_16x16x32_bf16 v[164:167], a[92:95], a[12:15], v[164:167]\n"
"ds_read_b64_tr_b16 a[124:125], v9 offset:4352\n"
"ds_read_b64_tr_b16 a[126:127], v9 offset:4864\n"
"v_mfma_f32_16x16x32_bf16 v[168:171], a[88:91], a[16:19], v[168:171]\n"
"ds_read_b128 a[240:243], v10 offset:0\n"
"ds_read_b128 a[244:247], v10 offset:512\n"
"v_mfma_f32_16x16x32_bf16 v[168:171], a[92:95], a[20:23], v[168:171]\n"
"s_add_u32 s75, s58, s83\n"
"s_add_u32 m0, s60, 81920\n"
"v_mfma_f32_16x16x32_bf16 v[172:175], a[88:91], a[24:27], v[172:175]\n"
"s_add_u32 s71, s75, 128\n"
"ds_read_b128 a[248:251], v10 offset:2048\n"
"v_mfma_f32_16x16x32_bf16 v[172:175], a[92:95], a[28:31], v[172:175]\n"
"buffer_load_dwordx4 v3, s[48:51], s71 offen offset:0 lds\n"
"v_mfma_f32_16x16x32_bf16 v[176:179], a[88:91], a[32:35], v[176:179]\n"
"ds_read_b128 a[252:255], v10 offset:2560\n"
"ds_read_b128 v[232:235], v10 offset:4096\n"
"v_mfma_f32_16x16x32_bf16 v[176:179], a[92:95], a[36:39], v[176:179]\n"
"ds_read_b128 v[236:239], v10 offset:4608\n"
"ds_read_b64_tr_b16 a[128:129], v9 offset:264\n"
"v_mfma_f32_16x16x32_bf16 v[180:183], a[88:91], a[40:43], v[180:183]\n"
"s_add_u32 s64, s75, 64640\n"
"ds_read_b64_tr_b16 a[130:131], v9 offset:776\n"
"v_mfma_f32_16x16x32_bf16 v[180:183], a[92:95], a[44:47], v[180:183]\n"
"buffer_load_dwordx4 v3, s[48:51], s64 offen offset:1024 lds\n"
"v_mfma_f32_16x16x32_bf16 v[40:43], a[96:99], a[0:3], v[40:43]\n"
"ds_read_b64_tr_b16 a[132:133], v9 offset:4360\n"
"ds_read_b64_tr_b16 a[134:135], v9 offset:4872\n"
"v_mfma_f32_16x16x32_bf16 v[40:43], a[100:103], a[4:7], v[40:43]\n"
"s_add_u32 s83, s83, 524288\n"
"s_cmp_eq_u32 s83, 4718592\n"
"s_cselect_b32 s83, 0, s83\n"
"v_mfma_f32_16x16x32_bf16 v[44:47], a[96:99], a[8:11], v[44:47]\n"
"v_add_u32 v10, 0x10000, v10\n"
"v_mfma_f32_16x16x32_bf16 v[44:47], a[100:103], a[12:15], v[44:47]\n"
"v_mfma_f32_16x16x32_bf16 v[48:51], a[96:99], a[16:19], v[48:51]\n"
"s_add_u32 s65, s75, 129152\n"
"v_mfma_f32_16x16x32_bf16 v[48:51], a[100:103], a[20:23], v[48:51]\n"
"buffer_load_dwordx4 v3, s[48:51], s65 offen offset:2048 lds\n"
"v_mfma_f32_16x16x32_bf16 v[52:55], a[96:99], a[24:27], v[52:55]\n"
"v_mfma_f32_16x16x32_bf16 v[52:55], a[100:103], a[28:31], v[52:55]\n"
"v_mfma_f32_16x16x32_bf16 v[56:59], a[96:99], a[32:35], v[56:59]\n"
"v_mfma_f32_16x16x32_bf16 v[56:59], a[100:103], a[36:39], v[56:59]\n"
"s_add_u32 s66, s75, 193664\n"
"v_mfma_f32_16x16x32_bf16 v[60:63], a[96:99], a[40:43], v[60:63]\n"
"buffer_load_dwordx4 v3, s[48:51], s66 offen offset:3072 lds\n"
"v_mfma_f32_16x16x32_bf16 v[60:63], a[100:103], a[44:47], v[60:63]\n"
"s_waitcnt lgkmcnt(0)\n"
"v_mfma_f32_16x16x32_bf16 v[184:187], a[104:107], a[0:3], v[184:187]\n"
"s_cmp_eq_u32 s62, 1\n"
"s_cselect_b32 s50, 0, s50\n"
"v_mfma_f32_16x16x32_bf16 v[184:187], a[108:111], a[4:7], v[184:187]\n"
"v_mfma_f32_16x16x32_bf16 v[72:75], a[104:107], a[8:11], v[72:75]\n"
"v_mfma_f32_16x16x32_bf16 v[72:75], a[108:111], a[12:15], v[72:75]\n"
"v_mfma_f32_16x16x32_bf16 v[88:91], a[104:107], a[16:19], v[88:91]\n"
"v_mfma_f32_16x16x32_bf16 v[88:91], a[108:111], a[20:23], v[88:91]\n"
"v_mfma_f32_16x16x32_bf16 v[104:107], a[104:107], a[24:27], v[104:107]\n"
"v_mfma_f32_16x16x32_bf16 v[104:107], a[108:111], a[28:31], v[104:107]\n"
"v_mfma_f32_16x16x32_bf16 v[120:123], a[104:107], a[32:35], v[120:123]\n"
"v_mfma_f32_16x16x32_bf16 v[120:123], a[108:111], a[36:39], v[120:123]\n"
"v_mfma_f32_16x16x32_bf16 v[192:195], a[104:107], a[40:43], v[192:195]\n"
"v_mfma_f32_16x16x32_bf16 v[192:195], a[108:111], a[44:47], v[192:195]\n"
"v_mfma_f32_16x16x32_bf16 v[64:67], a[112:115], a[0:3], v[64:67]\n"
"v_mfma_f32_16x16x32_bf16 v[64:67], a[116:119], a[4:7], v[64:67]\n"
"v_mfma_f32_16x16x32_bf16 v[76:79], a[112:115], a[8:11], v[76:79]\n"
"v_mfma_f32_16x16x32_bf16 v[76:79], a[116:119], a[12:15], v[76:79]\n"
"v_mfma_f32_16x16x32_bf16 v[92:95], a[112:115], a[16:19], v[92:95]\n"
"v_mfma_f32_16x16x32_bf16 v[92:95], a[116:119], a[20:23], v[92:95]\n"
"v_mfma_f32_16x16x32_bf16 v[108:111], a[112:115], a[24:27], v[108:111]\n"
"v_mfma_f32_16x16x32_bf16 v[108:111], a[116:119], a[28:31], v[108:111]\n"
"v_mfma_f32_16x16x32_bf16 v[124:127], a[112:115], a[32:35], v[124:127]\n"
"v_mfma_f32_16x16x32_bf16 v[124:127], a[116:119], a[36:39], v[124:127]\n"
"s_waitcnt vmcnt(4)\n"
"s_barrier\n"
"v_mfma_f32_16x16x32_bf16 v[196:199], a[112:115], a[40:43], v[196:199]\n"
"ds_read_b64_tr_b16 a[72:73], v9 offset:32768\n"
"ds_read_b64_tr_b16 a[74:75], v9 offset:33280\n"
"v_mfma_f32_16x16x32_bf16 v[196:199], a[116:119], a[44:47], v[196:199]\n"
"ds_read_b64_tr_b16 a[76:77], v9 offset:36864\n"
"ds_read_b64_tr_b16 a[78:79], v9 offset:37376\n"
"v_mfma_f32_16x16x32_bf16 v[188:191], a[120:123], a[0:3], v[188:191]\n"
"ds_read_b64_tr_b16 a[80:81], v9 offset:32776\n"
"ds_read_b64_tr_b16 a[82:83], v9 offset:33288\n"
"v_mfma_f32_16x16x32_bf16 v[188:191], a[124:127], a[4:7], v[188:191]\n"
"ds_read_b64_tr_b16 a[84:85], v9 offset:36872\n"
"ds_read_b64_tr_b16 a[86:87], v9 offset:37384\n"
"v_mfma_f32_16x16x32_bf16 v[80:83], a[120:123], a[8:11], v[80:83]\n"
"ds_read_b64_tr_b16 a[88:89], v9 offset:33024\n"
"ds_read_b64_tr_b16 a[90:91], v9 offset:33536\n"
"v_mfma_f32_16x16x32_bf16 v[80:83], a[124:127], a[12:15], v[80:83]\n"
"ds_read_b64_tr_b16 a[92:93], v9 offset:37120\n"
"ds_read_b64_tr_b16 a[94:95], v9 offset:37632\n"
"v_mfma_f32_16x16x32_bf16 v[96:99], a[120:123], a[16:19], v[96:99]\n"
"ds_read_b64_tr_b16 a[96:97], v9 offset:33032\n"
"ds_read_b64_tr_b16 a[98:99], v9 offset:33544\n"
"v_mfma_f32_16x16x32_bf16 v[96:99], a[124:127], a[20:23], v[96:99]\n"
"s_add_u32 m0, s60, 16384\n"
"s_add_u32 s67, s56, 8192\n"
"v_mfma_f32_16x16x32_bf16 v[112:115], a[120:123], a[24:27], v[112:115]\n"
"buffer_load_dwordx4 v1, s[40:43], s67 offen offset:0 nt lds\n"
"v_mfma_f32_16x16x32_bf16 v[112:115], a[124:127], a[28:31], v[112:115]\n"
"ds_read_b64_tr_b16 a[100:101], v9 offset:37128\n"
"ds_read_b64_tr_b16 a[102:103], v9 offset:37640\n"
"v_mfma_f32_16x16x32_bf16 v[128:131], a[120:123], a[32:35], v[128:131]\n"
"v_add_u32 v9, 0x10000, v9\n"
"v_mfma_f32_16x16x32_bf16 v[128:131], a[124:127], a[36:39], v[128:131]\n"
"v_mfma_f32_16x16x32_bf16 v[200:203], a[120:123], a[40:43], v[200:203]\n"
"v_mfma_f32_16x16x32_bf16 v[200:203], a[124:127], a[44:47], v[200:203]\n"
"v_mfma_f32_16x16x32_bf16 v[68:71], a[128:131], a[0:3], v[68:71]\n"
"v_mfma_f32_16x16x32_bf16 v[68:71], a[132:135], a[4:7], v[68:71]\n"
"v_mfma_f32_16x16x32_bf16 v[84:87], a[128:131], a[8:11], v[84:87]\n"
"v_mfma_f32_16x16x32_bf16 v[84:87], a[132:135], a[12:15], v[84:87]\n"
"v_mfma_f32_16x16x32_bf16 v[100:103], a[128:131], a[16:19], v[100:103]\n"
"v_mfma_f32_16x16x32_bf16 v[100:103], a[132:135], a[20:23], v[100:103]\n"
"v_mfma_f32_16x16x32_bf16 v[116:119], a[128:131], a[24:27], v[116:119]\n"
"v_mfma_f32_16x16x32_bf16 v[116:119], a[132:135], a[28:31], v[116:119]\n"
"v_mfma_f32_16x16x32_bf16 v[132:135], a[128:131], a[32:35], v[132:135]\n"
"v_mfma_f32_16x16x32_bf16 v[132:135], a[132:135], a[36:39], v[132:135]\n"
"v_mfma_f32_16x16x32_bf16 v[204:207], a[128:131], a[40:43], v[204:207]\n"
"buffer_load_dwordx4 v2, s[40:43], s67 offen offset:1024 nt lds\n"
"v_mfma_f32_16x16x32_bf16 v[204:207], a[132:135], a[44:47], v[204:207]\n"
"s_waitcnt lgkmcnt(0)\n"
"v_mfma_f32_16x16x32_bf16 v[136:139], a[72:75], a[216:219], v[136:139]\n"
"v_mfma_f32_16x16x32_bf16 v[136:139], a[76:79], a[220:223], v[136:139]\n"
"v_mfma_f32_16x16x32_bf16 v[140:143], a[72:75], a[224:227], v[140:143]\n"
"v_mfma_f32_16x16x32_bf16 v[140:143], a[76:79], a[228:231], v[140:143]\n"
"v_mfma_f32_16x16x32_bf16 v[144:147], a[72:75], a[232:235], v[144:147]\n"
"s_nop 4\n"
"v_mul_f32 v136, s79, v136\n"
"v_mul_f32 v137, s79, v137\n"
"v_mfma_f32_16x16x32_bf16 v[144:147], a[76:79], a[236:239], v[144:147]\n"
"v_mul_f32 v138, s79, v138\n"
"v_mul_f32 v139, s79, v139\n"
"v_mfma_f32_16x16x32_bf16 v[148:151], a[72:75], a[240:243], v[148:151]\n"
"v_cvt_pk_bf16_f32 v136, v136, v137\n"
"v_cvt_pk_bf16_f32 v137, v138, v139\n"
"v_mfma_f32_16x16x32_bf16 v[148:151], a[76:79], a[244:247], v[148:151]\n"
"v_mul_f32 v140, s79, v140\n"
"v_mul_f32 v141, s79, v141\n"
"v_mfma_f32_16x16x32_bf16 v[152:155], a[72:75], a[248:251], v[152:155]\n"
"v_mul_f32 v142, s79, v142\n"
"v_mul_f32 v143, s79, v143\n"
"v_mfma_f32_16x16x32_bf16 v[152:155], a[76:79], a[252:255], v[152:155]\n"
"v_cvt_pk_bf16_f32 v140, v140, v141\n"
"v_cvt_pk_bf16_f32 v141, v142, v143\n"
"v_mfma_f32_16x16x32_bf16 v[156:159], a[72:75], v[232:235], v[156:159]\n"
"v_mul_f32 v144, s79, v144\n"
"v_mul_f32 v145, s79, v145\n"
"v_mfma_f32_16x16x32_bf16 v[156:159], a[76:79], v[236:239], v[156:159]\n"
"v_mul_f32 v146, s79, v146\n"
"v_mul_f32 v147, s79, v147\n"
"v_mfma_f32_16x16x32_bf16 v[16:19], a[80:83], a[216:219], v[16:19]\n"
"v_cvt_pk_bf16_f32 v144, v144, v145\n"
"v_cvt_pk_bf16_f32 v145, v146, v147\n"
"v_mfma_f32_16x16x32_bf16 v[16:19], a[84:87], a[220:223], v[16:19]\n"
"v_mul_f32 v148, s79, v148\n"
"v_mul_f32 v149, s79, v149\n"
"v_mfma_f32_16x16x32_bf16 v[20:23], a[80:83], a[224:227], v[20:23]\n"
"v_mul_f32 v150, s79, v150\n"
"v_mul_f32 v151, s79, v151\n"
"v_mfma_f32_16x16x32_bf16 v[20:23], a[84:87], a[228:231], v[20:23]\n"
"buffer_load_dwordx4 v1, s[40:43], s67 offen offset:2048 nt lds\n"
"v_mfma_f32_16x16x32_bf16 v[24:27], a[80:83], a[232:235], v[24:27]\n"
"v_mul_f32 v16, s79, v16\n"
"v_mul_f32 v17, s79, v17\n"
"v_mfma_f32_16x16x32_bf16 v[24:27], a[84:87], a[236:239], v[24:27]\n"
"v_mul_f32 v18, s79, v18\n"
"v_mul_f32 v19, s79, v19\n"
"v_mfma_f32_16x16x32_bf16 v[28:31], a[80:83], a[240:243], v[28:31]\n"
"v_cvt_pk_bf16_f32 v138, v16, v17\n"
"v_cvt_pk_bf16_f32 v139, v18, v19\n"
"v_mfma_f32_16x16x32_bf16 v[28:31], a[84:87], a[244:247], v[28:31]\n"
"v_mul_f32 v20, s79, v20\n"
"v_mul_f32 v21, s79, v21\n"
"v_mfma_f32_16x16x32_bf16 v[32:35], a[80:83], a[248:251], v[32:35]\n"
"v_mul_f32 v22, s79, v22\n"
"v_mul_f32 v23, s79, v23\n"
"v_mfma_f32_16x16x32_bf16 v[32:35], a[84:87], a[252:255], v[32:35]\n"
"s_waitcnt vmcnt(3)\n"
"s_barrier\n"
"v_mfma_f32_16x16x32_bf16 v[36:39], a[80:83], v[232:235], v[36:39]\n"
"ds_read_b64_tr_b16 a[104:105], v9 offset:0\n"
"ds_read_b64_tr_b16 a[106:107], v9 offset:512\n"
"v_mfma_f32_16x16x32_bf16 v[36:39], a[84:87], v[236:239], v[36:39]\n"
"ds_read_b64_tr_b16 a[108:109], v9 offset:4096\n"
"ds_read_b64_tr_b16 a[110:111], v9 offset:4608\n"
"v_mfma_f32_16x16x32_bf16 v[160:163], a[88:91], a[216:219], v[160:163]\n"
"ds_read_b64_tr_b16 a[112:113], v9 offset:8\n"
"ds_read_b64_tr_b16 a[114:115], v9 offset:520\n"
"v_mfma_f32_16x16x32_bf16 v[160:163], a[92:95], a[220:223], v[160:163]\n"
"ds_read_b64_tr_b16 a[116:117], v9 offset:4104\n"
"ds_read_b64_tr_b16 a[118:119], v9 offset:4616\n"
"v_mfma_f32_16x16x32_bf16 v[164:167], a[88:91], a[224:227], v[164:167]\n"
"ds_read_b64_tr_b16 a[120:121], v9 offset:256\n"
"ds_read_b64_tr_b16 a[122:123], v9 offset:768\n"
"v_mfma_f32_16x16x32_bf16 v[164:167], a[92:95], a[228:231], v[164:167]\n"
"ds_read_b64_tr_b16 a[124:125], v9 offset:4352\n"
"ds_read_b64_tr_b16 a[126:127], v9 offset:4864\n"
"v_mfma_f32_16x16x32_bf16 v[168:171], a[88:91], a[232:235], v[168:171]\n"
"ds_read_b64_tr_b16 a[128:129], v9 offset:264\n"
"ds_read_b64_tr_b16 a[130:131], v9 offset:776\n"
"v_mfma_f32_16x16x32_bf16 v[168:171], a[92:95], a[236:239], v[168:171]\n"
"ds_read_b64_tr_b16 a[132:133], v9 offset:4360\n"
"ds_read_b64_tr_b16 a[134:135], v9 offset:4872\n"
"v_mfma_f32_16x16x32_bf16 v[172:175], a[88:91], a[240:243], v[172:175]\n"
"ds_read_b128 a[0:3], v6 offset:32768\n"
"ds_read_b128 a[4:7], v6 offset:33024\n"
"v_mfma_f32_16x16x32_bf16 v[172:175], a[92:95], a[244:247], v[172:175]\n"
"ds_read_b128 a[8:11], v6 offset:33280\n"
"ds_read_b128 a[12:15], v6 offset:33536\n"
"v_mfma_f32_16x16x32_bf16 v[176:179], a[88:91], a[248:251], v[176:179]\n"
"buffer_load_dwordx4 v2, s[40:43], s67 offen offset:3072 nt lds\n"
"v_mfma_f32_16x16x32_bf16 v[176:179], a[92:95], a[252:255], v[176:179]\n"
"ds_read_b128 a[16:19], v6 offset:36864\n"
"ds_read_b128 a[20:23], v6 offset:37120\n"
"v_mfma_f32_16x16x32_bf16 v[180:183], a[88:91], v[232:235], v[180:183]\n"
"ds_read_b128 a[24:27], v6 offset:37376\n"
"ds_read_b128 a[28:31], v6 offset:37632\n"
"v_mfma_f32_16x16x32_bf16 v[180:183], a[92:95], v[236:239], v[180:183]\n"
"v_mul_f32 v160, s79, v160\n"
"v_mul_f32 v161, s79, v161\n"
"v_mfma_f32_16x16x32_bf16 v[40:43], a[96:99], a[216:219], v[40:43]\n"
"v_mul_f32 v162, s79, v162\n"
"v_mul_f32 v163, s79, v163\n"
"v_mfma_f32_16x16x32_bf16 v[40:43], a[100:103], a[220:223], v[40:43]\n"
"v_cvt_pk_bf16_f32 v160, v160, v161\n"
"v_cvt_pk_bf16_f32 v161, v162, v163\n"
"v_mfma_f32_16x16x32_bf16 v[44:47], a[96:99], a[224:227], v[44:47]\n"
"v_cvt_pk_bf16_f32 v142, v20, v21\n"
"v_cvt_pk_bf16_f32 v143, v22, v23\n"
"v_mfma_f32_16x16x32_bf16 v[44:47], a[100:103], a[228:231], v[44:47]\n"
"v_mul_f32 v164, s79, v164\n"
"v_mul_f32 v165, s79, v165\n"
"v_mfma_f32_16x16x32_bf16 v[48:51], a[96:99], a[232:235], v[48:51]\n"
"v_mul_f32 v40, s79, v40\n"
"v_mul_f32 v41, s79, v41\n"
"v_mfma_f32_16x16x32_bf16 v[48:51], a[100:103], a[236:239], v[48:51]\n"
"v_mul_f32 v42, s79, v42\n"
"v_mul_f32 v43, s79, v43\n"
"v_mfma_f32_16x16x32_bf16 v[52:55], a[96:99], a[240:243], v[52:55]\n"
"v_cvt_pk_bf16_f32 v162, v40, v41\n"
"v_cvt_pk_bf16_f32 v163, v42, v43\n"
"v_mfma_f32_16x16x32_bf16 v[52:55], a[100:103], a[244:247], v[52:55]\n"
"v_mul_f32 v24, s79, v24\n"
"v_mul_f32 v25, s79, v25\n"
"v_mfma_f32_16x16x32_bf16 v[56:59], a[96:99], a[248:251], v[56:59]\n"
"v_mul_f32 v26, s79, v26\n"
"v_mul_f32 v27, s79, v27\n"
"v_mfma_f32_16x16x32_bf16 v[56:59], a[100:103], a[252:255], v[56:59]\n"
"v_mul_f32 v166, s79, v166\n"
"v_mul_f32 v167, s79, v167\n"
"v_mfma_f32_16x16x32_bf16 v[60:63], a[96:99], v[232:235], v[60:63]\n"
"v_cvt_pk_bf16_f32 v164, v164, v165\n"
"v_cvt_pk_bf16_f32 v146, v24, v25\n"
"v_mfma_f32_16x16x32_bf16 v[60:63], a[100:103], v[236:239], v[60:63]\n"
"v_cvt_pk_bf16_f32 v147, v26, v27\n"
"s_waitcnt lgkmcnt(0)\n"
"v_mfma_f32_16x16x32_bf16 v[184:187], a[104:107], a[216:219], v[184:187]\n"
"v_mul_f32 v44, s79, v44\n"
"v_mul_f32 v45, s79, v45\n"
"v_mfma_f32_16x16x32_bf16 v[184:187], a[108:111], a[220:223], v[184:187]\n"
"s_add_u32 m0, s60, 0\n"
"s_add_u32 s68, s56, 24576\n"
"v_mfma_f32_16x16x32_bf16 v[64:67], a[112:115], a[216:219], v[64:67]\n"
"buffer_load_dwordx4 v1, s[36:39], s68 offen offset:0 nt lds\n"
"v_mfma_f32_16x16x32_bf16 v[64:67], a[116:119], a[220:223], v[64:67]\n"
"v_mul_f32 v46, s79, v46\n"
"v_mul_f32 v47, s79, v47\n"
"v_mfma_f32_16x16x32_bf16 v[188:191], a[120:123], a[216:219], v[188:191]\n"
"v_mul_f32 v184, s79, v184\n"
"v_mul_f32 v185, s79, v185\n"
"v_mfma_f32_16x16x32_bf16 v[188:191], a[124:127], a[220:223], v[188:191]\n"
"v_mul_f32 v186, s79, v186\n"
"v_mul_f32 v187, s79, v187\n"
"v_mfma_f32_16x16x32_bf16 v[68:71], a[128:131], a[216:219], v[68:71]\n"
"v_cvt_pk_bf16_f32 v184, v184, v185\n"
"v_mul_f32 v64, s79, v64\n"
"v_mfma_f32_16x16x32_bf16 v[68:71], a[132:135], a[220:223], v[68:71]\n"
"v_mul_f32 v65, s79, v65\n"
"v_mul_f32 v66, s79, v66\n"
"v_mfma_f32_16x16x32_bf16 v[72:75], a[104:107], a[224:227], v[72:75]\n"
"v_mul_f32 v67, s79, v67\n"
"v_cvt_pk_bf16_f32 v185, v186, v187\n"
"v_mfma_f32_16x16x32_bf16 v[72:75], a[108:111], a[228:231], v[72:75]\n"
"v_cvt_pk_bf16_f32 v186, v64, v65\n"
"v_cvt_pk_bf16_f32 v187, v66, v67\n"
"v_mfma_f32_16x16x32_bf16 v[76:79], a[112:115], a[224:227], v[76:79]\n"
"v_mul_f32 v188, s79, v188\n"
"v_mul_f32 v189, s79, v189\n"
"v_mfma_f32_16x16x32_bf16 v[76:79], a[116:119], a[228:231], v[76:79]\n"
"v_mul_f32 v190, s79, v190\n"
"v_mul_f32 v191, s79, v191\n"
"v_mfma_f32_16x16x32_bf16 v[80:83], a[120:123], a[224:227], v[80:83]\n"
"v_cvt_pk_bf16_f32 v188, v188, v189\n"
"v_mul_f32 v68, s79, v68\n"
"v_mfma_f32_16x16x32_bf16 v[80:83], a[124:127], a[228:231], v[80:83]\n"
"v_mul_f32 v69, s79, v69\n"
"v_mul_f32 v70, s79, v70\n"
"v_mfma_f32_16x16x32_bf16 v[84:87], a[128:131], a[224:227], v[84:87]\n"
"v_mul_f32 v71, s79, v71\n"
"v_cvt_pk_bf16_f32 v189, v190, v191\n"
"v_mfma_f32_16x16x32_bf16 v[84:87], a[132:135], a[228:231], v[84:87]\n"
"v_cvt_pk_bf16_f32 v190, v68, v69\n"
"v_cvt_pk_bf16_f32 v191, v70, v71\n"
"v_mfma_f32_16x16x32_bf16 v[88:91], a[104:107], a[232:235], v[88:91]\n"
"v_cvt_pk_bf16_f32 v165, v166, v167\n"
"v_cvt_pk_bf16_f32 v166, v44, v45\n"
"v_mfma_f32_16x16x32_bf16 v[88:91], a[108:111], a[236:239], v[88:91]\n"
"v_cvt_pk_bf16_f32 v167, v46, v47\n"
"v_mul_f32 v72, s79, v72\n"
"v_mfma_f32_16x16x32_bf16 v[92:95], a[112:115], a[232:235], v[92:95]\n"
"buffer_load_dwordx4 v2, s[36:39], s68 offen offset:1024 nt lds\n"
"v_mfma_f32_16x16x32_bf16 v[92:95], a[116:119], a[236:239], v[92:95]\n"
"v_mul_f32 v73, s79, v73\n"
"v_mul_f32 v74, s79, v74\n"
"v_mfma_f32_16x16x32_bf16 v[96:99], a[120:123], a[232:235], v[96:99]\n"
"v_mul_f32 v75, s79, v75\n"
"v_cvt_pk_bf16_f32 v72, v72, v73\n"
"v_mfma_f32_16x16x32_bf16 v[96:99], a[124:127], a[236:239], v[96:99]\n"
"v_mul_f32 v76, s79, v76\n"
"v_mul_f32 v77, s79, v77\n"
"v_mfma_f32_16x16x32_bf16 v[100:103], a[128:131], a[232:235], v[100:103]\n"
"v_mul_f32 v78, s79, v78\n"
"v_mul_f32 v79, s79, v79\n"
"v_mfma_f32_16x16x32_bf16 v[100:103], a[132:135], a[236:239], v[100:103]\n"
"v_cvt_pk_bf16_f32 v73, v74, v75\n"
"v_cvt_pk_bf16_f32 v74, v76, v77\n"
"v_mfma_f32_16x16x32_bf16 v[104:107], a[104:107], a[240:243], v[104:107]\n"
"v_cvt_pk_bf16_f32 v75, v78, v79\n"
"v_accvgpr_write_b32 a216, v72\n"
"v_mfma_f32_16x16x32_bf16 v[104:107], a[108:111], a[244:247], v[104:107]\n"
"v_accvgpr_write_b32 a217, v73\n"
"v_accvgpr_write_b32 a218, v74\n"
"v_mfma_f32_16x16x32_bf16 v[108:111], a[112:115], a[240:243], v[108:111]\n"
"v_accvgpr_write_b32 a219, v75\n"
"v_mul_f32 v80, s79, v80\n"
"v_mfma_f32_16x16x32_bf16 v[108:111], a[116:119], a[244:247], v[108:111]\n"
"v_mul_f32 v81, s79, v81\n"
"v_mul_f32 v82, s79, v82\n"
"v_mfma_f32_16x16x32_bf16 v[112:115], a[120:123], a[240:243], v[112:115]\n"
"v_mul_f32 v83, s79, v83\n"
"v_cvt_pk_bf16_f32 v80, v80, v81\n"
"v_mfma_f32_16x16x32_bf16 v[112:115], a[124:127], a[244:247], v[112:115]\n"
"s_barrier\n"
"v_mfma_f32_16x16x32_bf16 v[116:119], a[128:131], a[240:243], v[116:119]\n"
"v_mul_f32 v84, s79, v84\n"
"v_mul_f32 v85, s79, v85\n"
"v_mfma_f32_16x16x32_bf16 v[116:119], a[132:135], a[244:247], v[116:119]\n"
"v_mul_f32 v86, s79, v86\n"
"v_mul_f32 v87, s79, v87\n"
"v_mfma_f32_16x16x32_bf16 v[120:123], a[104:107], a[248:251], v[120:123]\n"
"v_cvt_pk_bf16_f32 v81, v82, v83\n"
"v_cvt_pk_bf16_f32 v82, v84, v85\n"
"v_mfma_f32_16x16x32_bf16 v[120:123], a[108:111], a[252:255], v[120:123]\n"
"v_cvt_pk_bf16_f32 v83, v86, v87\n"
"v_accvgpr_write_b32 a236, v80\n"
"v_mfma_f32_16x16x32_bf16 v[124:127], a[112:115], a[248:251], v[124:127]\n"
"v_accvgpr_write_b32 a237, v81\n"
"v_accvgpr_write_b32 a238, v82\n"
"v_mfma_f32_16x16x32_bf16 v[124:127], a[116:119], a[252:255], v[124:127]\n"
"buffer_load_dwordx4 v1, s[36:39], s68 offen offset:2048 nt lds\n"
"v_mfma_f32_16x16x32_bf16 v[128:131], a[120:123], a[248:251], v[128:131]\n"
"v_accvgpr_write_b32 a239, v83\n"
"v_cvt_pk_bf16_f32 v148, v148, v149\n"
"v_mfma_f32_16x16x32_bf16 v[128:131], a[124:127], a[252:255], v[128:131]\n"
"v_mul_f32 v28, s79, v28\n"
"v_mul_f32 v29, s79, v29\n"
"v_mfma_f32_16x16x32_bf16 v[132:135], a[128:131], a[248:251], v[132:135]\n"
"v_mul_f32 v30, s79, v30\n"
"v_mul_f32 v31, s79, v31\n"
"v_mfma_f32_16x16x32_bf16 v[132:135], a[132:135], a[252:255], v[132:135]\n"
"v_cvt_pk_bf16_f32 v149, v150, v151\n"
"v_cvt_pk_bf16_f32 v150, v28, v29\n"
"v_mfma_f32_16x16x32_bf16 v[192:195], a[104:107], v[232:235], v[192:195]\n"
"v_cvt_pk_bf16_f32 v151, v30, v31\n"
"v_mul_f32 v152, s79, v152\n"
"v_mfma_f32_16x16x32_bf16 v[192:195], a[108:111], v[236:239], v[192:195]\n"
"v_mul_f32 v153, s79, v153\n"
"v_mul_f32 v154, s79, v154\n"
"v_mfma_f32_16x16x32_bf16 v[196:199], a[112:115], v[232:235], v[196:199]\n"
"v_mul_f32 v155, s79, v155\n"
"v_cvt_pk_bf16_f32 v152, v152, v153\n"
"v_mfma_f32_16x16x32_bf16 v[196:199], a[116:119], v[236:239], v[196:199]\n"
"v_mul_f32 v168, s79, v168\n"
"v_mul_f32 v169, s79, v169\n"
"v_mfma_f32_16x16x32_bf16 v[200:203], a[120:123], v[232:235], v[200:203]\n"
"v_mul_f32 v32, s79, v32\n"
"v_mul_f32 v33, s79, v33\n"
"v_mfma_f32_16x16x32_bf16 v[200:203], a[124:127], v[236:239], v[200:203]\n"
"v_mul_f32 v34, s79, v34\n"
"v_mul_f32 v35, s79, v35\n"
"v_mfma_f32_16x16x32_bf16 v[204:207], a[128:131], v[232:235], v[204:207]\n"
"v_cvt_pk_bf16_f32 v153, v154, v155\n"
"v_mul_f32 v170, s79, v170\n"
"v_mfma_f32_16x16x32_bf16 v[204:207], a[132:135], v[236:239], v[204:207]\n"
"v_mul_f32 v171, s79, v171\n"
"v_cvt_pk_bf16_f32 v168, v168, v169\n"
"v_mfma_f32_16x16x32_bf16 v[16:19], a[0:3], v[136:139], 0\n"
"v_cvt_pk_bf16_f32 v154, v32, v33\n"
"v_cvt_pk_bf16_f32 v155, v34, v35\n"
"v_mfma_f32_16x16x32_bf16 v[16:19], a[4:7], v[160:163], v[16:19]\n"
"v_mul_f32 v48, s79, v48\n"
"v_mul_f32 v49, s79, v49\n"
"v_mfma_f32_16x16x32_bf16 v[16:19], a[8:11], v[184:187], v[16:19]\n"
"v_mul_f32 v50, s79, v50\n"
"v_mul_f32 v51, s79, v51\n"
"v_mfma_f32_16x16x32_bf16 v[16:19], a[12:15], v[188:191], v[16:19]\n"
"v_cvt_pk_bf16_f32 v169, v170, v171\n"
"v_cvt_pk_bf16_f32 v170, v48, v49\n"
"v_mfma_f32_16x16x32_bf16 v[20:23], a[16:19], v[136:139], 0\n"
"buffer_load_dwordx4 v2, s[36:39], s68 offen offset:3072 nt lds\n"
"v_mfma_f32_16x16x32_bf16 v[20:23], a[20:23], v[160:163], v[20:23]\n"
"v_cvt_pk_bf16_f32 v171, v50, v51\n"
"v_mul_f32 v88, s79, v88\n"
"v_mfma_f32_16x16x32_bf16 v[20:23], a[24:27], v[184:187], v[20:23]\n"
"v_mul_f32 v89, s79, v89\n"
"v_mul_f32 v90, s79, v90\n"
"v_mfma_f32_16x16x32_bf16 v[20:23], a[28:31], v[188:191], v[20:23]\n"
"v_mul_f32 v91, s79, v91\n"
"v_cvt_pk_bf16_f32 v88, v88, v89\n"
"v_mfma_f32_16x16x32_bf16 v[24:27], a[0:3], v[140:143], 0\n"
"v_mul_f32 v92, s79, v92\n"
"v_mul_f32 v93, s79, v93\n"
"v_mfma_f32_16x16x32_bf16 v[24:27], a[4:7], v[164:167], v[24:27]\n"
"v_mul_f32 v94, s79, v94\n"
"v_mul_f32 v95, s79, v95\n"
"v_mfma_f32_16x16x32_bf16 v[24:27], a[8:11], a[216:219], v[24:27]\n"
"v_cvt_pk_bf16_f32 v89, v90, v91\n"
"v_cvt_pk_bf16_f32 v90, v92, v93\n"
"v_mfma_f32_16x16x32_bf16 v[24:27], a[12:15], a[236:239], v[24:27]\n"
"v_cvt_pk_bf16_f32 v91, v94, v95\n"
"v_accvgpr_write_b32 a220, v88\n"
"v_mfma_f32_16x16x32_bf16 v[28:31], a[16:19], v[140:143], 0\n"
"v_accvgpr_write_b32 a221, v89\n"
"v_accvgpr_write_b32 a222, v90\n"
"v_mfma_f32_16x16x32_bf16 v[28:31], a[20:23], v[164:167], v[28:31]\n"
"v_accvgpr_write_b32 a223, v91\n"
"v_mul_f32 v96, s79, v96\n"
"v_mfma_f32_16x16x32_bf16 v[28:31], a[24:27], a[216:219], v[28:31]\n"
"v_mul_f32 v97, s79, v97\n"
"v_mul_f32 v98, s79, v98\n"
"v_mfma_f32_16x16x32_bf16 v[28:31], a[28:31], a[236:239], v[28:31]\n"
"v_mul_f32 v99, s79, v99\n"
"v_cvt_pk_bf16_f32 v96, v96, v97\n"
"v_mfma_f32_16x16x32_bf16 v[32:35], a[0:3], v[144:147], 0\n"
"v_mul_f32 v100, s79, v100\n"
"v_mul_f32 v101, s79, v101\n"
"v_mfma_f32_16x16x32_bf16 v[32:35], a[4:7], v[168:171], v[32:35]\n"
"v_mul_f32 v102, s79, v102\n"
"v_mul_f32 v103, s79, v103\n"
"v_mfma_f32_16x16x32_bf16 v[32:35], a[8:11], a[220:223], v[32:35]\n"
"v_cvt_pk_bf16_f32 v97, v98, v99\n"
"v_cvt_pk_bf16_f32 v98, v100, v101\n"
"v_cvt_pk_bf16_f32 v99, v102, v103\n"
"v_accvgpr_write_b32 a240, v96\n"
"v_accvgpr_write_b32 a241, v97\n"
"v_accvgpr_write_b32 a242, v98\n"
"v_accvgpr_write_b32 a243, v99\n"
"v_mul_f32 v156, s79, v156\n"
"v_mul_f32 v157, s79, v157\n"
"v_mfma_f32_16x16x32_bf16 v[32:35], a[12:15], a[240:243], v[32:35]\n"
"v_mul_f32 v158, s79, v158\n"
"v_mul_f32 v159, s79, v159\n"
"v_cvt_pk_bf16_f32 v156, v156, v157\n"
"v_mul_f32 v36, s79, v36\n"
"v_mul_f32 v37, s79, v37\n"
"v_mul_f32 v38, s79, v38\n"
"v_mul_f32 v39, s79, v39\n"
"v_cvt_pk_bf16_f32 v157, v158, v159\n"
"v_cvt_pk_bf16_f32 v158, v36, v37\n"
"v_cvt_pk_bf16_f32 v159, v38, v39\n"
"v_mfma_f32_16x16x32_bf16 v[36:39], a[16:19], v[144:147], 0\n"
"v_mul_f32 v172, s79, v172\n"
"v_mul_f32 v173, s79, v173\n"
"v_mfma_f32_16x16x32_bf16 v[36:39], a[20:23], v[168:171], v[36:39]\n"
"v_mul_f32 v174, s79, v174\n"
"v_mul_f32 v175, s79, v175\n"
"v_mfma_f32_16x16x32_bf16 v[36:39], a[24:27], a[220:223], v[36:39]\n"
"v_cvt_pk_bf16_f32 v172, v172, v173\n"
"v_mul_f32 v52, s79, v52\n"
"v_mfma_f32_16x16x32_bf16 v[36:39], a[28:31], a[240:243], v[36:39]\n"
"v_mul_f32 v53, s79, v53\n"
"v_mul_f32 v54, s79, v54\n"
"v_mfma_f32_16x16x32_bf16 v[40:43], a[0:3], v[148:151], 0\n"
"v_mul_f32 v55, s79, v55\n"
"v_cvt_pk_bf16_f32 v173, v174, v175\n"
"v_cvt_pk_bf16_f32 v174, v52, v53\n"
"v_cvt_pk_bf16_f32 v175, v54, v55\n"
"v_mul_f32 v104, s79, v104\n"
"v_mul_f32 v105, s79, v105\n"
"v_mul_f32 v106, s79, v106\n"
"v_mul_f32 v107, s79, v107\n"
"v_cvt_pk_bf16_f32 v104, v104, v105\n"
"v_mfma_f32_16x16x32_bf16 v[40:43], a[4:7], v[172:175], v[40:43]\n"
"v_mul_f32 v108, s79, v108\n"
"v_mul_f32 v109, s79, v109\n"
"v_mul_f32 v110, s79, v110\n"
"v_mul_f32 v111, s79, v111\n"
"v_cvt_pk_bf16_f32 v105, v106, v107\n"
"v_cvt_pk_bf16_f32 v106, v108, v109\n"
"v_cvt_pk_bf16_f32 v107, v110, v111\n"
"v_accvgpr_write_b32 a224, v104\n"
"v_accvgpr_write_b32 a225, v105\n"
"v_accvgpr_write_b32 a226, v106\n"
"v_accvgpr_write_b32 a227, v107\n"
"v_mul_f32 v112, s79, v112\n"
"v_mul_f32 v113, s79, v113\n"
"v_mul_f32 v114, s79, v114\n"
"v_mul_f32 v115, s79, v115\n"
"v_cvt_pk_bf16_f32 v112, v112, v113\n"
"v_mfma_f32_16x16x32_bf16 v[40:43], a[8:11], a[224:227], v[40:43]\n"
"v_mul_f32 v116, s79, v116\n"
"v_mul_f32 v117, s79, v117\n"
"v_mul_f32 v118, s79, v118\n"
"v_mul_f32 v119, s79, v119\n"
"v_cvt_pk_bf16_f32 v113, v114, v115\n"
"v_cvt_pk_bf16_f32 v114, v116, v117\n"
"v_cvt_pk_bf16_f32 v115, v118, v119\n"
"v_accvgpr_write_b32 a244, v112\n"
"v_accvgpr_write_b32 a245, v113\n"
"v_accvgpr_write_b32 a246, v114\n"
"v_accvgpr_write_b32 a247, v115\n"
"s_add_u32 m0, s60, 49152\n"
"s_add_u32 s69, s56, 16384\n"
"v_mfma_f32_16x16x32_bf16 v[40:43], a[12:15], a[244:247], v[40:43]\n"
"buffer_load_dwordx4 v1, s[40:43], s69 offen offset:0 nt lds\n"
"v_mfma_f32_16x16x32_bf16 v[44:47], a[16:19], v[148:151], 0\n"
"v_mul_f32 v176, s79, v176\n"
"v_mul_f32 v177, s79, v177\n"
"v_mfma_f32_16x16x32_bf16 v[44:47], a[20:23], v[172:175], v[44:47]\n"
"v_mul_f32 v178, s79, v178\n"
"v_mul_f32 v179, s79, v179\n"
"v_mfma_f32_16x16x32_bf16 v[44:47], a[24:27], a[224:227], v[44:47]\n"
"v_cvt_pk_bf16_f32 v176, v176, v177\n"
"v_mul_f32 v56, s79, v56\n"
"v_mfma_f32_16x16x32_bf16 v[44:47], a[28:31], a[244:247], v[44:47]\n"
"v_mul_f32 v57, s79, v57\n"
"v_mul_f32 v58, s79, v58\n"
"v_mfma_f32_16x16x32_bf16 v[48:51], a[0:3], v[152:155], 0\n"
"v_mul_f32 v59, s79, v59\n"
"v_cvt_pk_bf16_f32 v177, v178, v179\n"
"v_cvt_pk_bf16_f32 v178, v56, v57\n"
"v_cvt_pk_bf16_f32 v179, v58, v59\n"
"v_mul_f32 v120, s79, v120\n"
"v_mul_f32 v121, s79, v121\n"
"v_mul_f32 v122, s79, v122\n"
"v_mul_f32 v123, s79, v123\n"
"v_cvt_pk_bf16_f32 v120, v120, v121\n"
"v_mfma_f32_16x16x32_bf16 v[48:51], a[4:7], v[176:179], v[48:51]\n"
"v_mul_f32 v124, s79, v124\n"
"v_mul_f32 v125, s79, v125\n"
"v_mul_f32 v126, s79, v126\n"
"v_mul_f32 v127, s79, v127\n"
"v_cvt_pk_bf16_f32 v121, v122, v123\n"
"v_cvt_pk_bf16_f32 v122, v124, v125\n"
"v_cvt_pk_bf16_f32 v123, v126, v127\n"
"v_accvgpr_write_b32 a228, v120\n"
"v_accvgpr_write_b32 a229, v121\n"
"v_accvgpr_write_b32 a230, v122\n"
"v_accvgpr_write_b32 a231, v123\n"
"v_mul_f32 v128, s79, v128\n"
"v_mul_f32 v129, s79, v129\n"
"v_mul_f32 v130, s79, v130\n"
"v_mul_f32 v131, s79, v131\n"
"v_cvt_pk_bf16_f32 v128, v128, v129\n"
"v_mfma_f32_16x16x32_bf16 v[48:51], a[8:11], a[228:231], v[48:51]\n"
"v_mul_f32 v132, s79, v132\n"
"v_mul_f32 v133, s79, v133\n"
"v_mul_f32 v134, s79, v134\n"
"v_mul_f32 v135, s79, v135\n"
"v_cvt_pk_bf16_f32 v129, v130, v131\n"
"v_cvt_pk_bf16_f32 v130, v132, v133\n"
"v_cvt_pk_bf16_f32 v131, v134, v135\n"
"v_accvgpr_write_b32 a248, v128\n"
"v_accvgpr_write_b32 a249, v129\n"
"v_accvgpr_write_b32 a250, v130\n"
"v_accvgpr_write_b32 a251, v131\n"
"v_mul_f32 v192, s79, v192\n"
"v_mul_f32 v193, s79, v193\n"
"v_mfma_f32_16x16x32_bf16 v[48:51], a[12:15], a[248:251], v[48:51]\n"
"v_mul_f32 v194, s79, v194\n"
"v_mul_f32 v195, s79, v195\n"
"v_mfma_f32_16x16x32_bf16 v[52:55], a[16:19], v[152:155], 0\n"
"v_cvt_pk_bf16_f32 v192, v192, v193\n"
"v_mul_f32 v196, s79, v196\n"
"v_mfma_f32_16x16x32_bf16 v[52:55], a[20:23], v[176:179], v[52:55]\n"
"v_mul_f32 v197, s79, v197\n"
"v_mul_f32 v198, s79, v198\n"
"v_mfma_f32_16x16x32_bf16 v[52:55], a[24:27], a[228:231], v[52:55]\n"
"v_mul_f32 v199, s79, v199\n"
"v_cvt_pk_bf16_f32 v193, v194, v195\n"
"v_cvt_pk_bf16_f32 v194, v196, v197\n"
"v_cvt_pk_bf16_f32 v195, v198, v199\n"
"v_accvgpr_write_b32 a232, v192\n"
"v_accvgpr_write_b32 a233, v193\n"
"v_accvgpr_write_b32 a234, v194\n"
"v_accvgpr_write_b32 a235, v195\n"
"v_mfma_f32_16x16x32_bf16 v[52:55], a[28:31], a[248:251], v[52:55]\n"
"ds_read_b128 v[192:195], v6 offset:49152\n"
"v_mul_f32 v200, s79, v200\n"
"v_mul_f32 v201, s79, v201\n"
"v_mul_f32 v180, s79, v180\n"
"v_mul_f32 v181, s79, v181\n"
"v_mfma_f32_16x16x32_bf16 v[56:59], a[0:3], v[156:159], 0\n"
"v_mul_f32 v202, s79, v202\n"
"v_mul_f32 v203, s79, v203\n"
"v_cvt_pk_bf16_f32 v200, v200, v201\n"
"ds_read_b128 v[196:199], v6 offset:49408\n"
"v_mul_f32 v182, s79, v182\n"
"v_mul_f32 v183, s79, v183\n"
"v_cvt_pk_bf16_f32 v180, v180, v181\n"
"v_mul_f32 v204, s79, v204\n"
"v_mul_f32 v205, s79, v205\n"
"v_mul_f32 v206, s79, v206\n"
"v_mul_f32 v207, s79, v207\n"
"v_cvt_pk_bf16_f32 v201, v202, v203\n"
"v_mul_f32 v60, s79, v60\n"
"v_mul_f32 v61, s79, v61\n"
"v_mul_f32 v62, s79, v62\n"
"v_mul_f32 v63, s79, v63\n"
"v_cvt_pk_bf16_f32 v181, v182, v183\n"
"v_cvt_pk_bf16_f32 v202, v204, v205\n"
"v_cvt_pk_bf16_f32 v203, v206, v207\n"
"v_cvt_pk_bf16_f32 v182, v60, v61\n"
"v_cvt_pk_bf16_f32 v183, v62, v63\n"
"v_accvgpr_write_b32 a252, v200\n"
"v_accvgpr_write_b32 a253, v201\n"
"v_accvgpr_write_b32 a254, v202\n"
"v_accvgpr_write_b32 a255, v203\n"
"v_mfma_f32_16x16x32_bf16 v[56:59], a[4:7], v[180:183], v[56:59]\n"
"ds_read_b128 v[200:203], v6 offset:49664\n"
"ds_read_b128 v[204:207], v6 offset:49920\n"
"v_mfma_f32_16x16x32_bf16 v[56:59], a[8:11], a[232:235], v[56:59]\n"
"v_max3_f32 v250, v16, v17, v18\n"
"v_max3_f32 v251, v19, v20, v21\n"
"v_mfma_f32_16x16x32_bf16 v[56:59], a[12:15], a[252:255], v[56:59]\n"
"v_max3_f32 v250, v250, v22, v23\n"
"v_max_f32 v250, v250, v251\n"
"v_mfma_f32_16x16x32_bf16 v[60:63], a[16:19], v[156:159], 0\n"
"v_mov_b32 v251, v250\n"
"v_max3_f32 v252, v24, v25, v26\n"
"v_mfma_f32_16x16x32_bf16 v[60:63], a[20:23], v[180:183], v[60:63]\n"
"buffer_load_dwordx4 v2, s[40:43], s69 offen offset:1024 nt lds\n"
"v_permlane16_swap_b32 v250, v251\n"
"v_mfma_f32_16x16x32_bf16 v[60:63], a[24:27], a[232:235], v[60:63]\n"
"v_max_f32 v250, v250, v251\n"
"v_mov_b32 v251, v250\n"
"v_mfma_f32_16x16x32_bf16 v[60:63], a[28:31], a[252:255], v[60:63]\n"
"s_nop 0\n"
"v_permlane32_swap_b32 v250, v251\n"
"v_max_f32 v250, v250, v251\n"
"v_sub_f32 v112, 0, v250\n"
"v_sub_f32 v113, 0, v250\n"
"v_sub_f32 v114, 0, v250\n"
"v_sub_f32 v115, 0, v250\n"
"v_max3_f32 v253, v27, v28, v29\n"
"s_waitcnt lgkmcnt(2)\n"
"v_mfma_f32_16x16x32_bf16 v[64:67], v[192:195], v[136:139], v[112:115]\n"
"v_max3_f32 v252, v252, v30, v31\n"
"v_max_f32 v252, v252, v253\n"
"v_mfma_f32_16x16x32_bf16 v[64:67], v[196:199], v[160:163], v[64:67]\n"
"v_mov_b32 v253, v252\n"
"s_waitcnt lgkmcnt(0)\n"
"v_mfma_f32_16x16x32_bf16 v[64:67], v[200:203], v[184:187], v[64:67]\n"
"v_permlane16_swap_b32 v252, v253\n"
"v_max_f32 v252, v252, v253\n"
"v_mov_b32 v253, v252\n"
"v_mfma_f32_16x16x32_bf16 v[64:67], v[204:207], v[188:191], v[64:67]\n"
"s_nop 0\n"
"v_permlane32_swap_b32 v252, v253\n"
"v_max_f32 v252, v252, v253\n"
"v_sub_f32 v116, 0, v252\n"
"v_sub_f32 v117, 0, v252\n"
"v_sub_f32 v118, 0, v252\n"
"v_sub_f32 v119, 0, v252\n"
"v_sub_f32 v16, v16, v250\n"
"v_sub_f32 v17, v17, v250\n"
"v_mfma_f32_16x16x32_bf16 v[72:75], v[192:195], v[140:143], v[116:119]\n"
"v_sub_f32 v18, v18, v250\n"
"v_sub_f32 v19, v19, v250\n"
"v_sub_f32 v20, v20, v250\n"
"v_sub_f32 v21, v21, v250\n"
"v_sub_f32 v22, v22, v250\n"
"v_sub_f32 v23, v23, v250\n"
"v_max3_f32 v250, v32, v33, v34\n"
"v_max3_f32 v251, v35, v36, v37\n"
"v_max3_f32 v250, v250, v38, v39\n"
"v_max_f32 v250, v250, v251\n"
"v_mfma_f32_16x16x32_bf16 v[72:75], v[196:199], v[164:167], v[72:75]\n"
"v_mov_b32 v251, v250\n"
"v_sub_f32 v24, v24, v252\n"
"v_mfma_f32_16x16x32_bf16 v[72:75], v[200:203], a[216:219], v[72:75]\n"
"v_permlane16_swap_b32 v250, v251\n"
"v_max_f32 v250, v250, v251\n"
"v_mov_b32 v251, v250\n"
"v_mfma_f32_16x16x32_bf16 v[72:75], v[204:207], a[236:239], v[72:75]\n"
"s_nop 0\n"
"v_permlane32_swap_b32 v250, v251\n"
"v_max_f32 v250, v250, v251\n"
"v_sub_f32 v120, 0, v250\n"
"v_sub_f32 v121, 0, v250\n"
"v_sub_f32 v122, 0, v250\n"
"v_sub_f32 v123, 0, v250\n"
"v_sub_f32 v25, v25, v252\n"
"v_sub_f32 v26, v26, v252\n"
"v_mfma_f32_16x16x32_bf16 v[80:83], v[192:195], v[144:147], v[120:123]\n"
"v_sub_f32 v27, v27, v252\n"
"v_sub_f32 v28, v28, v252\n"
"v_sub_f32 v29, v29, v252\n"
"v_sub_f32 v30, v30, v252\n"
"v_sub_f32 v31, v31, v252\n"
"v_max3_f32 v252, v40, v41, v42\n"
"v_max3_f32 v253, v43, v44, v45\n"
"v_max3_f32 v252, v252, v46, v47\n"
"v_max_f32 v252, v252, v253\n"
"v_mfma_f32_16x16x32_bf16 v[80:83], v[196:199], v[168:171], v[80:83]\n"
"v_mov_b32 v253, v252\n"
"v_sub_f32 v32, v32, v250\n"
"v_mfma_f32_16x16x32_bf16 v[80:83], v[200:203], a[220:223], v[80:83]\n"
"v_permlane16_swap_b32 v252, v253\n"
"v_max_f32 v252, v252, v253\n"
"v_mov_b32 v253, v252\n"
"v_mfma_f32_16x16x32_bf16 v[80:83], v[204:207], a[240:243], v[80:83]\n"
"s_nop 0\n"
"v_permlane32_swap_b32 v252, v253\n"
"v_max_f32 v252, v252, v253\n"
"v_sub_f32 v124, 0, v252\n"
"v_sub_f32 v125, 0, v252\n"
"v_sub_f32 v126, 0, v252\n"
"v_sub_f32 v127, 0, v252\n"
"v_sub_f32 v33, v33, v250\n"
"v_sub_f32 v34, v34, v250\n"
"v_mfma_f32_16x16x32_bf16 v[88:91], v[192:195], v[148:151], v[124:127]\n"
"v_sub_f32 v35, v35, v250\n"
"v_sub_f32 v36, v36, v250\n"
"v_sub_f32 v37, v37, v250\n"
"v_sub_f32 v38, v38, v250\n"
"v_sub_f32 v39, v39, v250\n"
"v_max3_f32 v250, v48, v49, v50\n"
"v_max3_f32 v251, v51, v52, v53\n"
"v_max3_f32 v250, v250, v54, v55\n"
"v_max_f32 v250, v250, v251\n"
"v_mfma_f32_16x16x32_bf16 v[88:91], v[196:199], v[172:175], v[88:91]\n"
"v_mov_b32 v251, v250\n"
"ds_read_b128 v[208:211], v6 offset:53248\n"
"v_mfma_f32_16x16x32_bf16 v[88:91], v[200:203], a[224:227], v[88:91]\n"
"v_permlane16_swap_b32 v250, v251\n"
"v_max_f32 v250, v250, v251\n"
"v_mov_b32 v251, v250\n"
"buffer_load_dwordx4 v1, s[40:43], s69 offen offset:2048 nt lds\n"
"v_mfma_f32_16x16x32_bf16 v[88:91], v[204:207], a[244:247], v[88:91]\n"
"v_permlane32_swap_b32 v250, v251\n"
"v_max_f32 v250, v250, v251\n"
"v_sub_f32 v128, 0, v250\n"
"v_sub_f32 v129, 0, v250\n"
"v_sub_f32 v130, 0, v250\n"
"v_sub_f32 v131, 0, v250\n"
"ds_read_b128 v[212:215], v6 offset:53504\n"
"v_sub_f32 v40, v40, v252\n"
"v_mfma_f32_16x16x32_bf16 v[96:99], v[192:195], v[152:155], v[128:131]\n"
"v_sub_f32 v41, v41, v252\n"
"v_sub_f32 v42, v42, v252\n"
"v_sub_f32 v43, v43, v252\n"
"v_sub_f32 v44, v44, v252\n"
"v_sub_f32 v45, v45, v252\n"
"v_sub_f32 v46, v46, v252\n"
"v_sub_f32 v47, v47, v252\n"
"v_max3_f32 v252, v56, v57, v58\n"
"v_max3_f32 v253, v59, v60, v61\n"
"v_max3_f32 v252, v252, v62, v63\n"
"v_max_f32 v252, v252, v253\n"
"v_mfma_f32_16x16x32_bf16 v[96:99], v[196:199], v[176:179], v[96:99]\n"
"ds_read_b128 v[216:219], v6 offset:53760\n"
"v_mov_b32 v253, v252\n"
"v_mfma_f32_16x16x32_bf16 v[96:99], v[200:203], a[228:231], v[96:99]\n"
"s_nop 0\n"
"v_permlane16_swap_b32 v252, v253\n"
"ds_read_b128 v[220:223], v6 offset:54016\n"
"v_max_f32 v252, v252, v253\n"
"v_mov_b32 v253, v252\n"
"v_mfma_f32_16x16x32_bf16 v[96:99], v[204:207], a[248:251], v[96:99]\n"
"s_nop 0\n"
"v_permlane32_swap_b32 v252, v253\n"
"v_max_f32 v252, v252, v253\n"
"v_sub_f32 v132, 0, v252\n"
"v_sub_f32 v133, 0, v252\n"
"v_sub_f32 v134, 0, v252\n"
"v_sub_f32 v135, 0, v252\n"
"v_exp_f32 v16, v16\n"
"s_nop 0\n"
"v_mfma_f32_16x16x32_bf16 v[104:107], v[192:195], v[156:159], v[132:135]\n"
"v_exp_f32 v17, v17\n"
"v_mfma_f32_16x16x32_bf16 v[104:107], v[196:199], v[180:183], v[104:107]\n"
"v_exp_f32 v18, v18\n"
"v_mfma_f32_16x16x32_bf16 v[104:107], v[200:203], a[232:235], v[104:107]\n"
"v_exp_f32 v19, v19\n"
"v_mfma_f32_16x16x32_bf16 v[104:107], v[204:207], a[252:255], v[104:107]\n"
"v_cvt_pk_bf16_f32 v16, v16, v17\n"
"s_waitcnt lgkmcnt(2)\n"
"v_mfma_f32_16x16x32_bf16 v[68:71], v[208:211], v[136:139], v[112:115]\n"
"v_exp_f32 v20, v20\n"
"v_mfma_f32_16x16x32_bf16 v[68:71], v[212:215], v[160:163], v[68:71]\n"
"v_cvt_pk_bf16_f32 v17, v18, v19\n"
"s_waitcnt lgkmcnt(1)\n"
"v_mfma_f32_16x16x32_bf16 v[68:71], v[216:219], v[184:187], v[68:71]\n"
"v_sub_f32 v48, v48, v250\n"
"s_waitcnt lgkmcnt(0)\n"
"v_mfma_f32_16x16x32_bf16 v[68:71], v[220:223], v[188:191], v[68:71]\n"
"s_barrier\n"
"v_mfma_f32_16x16x32_bf16 v[76:79], v[208:211], v[140:143], v[116:119]\n"
"buffer_load_dwordx4 v2, s[40:43], s69 offen offset:3072 nt lds\n"
"v_mfma_f32_16x16x32_bf16 v[76:79], v[212:215], v[164:167], v[76:79]\n"
"ds_read_b64_tr_b16 v[224:225], v7 offset:0\n"
"ds_read_b64_tr_b16 v[226:227], v7 offset:4096\n"
"v_mfma_f32_16x16x32_bf16 v[76:79], v[216:219], a[216:219], v[76:79]\n"
"ds_read_b64_tr_b16 v[228:229], v8 offset:0\n"
"ds_read_b64_tr_b16 v[230:231], v8 offset:4096\n"
"v_mfma_f32_16x16x32_bf16 v[76:79], v[220:223], a[236:239], v[76:79]\n"
"v_exp_f32 v21, v21\n"
"v_mfma_f32_16x16x32_bf16 v[84:87], v[208:211], v[144:147], v[120:123]\n"
"v_exp_f32 v22, v22\n"
"v_mfma_f32_16x16x32_bf16 v[84:87], v[212:215], v[168:171], v[84:87]\n"
"v_exp_f32 v23, v23\n"
"v_mfma_f32_16x16x32_bf16 v[84:87], v[216:219], a[220:223], v[84:87]\n"
"v_exp_f32 v24, v24\n"
"v_mfma_f32_16x16x32_bf16 v[84:87], v[220:223], a[240:243], v[84:87]\n"
"v_exp_f32 v25, v25\n"
"v_mfma_f32_16x16x32_bf16 v[92:95], v[208:211], v[148:151], v[124:127]\n"
"v_cvt_pk_bf16_f32 v18, v20, v21\n"
"v_cvt_pk_bf16_f32 v19, v22, v23\n"
"v_mfma_f32_16x16x32_bf16 v[92:95], v[212:215], v[172:175], v[92:95]\n"
"v_exp_f32 v26, v26\n"
"v_mfma_f32_16x16x32_bf16 v[92:95], v[216:219], a[224:227], v[92:95]\n"
"v_exp_f32 v27, v27\n"
"v_mfma_f32_16x16x32_bf16 v[92:95], v[220:223], a[244:247], v[92:95]\n"
"v_exp_f32 v28, v28\n"
"v_mfma_f32_16x16x32_bf16 v[100:103], v[208:211], v[152:155], v[128:131]\n"
"v_exp_f32 v29, v29\n"
"v_mfma_f32_16x16x32_bf16 v[100:103], v[212:215], v[176:179], v[100:103]\n"
"v_exp_f32 v30, v30\n"
"v_mfma_f32_16x16x32_bf16 v[100:103], v[216:219], a[228:231], v[100:103]\n"
"v_exp_f32 v31, v31\n"
"v_mfma_f32_16x16x32_bf16 v[100:103], v[220:223], a[248:251], v[100:103]\n"
"s_add_u32 m0, s60, 32768\n"
"s_add_u32 s70, s56, 32768\n"
"v_mfma_f32_16x16x32_bf16 v[108:111], v[208:211], v[156:159], v[132:135]\n"
"v_cvt_pk_bf16_f32 v24, v24, v25\n"
"v_cvt_pk_bf16_f32 v25, v26, v27\n"
"v_mfma_f32_16x16x32_bf16 v[108:111], v[212:215], v[180:183], v[108:111]\n"
"v_exp_f32 v32, v32\n"
"v_mfma_f32_16x16x32_bf16 v[108:111], v[216:219], a[232:235], v[108:111]\n"
"v_exp_f32 v33, v33\n"
"v_mfma_f32_16x16x32_bf16 v[108:111], v[220:223], a[252:255], v[108:111]\n"
"v_cvt_pk_bf16_f32 v26, v28, v29\n"
"s_waitcnt lgkmcnt(0)\n"
"v_mfma_f32_16x16x32_bf16 a[0:3], v[224:227], v[16:19], 0\n"
"buffer_load_dwordx4 v1, s[36:39], s70 offen offset:0 nt lds\n"
"v_cvt_pk_bf16_f32 v27, v30, v31\n"
"v_exp_f32 v34, v34\n"
"v_exp_f32 v35, v35\n"
"v_exp_f32 v36, v36\n"
"v_exp_f32 v37, v37\n"
"v_exp_f32 v38, v38\n"
"v_exp_f32 v39, v39\n"
"v_mfma_f32_16x16x32_bf16 a[8:11], v[224:227], v[24:27], 0\n"
"v_cvt_pk_bf16_f32 v32, v32, v33\n"
"v_cvt_pk_bf16_f32 v33, v34, v35\n"
"v_exp_f32 v40, v40\n"
"v_exp_f32 v41, v41\n"
"v_cvt_pk_bf16_f32 v34, v36, v37\n"
"v_cvt_pk_bf16_f32 v35, v38, v39\n"
"v_exp_f32 v42, v42\n"
"v_exp_f32 v43, v43\n"
"v_exp_f32 v44, v44\n"
"v_exp_f32 v45, v45\n"
"v_exp_f32 v46, v46\n"
"v_exp_f32 v47, v47\n"
"v_mfma_f32_16x16x32_bf16 a[16:19], v[224:227], v[32:35], 0\n"
"v_sub_f32 v49, v49, v250\n"
"v_cvt_pk_bf16_f32 v40, v40, v41\n"
"v_sub_f32 v50, v50, v250\n"
"v_sub_f32 v51, v51, v250\n"
"v_cvt_pk_bf16_f32 v41, v42, v43\n"
"v_exp_f32 v48, v48\n"
"v_exp_f32 v49, v49\n"
"v_sub_f32 v52, v52, v250\n"
"v_sub_f32 v53, v53, v250\n"
"v_sub_f32 v54, v54, v250\n"
"v_sub_f32 v55, v55, v250\n"
"v_cvt_pk_bf16_f32 v42, v44, v45\n"
"v_cvt_pk_bf16_f32 v43, v46, v47\n"
"v_exp_f32 v50, v50\n"
"v_exp_f32 v51, v51\n"
"v_exp_f32 v52, v52\n"
"v_exp_f32 v53, v53\n"
"v_exp_f32 v54, v54\n"
"v_exp_f32 v55, v55\n"
"v_mfma_f32_16x16x32_bf16 a[24:27], v[224:227], v[40:43], 0\n"
"ds_read_b64_tr_b16 v[232:233], v7 offset:256\n"
"ds_read_b64_tr_b16 v[234:235], v7 offset:4352\n"
"v_sub_f32 v56, v56, v252\n"
"v_sub_f32 v57, v57, v252\n"
"v_cvt_pk_bf16_f32 v48, v48, v49\n"
"v_sub_f32 v58, v58, v252\n"
"v_sub_f32 v59, v59, v252\n"
"v_cvt_pk_bf16_f32 v49, v50, v51\n"
"v_exp_f32 v56, v56\n"
"v_exp_f32 v57, v57\n"
"v_sub_f32 v60, v60, v252\n"
"v_sub_f32 v61, v61, v252\n"
"v_sub_f32 v62, v62, v252\n"
"v_sub_f32 v63, v63, v252\n"
"v_cvt_pk_bf16_f32 v50, v52, v53\n"
"v_cvt_pk_bf16_f32 v51, v54, v55\n"
"v_exp_f32 v58, v58\n"
"v_exp_f32 v59, v59\n"
"v_exp_f32 v60, v60\n"
"v_exp_f32 v61, v61\n"
"v_exp_f32 v62, v62\n"
"v_exp_f32 v63, v63\n"
"v_mfma_f32_16x16x32_bf16 a[32:35], v[224:227], v[48:51], 0\n"
"v_cvt_pk_bf16_f32 v56, v56, v57\n"
"v_cvt_pk_bf16_f32 v57, v58, v59\n"
"v_cvt_pk_bf16_f32 v58, v60, v61\n"
"v_cvt_pk_bf16_f32 v59, v62, v63\n"
"ds_read_b64_tr_b16 v[236:237], v8 offset:256\n"
"ds_read_b64_tr_b16 v[238:239], v8 offset:4352\n"
"v_mfma_f32_16x16x32_bf16 a[40:43], v[224:227], v[56:59], 0\n"
"ds_read_b64_tr_b16 v[192:193], v7 offset:512\n"
"ds_read_b64_tr_b16 v[194:195], v7 offset:4608\n"
"v_mfma_f32_16x16x32_bf16 a[4:7], v[228:231], v[16:19], 0\n"
"ds_read_b64_tr_b16 v[196:197], v8 offset:512\n"
"ds_read_b64_tr_b16 v[198:199], v8 offset:4608\n"
"v_mfma_f32_16x16x32_bf16 a[12:15], v[228:231], v[24:27], 0\n"
"ds_read_b64_tr_b16 v[200:201], v7 offset:768\n"
"ds_read_b64_tr_b16 v[202:203], v7 offset:4864\n"
"v_mfma_f32_16x16x32_bf16 a[20:23], v[228:231], v[32:35], 0\n"
"ds_read_b64_tr_b16 v[204:205], v8 offset:768\n"
"ds_read_b64_tr_b16 v[206:207], v8 offset:4864\n"
"v_mfma_f32_16x16x32_bf16 a[28:31], v[228:231], v[40:43], 0\n"
"v_add_u32 v6, 0x10000, v6\n"
"ds_read_b128 v[208:211], v6 offset:0\n"
"v_mfma_f32_16x16x32_bf16 a[36:39], v[228:231], v[48:51], 0\n"
"buffer_load_dwordx4 v2, s[36:39], s70 offen offset:1024 nt lds\n"
"v_mfma_f32_16x16x32_bf16 a[44:47], v[228:231], v[56:59], 0\n"
"ds_read_b128 v[212:215], v6 offset:256\n"
"s_waitcnt lgkmcnt(12)\n"
"v_mfma_f32_16x16x32_bf16 a[48:51], v[232:235], v[16:19], 0\n"
"ds_read_b128 v[216:219], v6 offset:512\n"
"ds_read_b128 v[220:223], v6 offset:768\n"
"v_mfma_f32_16x16x32_bf16 a[56:59], v[232:235], v[24:27], 0\n"
"v_max3_f32 v12, v64, v65, v66\n"
"v_max3_f32 v13, v67, v72, v73\n"
"v_mfma_f32_16x16x32_bf16 a[64:67], v[232:235], v[32:35], 0\n"
"v_max3_f32 v14, v74, v75, v80\n"
"v_max3_f32 v12, v12, v81, v82\n"
"v_mfma_f32_16x16x32_bf16 a[72:75], v[232:235], v[40:43], 0\n"
"v_max3_f32 v13, v13, v83, v88\n"
"v_max3_f32 v14, v14, v89, v90\n"
"v_mfma_f32_16x16x32_bf16 a[80:83], v[232:235], v[48:51], 0\n"
"v_max3_f32 v12, v12, v91, v96\n"
"v_max3_f32 v13, v13, v97, v98\n"
"v_mfma_f32_16x16x32_bf16 a[88:91], v[232:235], v[56:59], 0\n"
"v_max3_f32 v14, v14, v99, v104\n"
"s_waitcnt lgkmcnt(5)\n"
"v_mfma_f32_16x16x32_bf16 a[52:55], v[236:239], v[16:19], 0\n"
"v_max3_f32 v12, v12, v105, v106\n"
"v_max3_f32 v13, v13, v107, v68\n"
"v_mfma_f32_16x16x32_bf16 a[60:63], v[236:239], v[24:27], 0\n"
"v_max3_f32 v14, v14, v69, v70\n"
"v_max3_f32 v12, v12, v71, v76\n"
"v_mfma_f32_16x16x32_bf16 a[68:71], v[236:239], v[32:35], 0\n"
"v_max3_f32 v13, v13, v77, v78\n"
"v_max3_f32 v14, v14, v79, v84\n"
"v_mfma_f32_16x16x32_bf16 a[76:79], v[236:239], v[40:43], 0\n"
"v_max3_f32 v12, v12, v85, v86\n"
"v_max3_f32 v13, v13, v87, v92\n"
"v_mfma_f32_16x16x32_bf16 a[84:87], v[236:239], v[48:51], 0\n"
"buffer_load_dwordx4 v1, s[36:39], s70 offen offset:2048 nt lds\n"
"v_mfma_f32_16x16x32_bf16 a[92:95], v[236:239], v[56:59], 0\n"
"v_max3_f32 v14, v14, v93, v94\n"
"v_max3_f32 v12, v12, v95, v100\n"
"v_mfma_f32_16x16x32_bf16 a[96:99], v[192:195], v[16:19], 0\n"
"v_max3_f32 v13, v13, v101, v102\n"
"v_max3_f32 v14, v14, v103, v108\n"
"v_mfma_f32_16x16x32_bf16 a[104:107], v[192:195], v[24:27], 0\n"
"v_max3_f32 v12, v12, v109, v110\n"
"v_max3_f32 v12, v12, v13, v14\n"
"v_mfma_f32_16x16x32_bf16 a[112:115], v[192:195], v[32:35], 0\n"
"v_max_f32 v12, v12, v111\n"
"v_cmp_lt_f32 vcc, s78, v12\n"
"v_mfma_f32_16x16x32_bf16 a[120:123], v[192:195], v[40:43], 0\n"
"s_cbranch_vccnz LT0_%=\n"
"LR0_%=:\n"
"ds_read_b128 v[224:227], v6 offset:4096\n"
"v_mfma_f32_16x16x32_bf16 a[128:131], v[192:195], v[48:51], 0\n"
"ds_read_b128 v[228:231], v6 offset:4352\n"
"ds_read_b128 v[232:235], v6 offset:4608\n"
"v_mfma_f32_16x16x32_bf16 a[136:139], v[192:195], v[56:59], 0\n"
"ds_read_b128 v[236:239], v6 offset:4864\n"
"v_add_u32 v7, 0xfffe0000, v7\n"
"v_mfma_f32_16x16x32_bf16 a[100:103], v[196:199], v[16:19], 0\n"
"v_add_u32 v8, 0xfffe0000, v8\n"
"v_add_u32 v6, 0xfffe0000, v6\n"
"v_mfma_f32_16x16x32_bf16 a[108:111], v[196:199], v[24:27], 0\n"
"v_exp_f32 v64, v64\n"
"v_mfma_f32_16x16x32_bf16 a[116:119], v[196:199], v[32:35], 0\n"
"v_exp_f32 v65, v65\n"
"v_mfma_f32_16x16x32_bf16 a[124:127], v[196:199], v[40:43], 0\n"
"buffer_load_dwordx4 v2, s[36:39], s70 offen offset:3072 nt lds\n"
"v_mfma_f32_16x16x32_bf16 a[132:135], v[196:199], v[48:51], 0\n"
"v_exp_f32 v66, v66\n"
"v_mfma_f32_16x16x32_bf16 a[140:143], v[196:199], v[56:59], 0\n"
"v_exp_f32 v67, v67\n"
"v_mfma_f32_16x16x32_bf16 a[144:147], v[200:203], v[16:19], 0\n"
"v_exp_f32 v68, v68\n"
"v_mfma_f32_16x16x32_bf16 a[152:155], v[200:203], v[24:27], 0\n"
"v_exp_f32 v69, v69\n"
"v_mfma_f32_16x16x32_bf16 a[160:163], v[200:203], v[32:35], 0\n"
"v_exp_f32 v70, v70\n"
"v_mfma_f32_16x16x32_bf16 a[168:171], v[200:203], v[40:43], 0\n"
"v_exp_f32 v71, v71\n"
"v_mfma_f32_16x16x32_bf16 a[176:179], v[200:203], v[48:51], 0\n"
"v_cvt_pk_bf16_f32 v64, v64, v65\n"
"v_cvt_pk_bf16_f32 v65, v66, v67\n"
"v_mfma_f32_16x16x32_bf16 a[184:187], v[200:203], v[56:59], 0\n"
"v_cvt_pk_bf16_f32 v66, v68, v69\n"
"s_waitcnt lgkmcnt(0)\n"
"v_mfma_f32_16x16x32_bf16 a[148:151], v[204:207], v[16:19], 0\n"
"v_exp_f32 v72, v72\n"
"v_mfma_f32_16x16x32_bf16 a[156:159], v[204:207], v[24:27], 0\n"
"v_exp_f32 v73, v73\n"
"v_mfma_f32_16x16x32_bf16 a[164:167], v[204:207], v[32:35], 0\n"
"v_cvt_pk_bf16_f32 v67, v70, v71\n"
"v_cvt_pk_bf16_f32 v72, v72, v73\n"
"v_mfma_f32_16x16x32_bf16 a[172:175], v[204:207], v[40:43], 0\n"
"v_exp_f32 v74, v74\n"
"v_mfma_f32_16x16x32_bf16 a[180:183], v[204:207], v[48:51], 0\n"
"v_exp_f32 v75, v75\n"
"v_mfma_f32_16x16x32_bf16 a[188:191], v[204:207], v[56:59], 0\n"
"v_exp_f32 v76, v76\n"
"v_mfma_f32_16x16x32_bf16 a[192:195], v[240:243], v[16:19], 0\n"
"v_exp_f32 v77, v77\n"
"v_mfma_f32_16x16x32_bf16 a[196:199], v[240:243], v[24:27], 0\n"
"v_exp_f32 v78, v78\n"
"v_mfma_f32_16x16x32_bf16 a[200:203], v[240:243], v[32:35], 0\n"
"s_add_u32 m0, s60, 81920\n"
"s_add_u32 s71, s56, 24576\n"
"v_mfma_f32_16x16x32_bf16 a[204:207], v[240:243], v[40:43], 0\n"
"buffer_load_dwordx4 v1, s[40:43], s71 offen offset:0 nt lds\n"
"v_mfma_f32_16x16x32_bf16 a[208:211], v[240:243], v[48:51], 0\n"
"v_exp_f32 v79, v79\n"
"v_mfma_f32_16x16x32_bf16 a[212:215], v[240:243], v[56:59], 0\n"
"s_cmp_lg_u32 s63, 0\n"
"s_cbranch_scc1 LT1_%=\n"
"LR1_%=:\n"
"v_mfma_f32_16x16x32_bf16 v[16:19], v[208:211], v[136:139], v[112:115]\n"
"v_cvt_pk_bf16_f32 v73, v74, v75\n"
"v_cvt_pk_bf16_f32 v74, v76, v77\n"
"v_mfma_f32_16x16x32_bf16 v[16:19], v[212:215], v[160:163], v[16:19]\n"
"v_exp_f32 v80, v80\n"
"v_mfma_f32_16x16x32_bf16 v[16:19], v[216:219], v[184:187], v[16:19]\n"
"v_exp_f32 v81, v81\n"
"v_mfma_f32_16x16x32_bf16 v[16:19], v[220:223], v[188:191], v[16:19]\n"
"v_cvt_pk_bf16_f32 v75, v78, v79\n"
"v_cvt_pk_bf16_f32 v80, v80, v81\n"
"v_mfma_f32_16x16x32_bf16 v[24:27], v[208:211], v[140:143], v[116:119]\n"
"v_exp_f32 v82, v82\n"
"v_mfma_f32_16x16x32_bf16 v[24:27], v[212:215], v[164:167], v[24:27]\n"
"v_exp_f32 v83, v83\n"
"v_mfma_f32_16x16x32_bf16 v[24:27], v[216:219], a[216:219], v[24:27]\n"
"v_exp_f32 v84, v84\n"
"v_mfma_f32_16x16x32_bf16 v[24:27], v[220:223], a[236:239], v[24:27]\n"
"v_exp_f32 v85, v85\n"
"v_mfma_f32_16x16x32_bf16 v[32:35], v[208:211], v[144:147], v[120:123]\n"
"v_exp_f32 v86, v86\n"
"v_mfma_f32_16x16x32_bf16 v[32:35], v[212:215], v[168:171], v[32:35]\n"
"v_exp_f32 v87, v87\n"
"v_mfma_f32_16x16x32_bf16 v[32:35], v[216:219], a[220:223], v[32:35]\n"
"v_cvt_pk_bf16_f32 v81, v82, v83\n"
"v_cvt_pk_bf16_f32 v82, v84, v85\n"
"v_mfma_f32_16x16x32_bf16 v[32:35], v[220:223], a[240:243], v[32:35]\n"
"v_exp_f32 v88, v88\n"
"v_mfma_f32_16x16x32_bf16 v[40:43], v[208:211], v[148:151], v[124:127]\n"
"v_exp_f32 v89, v89\n"
"v_mfma_f32_16x16x32_bf16 v[40:43], v[212:215], v[172:175], v[40:43]\n"
"buffer_load_dwordx4 v2, s[40:43], s71 offen offset:1024 nt lds\n"
"v_mfma_f32_16x16x32_bf16 v[40:43], v[216:219], a[224:227], v[40:43]\n"
"s_waitcnt vmcnt(10)\n"
"s_barrier\n"
"v_mfma_f32_16x16x32_bf16 v[40:43], v[220:223], a[244:247], v[40:43]\n"
"ds_read_b64_tr_b16 v[192:193], v7 offset:0\n"
"ds_read_b64_tr_b16 v[194:195], v7 offset:4096\n"
"v_mfma_f32_16x16x32_bf16 v[48:51], v[208:211], v[152:155], v[128:131]\n"
"ds_read_b64_tr_b16 v[196:197], v8 offset:0\n"
"ds_read_b64_tr_b16 v[198:199], v8 offset:4096\n"
"v_mfma_f32_16x16x32_bf16 v[48:51], v[212:215], v[176:179], v[48:51]\n"
"v_cvt_pk_bf16_f32 v83, v86, v87\n"
"v_cvt_pk_bf16_f32 v88, v88, v89\n"
"v_mfma_f32_16x16x32_bf16 v[48:51], v[216:219], a[228:231], v[48:51]\n"
"v_exp_f32 v90, v90\n"
"v_mfma_f32_16x16x32_bf16 v[48:51], v[220:223], a[248:251], v[48:51]\n"
"v_exp_f32 v91, v91\n"
"v_mfma_f32_16x16x32_bf16 v[56:59], v[208:211], v[156:159], v[132:135]\n"
"v_exp_f32 v92, v92\n"
"v_mfma_f32_16x16x32_bf16 v[56:59], v[212:215], v[180:183], v[56:59]\n"
"v_exp_f32 v93, v93\n"
"v_mfma_f32_16x16x32_bf16 v[56:59], v[216:219], a[232:235], v[56:59]\n"
"v_exp_f32 v94, v94\n"
"v_mfma_f32_16x16x32_bf16 v[56:59], v[220:223], a[252:255], v[56:59]\n"
"v_exp_f32 v95, v95\n"
"v_mfma_f32_16x16x32_bf16 v[20:23], v[224:227], v[136:139], v[112:115]\n"
"v_cvt_pk_bf16_f32 v89, v90, v91\n"
"v_cvt_pk_bf16_f32 v90, v92, v93\n"
"v_mfma_f32_16x16x32_bf16 v[20:23], v[228:231], v[160:163], v[20:23]\n"
"v_exp_f32 v96, v96\n"
"v_mfma_f32_16x16x32_bf16 v[20:23], v[232:235], v[184:187], v[20:23]\n"
"buffer_load_dwordx4 v1, s[40:43], s71 offen offset:2048 nt lds\n"
"v_mfma_f32_16x16x32_bf16 v[20:23], v[236:239], v[188:191], v[20:23]\n"
"v_exp_f32 v97, v97\n"
"v_mfma_f32_16x16x32_bf16 v[28:31], v[224:227], v[140:143], v[116:119]\n"
"v_cvt_pk_bf16_f32 v91, v94, v95\n"
"ds_read_b64_tr_b16 v[200:201], v7 offset:256\n"
"v_mfma_f32_16x16x32_bf16 v[28:31], v[228:231], v[164:167], v[28:31]\n"
"v_exp_f32 v98, v98\n"
"v_mfma_f32_16x16x32_bf16 v[28:31], v[232:235], a[216:219], v[28:31]\n"
"v_exp_f32 v99, v99\n"
"v_mfma_f32_16x16x32_bf16 v[28:31], v[236:239], a[236:239], v[28:31]\n"
"v_exp_f32 v100, v100\n"
"v_mfma_f32_16x16x32_bf16 v[36:39], v[224:227], v[144:147], v[120:123]\n"
"v_exp_f32 v101, v101\n"
"v_mfma_f32_16x16x32_bf16 v[36:39], v[228:231], v[168:171], v[36:39]\n"
"v_exp_f32 v102, v102\n"
"v_mfma_f32_16x16x32_bf16 v[36:39], v[232:235], a[220:223], v[36:39]\n"
"v_exp_f32 v103, v103\n"
"v_mfma_f32_16x16x32_bf16 v[36:39], v[236:239], a[240:243], v[36:39]\n"
"ds_read_b64_tr_b16 v[202:203], v7 offset:4352\n"
"v_cvt_pk_bf16_f32 v96, v96, v97\n"
"v_mfma_f32_16x16x32_bf16 v[44:47], v[224:227], v[148:151], v[124:127]\n"
"v_cvt_pk_bf16_f32 v97, v98, v99\n"
"v_cvt_pk_bf16_f32 v98, v100, v101\n"
"v_mfma_f32_16x16x32_bf16 v[44:47], v[228:231], v[172:175], v[44:47]\n"
"buffer_load_dwordx4 v2, s[40:43], s71 offen offset:3072 nt lds\n"
"v_mfma_f32_16x16x32_bf16 v[44:47], v[232:235], a[224:227], v[44:47]\n"
"v_exp_f32 v104, v104\n"
"v_mfma_f32_16x16x32_bf16 v[44:47], v[236:239], a[244:247], v[44:47]\n"
"v_exp_f32 v105, v105\n"
"v_mfma_f32_16x16x32_bf16 v[52:55], v[224:227], v[152:155], v[128:131]\n"
"v_cvt_pk_bf16_f32 v99, v102, v103\n"
"v_cvt_pk_bf16_f32 v104, v104, v105\n"
"v_mfma_f32_16x16x32_bf16 v[52:55], v[228:231], v[176:179], v[52:55]\n"
"v_exp_f32 v106, v106\n"
"v_mfma_f32_16x16x32_bf16 v[52:55], v[232:235], a[228:231], v[52:55]\n"
"v_exp_f32 v107, v107\n"
"v_mfma_f32_16x16x32_bf16 v[52:55], v[236:239], a[248:251], v[52:55]\n"
"v_exp_f32 v108, v108\n"
"v_mfma_f32_16x16x32_bf16 v[60:63], v[224:227], v[156:159], v[132:135]\n"
"v_exp_f32 v109, v109\n"
"v_mfma_f32_16x16x32_bf16 v[60:63], v[228:231], v[180:183], v[60:63]\n"
"v_exp_f32 v110, v110\n"
"v_mfma_f32_16x16x32_bf16 v[60:63], v[232:235], a[232:235], v[60:63]\n"
"v_exp_f32 v111, v111\n"
"v_mfma_f32_16x16x32_bf16 v[60:63], v[236:239], a[252:255], v[60:63]\n"
"v_cvt_pk_bf16_f32 v105, v106, v107\n"
"s_waitcnt lgkmcnt(0)\n"
"v_mfma_f32_16x16x32_bf16 a[0:3], v[192:195], v[64:67], a[0:3]\n"
"v_cvt_pk_bf16_f32 v106, v108, v109\n"
"v_cvt_pk_bf16_f32 v107, v110, v111\n"
"v_mfma_f32_16x16x32_bf16 a[8:11], v[192:195], v[72:75], a[8:11]\n"
"s_add_u32 m0, s60, 65536\n"
"s_add_u32 s64, s56, 40960\n"
"v_mfma_f32_16x16x32_bf16 a[16:19], v[192:195], v[80:83], a[16:19]\n"
"buffer_load_dwordx4 v1, s[36:39], s64 offen offset:0 nt lds\n"
"v_mfma_f32_16x16x32_bf16 a[24:27], v[192:195], v[88:91], a[24:27]\n"
"ds_read_b64_tr_b16 v[204:205], v8 offset:256\n"
"ds_read_b64_tr_b16 v[206:207], v8 offset:4352\n"
"v_mfma_f32_16x16x32_bf16 a[32:35], v[192:195], v[96:99], a[32:35]\n"
"ds_read_b64_tr_b16 v[208:209], v7 offset:512\n"
"ds_read_b64_tr_b16 v[210:211], v7 offset:4608\n"
"v_mfma_f32_16x16x32_bf16 a[40:43], v[192:195], v[104:107], a[40:43]\n"
"ds_read_b64_tr_b16 v[212:213], v8 offset:512\n"
"ds_read_b64_tr_b16 v[214:215], v8 offset:4608\n"
"v_mfma_f32_16x16x32_bf16 a[4:7], v[196:199], v[64:67], a[4:7]\n"
"ds_read_b64_tr_b16 v[216:217], v7 offset:768\n"
"ds_read_b64_tr_b16 v[218:219], v7 offset:4864\n"
"v_mfma_f32_16x16x32_bf16 a[12:15], v[196:199], v[72:75], a[12:15]\n"
"ds_read_b64_tr_b16 v[220:221], v8 offset:768\n"
"ds_read_b64_tr_b16 v[222:223], v8 offset:4864\n"
"v_mfma_f32_16x16x32_bf16 a[20:23], v[196:199], v[80:83], a[20:23]\n"
"ds_read_b128 v[224:227], v6 offset:0\n"
"ds_read_b128 v[228:231], v6 offset:256\n"
"v_mfma_f32_16x16x32_bf16 a[28:31], v[196:199], v[88:91], a[28:31]\n"
"ds_read_b128 v[232:235], v6 offset:512\n"
"ds_read_b128 v[236:239], v6 offset:768\n"
"v_mfma_f32_16x16x32_bf16 a[36:39], v[196:199], v[96:99], a[36:39]\n"
"v_max3_f32 v12, v16, v17, v18\n"
"v_max3_f32 v13, v19, v24, v25\n"
"v_mfma_f32_16x16x32_bf16 a[44:47], v[196:199], v[104:107], a[44:47]\n"
"buffer_load_dwordx4 v2, s[36:39], s64 offen offset:1024 nt lds\n"
"v_mfma_f32_16x16x32_bf16 a[48:51], v[200:203], v[64:67], a[48:51]\n"
"v_max3_f32 v14, v26, v27, v32\n"
"v_max3_f32 v12, v12, v33, v34\n"
"v_mfma_f32_16x16x32_bf16 a[56:59], v[200:203], v[72:75], a[56:59]\n"
"v_max3_f32 v13, v13, v35, v40\n"
"v_max3_f32 v14, v14, v41, v42\n"
"v_mfma_f32_16x16x32_bf16 a[64:67], v[200:203], v[80:83], a[64:67]\n"
"v_max3_f32 v12, v12, v43, v48\n"
"v_max3_f32 v13, v13, v49, v50\n"
"v_mfma_f32_16x16x32_bf16 a[72:75], v[200:203], v[88:91], a[72:75]\n"
"v_max3_f32 v14, v14, v51, v56\n"
"v_max3_f32 v12, v12, v57, v58\n"
"v_mfma_f32_16x16x32_bf16 a[80:83], v[200:203], v[96:99], a[80:83]\n"
"v_max3_f32 v13, v13, v59, v20\n"
"v_max3_f32 v14, v14, v21, v22\n"
"v_mfma_f32_16x16x32_bf16 a[88:91], v[200:203], v[104:107], a[88:91]\n"
"v_max3_f32 v12, v12, v23, v28\n"
"s_waitcnt lgkmcnt(3)\n"
"v_mfma_f32_16x16x32_bf16 a[52:55], v[204:207], v[64:67], a[52:55]\n"
"v_max3_f32 v13, v13, v29, v30\n"
"v_max3_f32 v14, v14, v31, v36\n"
"v_mfma_f32_16x16x32_bf16 a[60:63], v[204:207], v[72:75], a[60:63]\n"
"v_max3_f32 v12, v12, v37, v38\n"
"v_max3_f32 v13, v13, v39, v44\n"
"v_mfma_f32_16x16x32_bf16 a[68:71], v[204:207], v[80:83], a[68:71]\n"
"v_max3_f32 v14, v14, v45, v46\n"
"v_max3_f32 v12, v12, v47, v52\n"
"v_mfma_f32_16x16x32_bf16 a[76:79], v[204:207], v[88:91], a[76:79]\n"
"v_max3_f32 v13, v13, v53, v54\n"
"v_max3_f32 v14, v14, v55, v60\n"
"v_mfma_f32_16x16x32_bf16 a[84:87], v[204:207], v[96:99], a[84:87]\n"
"buffer_load_dwordx4 v1, s[36:39], s64 offen offset:2048 nt lds\n"
"v_mfma_f32_16x16x32_bf16 a[92:95], v[204:207], v[104:107], a[92:95]\n"
"v_max3_f32 v12, v12, v61, v62\n"
"v_max3_f32 v12, v12, v13, v14\n"
"v_mfma_f32_16x16x32_bf16 a[96:99], v[208:211], v[64:67], a[96:99]\n"
"v_max_f32 v12, v12, v63\n"
"v_cmp_lt_f32 vcc, s78, v12\n"
"v_mfma_f32_16x16x32_bf16 a[104:107], v[208:211], v[72:75], a[104:107]\n"
"s_cbranch_vccnz LT2_%=\n"
"LR2_%=:\n"
"ds_read_b128 v[192:195], v6 offset:4096\n"
"v_mfma_f32_16x16x32_bf16 a[112:115], v[208:211], v[80:83], a[112:115]\n"
"ds_read_b128 v[196:199], v6 offset:4352\n"
"ds_read_b128 v[200:203], v6 offset:4608\n"
"v_mfma_f32_16x16x32_bf16 a[120:123], v[208:211], v[88:91], a[120:123]\n"
"ds_read_b128 v[204:207], v6 offset:4864\n"
"v_add_u32 v9, 0xffff0000, v9\n"
"v_mfma_f32_16x16x32_bf16 a[128:131], v[208:211], v[96:99], a[128:131]\n"
"v_exp_f32 v16, v16\n"
"v_mfma_f32_16x16x32_bf16 a[136:139], v[208:211], v[104:107], a[136:139]\n"
"v_exp_f32 v17, v17\n"
"v_mfma_f32_16x16x32_bf16 a[100:103], v[212:215], v[64:67], a[100:103]\n"
"v_exp_f32 v18, v18\n"
"v_mfma_f32_16x16x32_bf16 a[108:111], v[212:215], v[72:75], a[108:111]\n"
"v_exp_f32 v19, v19\n"
"v_mfma_f32_16x16x32_bf16 a[116:119], v[212:215], v[80:83], a[116:119]\n"
"v_exp_f32 v20, v20\n"
"v_mfma_f32_16x16x32_bf16 a[124:127], v[212:215], v[88:91], a[124:127]\n"
"buffer_load_dwordx4 v2, s[36:39], s64 offen offset:3072 nt lds\n"
"v_mfma_f32_16x16x32_bf16 a[132:135], v[212:215], v[96:99], a[132:135]\n"
"v_exp_f32 v21, v21\n"
"v_mfma_f32_16x16x32_bf16 a[140:143], v[212:215], v[104:107], a[140:143]\n"
"v_exp_f32 v22, v22\n"
"v_mfma_f32_16x16x32_bf16 a[144:147], v[216:219], v[64:67], a[144:147]\n"
"v_exp_f32 v23, v23\n"
"v_mfma_f32_16x16x32_bf16 a[152:155], v[216:219], v[72:75], a[152:155]\n"
"v_cvt_pk_bf16_f32 v16, v16, v17\n"
"v_cvt_pk_bf16_f32 v17, v18, v19\n"
"v_mfma_f32_16x16x32_bf16 a[160:163], v[216:219], v[80:83], a[160:163]\n"
"v_exp_f32 v24, v24\n"
"v_mfma_f32_16x16x32_bf16 a[168:171], v[216:219], v[88:91], a[168:171]\n"
"v_exp_f32 v25, v25\n"
"v_mfma_f32_16x16x32_bf16 a[176:179], v[216:219], v[96:99], a[176:179]\n"
"v_cvt_pk_bf16_f32 v18, v20, v21\n"
"v_cvt_pk_bf16_f32 v19, v22, v23\n"
"v_mfma_f32_16x16x32_bf16 a[184:187], v[216:219], v[104:107], a[184:187]\n"
"v_exp_f32 v26, v26\n"
"v_mfma_f32_16x16x32_bf16 a[148:151], v[220:223], v[64:67], a[148:151]\n"
"v_exp_f32 v27, v27\n"
"v_mfma_f32_16x16x32_bf16 a[156:159], v[220:223], v[72:75], a[156:159]\n"
"v_exp_f32 v28, v28\n"
"v_mfma_f32_16x16x32_bf16 a[164:167], v[220:223], v[80:83], a[164:167]\n"
"v_exp_f32 v29, v29\n"
"v_mfma_f32_16x16x32_bf16 a[172:175], v[220:223], v[88:91], a[172:175]\n"
"v_exp_f32 v30, v30\n"
"v_mfma_f32_16x16x32_bf16 a[180:183], v[220:223], v[96:99], a[180:183]\n"
"v_exp_f32 v31, v31\n"
"v_mfma_f32_16x16x32_bf16 a[188:191], v[220:223], v[104:107], a[188:191]\n"
"v_cvt_pk_bf16_f32 v24, v24, v25\n"
"v_cvt_pk_bf16_f32 v25, v26, v27\n"
"v_mfma_f32_16x16x32_bf16 a[192:195], v[240:243], v[64:67], a[192:195]\n"
"v_exp_f32 v32, v32\n"
"v_mfma_f32_16x16x32_bf16 a[196:199], v[240:243], v[72:75], a[196:199]\n"
"v_exp_f32 v33, v33\n"
"v_mfma_f32_16x16x32_bf16 a[200:203], v[240:243], v[80:83], a[200:203]\n"
"s_add_u32 m0, s60, 114688\n"
"s_add_u32 s65, s56, 32768\n"
"v_mfma_f32_16x16x32_bf16 a[204:207], v[240:243], v[88:91], a[204:207]\n"
"buffer_load_dwordx4 v1, s[40:43], s65 offen offset:0 nt lds\n"
"v_mfma_f32_16x16x32_bf16 a[208:211], v[240:243], v[96:99], a[208:211]\n"
"v_cvt_pk_bf16_f32 v26, v28, v29\n"
"v_cvt_pk_bf16_f32 v27, v30, v31\n"
"v_mfma_f32_16x16x32_bf16 a[212:215], v[240:243], v[104:107], a[212:215]\n"
"v_cvt_pk_bf16_f32 v32, v32, v33\n"
"v_mfma_f32_16x16x32_bf16 v[64:67], v[224:227], v[136:139], v[112:115]\n"
"s_cmp_lg_u32 s63, 0\n"
"s_cbranch_scc1 LT3_%=\n"
"LR3_%=:\n"
"s_waitcnt lgkmcnt(0)\n"
"v_mfma_f32_16x16x32_bf16 v[64:67], v[228:231], v[160:163], v[64:67]\n"
"v_exp_f32 v34, v34\n"
"v_mfma_f32_16x16x32_bf16 v[64:67], v[232:235], v[184:187], v[64:67]\n"
"v_exp_f32 v35, v35\n"
"v_mfma_f32_16x16x32_bf16 v[64:67], v[236:239], v[188:191], v[64:67]\n"
"v_exp_f32 v36, v36\n"
"v_mfma_f32_16x16x32_bf16 v[72:75], v[224:227], v[140:143], v[116:119]\n"
"v_exp_f32 v37, v37\n"
"v_mfma_f32_16x16x32_bf16 v[72:75], v[228:231], v[164:167], v[72:75]\n"
"v_exp_f32 v38, v38\n"
"v_mfma_f32_16x16x32_bf16 v[72:75], v[232:235], a[216:219], v[72:75]\n"
"v_exp_f32 v39, v39\n"
"v_mfma_f32_16x16x32_bf16 v[72:75], v[236:239], a[236:239], v[72:75]\n"
"v_cvt_pk_bf16_f32 v33, v34, v35\n"
"v_cvt_pk_bf16_f32 v34, v36, v37\n"
"v_mfma_f32_16x16x32_bf16 v[80:83], v[224:227], v[144:147], v[120:123]\n"
"v_exp_f32 v40, v40\n"
"v_mfma_f32_16x16x32_bf16 v[80:83], v[228:231], v[168:171], v[80:83]\n"
"v_exp_f32 v41, v41\n"
"v_mfma_f32_16x16x32_bf16 v[80:83], v[232:235], a[220:223], v[80:83]\n"
"v_cvt_pk_bf16_f32 v35, v38, v39\n"
"v_cvt_pk_bf16_f32 v40, v40, v41\n"
"v_mfma_f32_16x16x32_bf16 v[80:83], v[236:239], a[240:243], v[80:83]\n"
"v_exp_f32 v42, v42\n"
"v_mfma_f32_16x16x32_bf16 v[88:91], v[224:227], v[148:151], v[124:127]\n"
"v_exp_f32 v43, v43\n"
"v_mfma_f32_16x16x32_bf16 v[88:91], v[228:231], v[172:175], v[88:91]\n"
"buffer_load_dwordx4 v2, s[40:43], s65 offen offset:1024 nt lds\n"
"v_mfma_f32_16x16x32_bf16 v[88:91], v[232:235], a[224:227], v[88:91]\n"
"s_waitcnt vmcnt(10)\n"
"s_barrier\n"
"v_mfma_f32_16x16x32_bf16 v[88:91], v[236:239], a[244:247], v[88:91]\n"
"ds_read_b64_tr_b16 v[208:209], v7 offset:32768\n"
"ds_read_b64_tr_b16 v[210:211], v7 offset:36864\n"
"v_mfma_f32_16x16x32_bf16 v[96:99], v[224:227], v[152:155], v[128:131]\n"
"ds_read_b64_tr_b16 v[212:213], v8 offset:32768\n"
"ds_read_b64_tr_b16 v[214:215], v8 offset:36864\n"
"v_mfma_f32_16x16x32_bf16 v[96:99], v[228:231], v[176:179], v[96:99]\n"
"v_exp_f32 v44, v44\n"
"v_mfma_f32_16x16x32_bf16 v[96:99], v[232:235], a[228:231], v[96:99]\n"
"v_exp_f32 v45, v45\n"
"v_mfma_f32_16x16x32_bf16 v[96:99], v[236:239], a[248:251], v[96:99]\n"
"v_exp_f32 v46, v46\n"
"v_mfma_f32_16x16x32_bf16 v[104:107], v[224:227], v[156:159], v[132:135]\n"
"v_exp_f32 v47, v47\n"
"v_mfma_f32_16x16x32_bf16 v[104:107], v[228:231], v[180:183], v[104:107]\n"
"v_cvt_pk_bf16_f32 v41, v42, v43\n"
"v_cvt_pk_bf16_f32 v42, v44, v45\n"
"v_mfma_f32_16x16x32_bf16 v[104:107], v[232:235], a[232:235], v[104:107]\n"
"v_exp_f32 v48, v48\n"
"v_mfma_f32_16x16x32_bf16 v[104:107], v[236:239], a[252:255], v[104:107]\n"
"v_exp_f32 v49, v49\n"
"v_mfma_f32_16x16x32_bf16 v[68:71], v[192:195], v[136:139], v[112:115]\n"
"v_cvt_pk_bf16_f32 v43, v46, v47\n"
"ds_read_b64_tr_b16 v[216:217], v7 offset:33024\n"
"v_mfma_f32_16x16x32_bf16 v[68:71], v[196:199], v[160:163], v[68:71]\n"
"v_exp_f32 v50, v50\n"
"v_mfma_f32_16x16x32_bf16 v[68:71], v[200:203], v[184:187], v[68:71]\n"
"buffer_load_dwordx4 v1, s[40:43], s65 offen offset:2048 nt lds\n"
"v_mfma_f32_16x16x32_bf16 v[68:71], v[204:207], v[188:191], v[68:71]\n"
"v_exp_f32 v51, v51\n"
"v_mfma_f32_16x16x32_bf16 v[76:79], v[192:195], v[140:143], v[116:119]\n"
"v_exp_f32 v52, v52\n"
"v_mfma_f32_16x16x32_bf16 v[76:79], v[196:199], v[164:167], v[76:79]\n"
"v_exp_f32 v53, v53\n"
"v_mfma_f32_16x16x32_bf16 v[76:79], v[200:203], a[216:219], v[76:79]\n"
"v_exp_f32 v54, v54\n"
"v_mfma_f32_16x16x32_bf16 v[76:79], v[204:207], a[236:239], v[76:79]\n"
"v_exp_f32 v55, v55\n"
"v_mfma_f32_16x16x32_bf16 v[84:87], v[192:195], v[144:147], v[120:123]\n"
"ds_read_b64_tr_b16 v[218:219], v7 offset:37120\n"
"v_cvt_pk_bf16_f32 v48, v48, v49\n"
"v_mfma_f32_16x16x32_bf16 v[84:87], v[196:199], v[168:171], v[84:87]\n"
"v_cvt_pk_bf16_f32 v49, v50, v51\n"
"v_cvt_pk_bf16_f32 v50, v52, v53\n"
"v_mfma_f32_16x16x32_bf16 v[84:87], v[200:203], a[220:223], v[84:87]\n"
"v_exp_f32 v56, v56\n"
"v_mfma_f32_16x16x32_bf16 v[84:87], v[204:207], a[240:243], v[84:87]\n"
"v_exp_f32 v57, v57\n"
"v_mfma_f32_16x16x32_bf16 v[92:95], v[192:195], v[148:151], v[124:127]\n"
"v_cvt_pk_bf16_f32 v51, v54, v55\n"
"v_cvt_pk_bf16_f32 v56, v56, v57\n"
"v_mfma_f32_16x16x32_bf16 v[92:95], v[196:199], v[172:175], v[92:95]\n"
"buffer_load_dwordx4 v2, s[40:43], s65 offen offset:3072 nt lds\n"
"v_mfma_f32_16x16x32_bf16 v[92:95], v[200:203], a[224:227], v[92:95]\n"
"v_exp_f32 v58, v58\n"
"v_mfma_f32_16x16x32_bf16 v[92:95], v[204:207], a[244:247], v[92:95]\n"
"v_exp_f32 v59, v59\n"
"v_mfma_f32_16x16x32_bf16 v[100:103], v[192:195], v[152:155], v[128:131]\n"
"v_exp_f32 v60, v60\n"
"v_mfma_f32_16x16x32_bf16 v[100:103], v[196:199], v[176:179], v[100:103]\n"
"v_exp_f32 v61, v61\n"
"v_mfma_f32_16x16x32_bf16 v[100:103], v[200:203], a[228:231], v[100:103]\n"
"v_exp_f32 v62, v62\n"
"v_mfma_f32_16x16x32_bf16 v[100:103], v[204:207], a[248:251], v[100:103]\n"
"v_exp_f32 v63, v63\n"
"v_mfma_f32_16x16x32_bf16 v[108:111], v[192:195], v[156:159], v[132:135]\n"
"v_cvt_pk_bf16_f32 v57, v58, v59\n"
"v_cvt_pk_bf16_f32 v58, v60, v61\n"
"v_mfma_f32_16x16x32_bf16 v[108:111], v[196:199], v[180:183], v[108:111]\n"
"v_cvt_pk_bf16_f32 v59, v62, v63\n"
"ds_read_b64_tr_b16 v[220:221], v8 offset:33024\n"
"v_mfma_f32_16x16x32_bf16 v[108:111], v[200:203], a[232:235], v[108:111]\n"
"ds_read_b64_tr_b16 v[222:223], v8 offset:37120\n"
"ds_read_b64_tr_b16 v[224:225], v7 offset:33280\n"
"v_mfma_f32_16x16x32_bf16 v[108:111], v[204:207], a[252:255], v[108:111]\n"
"ds_read_b64_tr_b16 v[226:227], v7 offset:37376\n"
"s_waitcnt lgkmcnt(4)\n"
"v_mfma_f32_16x16x32_bf16 a[0:3], v[208:211], v[16:19], a[0:3]\n"
"s_add_u32 m0, s60, 98304\n"
"s_add_u32 s66, s56, 49152\n"
"v_mfma_f32_16x16x32_bf16 a[8:11], v[208:211], v[24:27], a[8:11]\n"
"buffer_load_dwordx4 v1, s[36:39], s66 offen offset:0 nt lds\n"
"v_mfma_f32_16x16x32_bf16 a[16:19], v[208:211], v[32:35], a[16:19]\n"
"ds_read_b64_tr_b16 v[228:229], v8 offset:33280\n"
"ds_read_b64_tr_b16 v[230:231], v8 offset:37376\n"
"v_mfma_f32_16x16x32_bf16 a[24:27], v[208:211], v[40:43], a[24:27]\n"
"ds_read_b64_tr_b16 v[232:233], v7 offset:33536\n"
"ds_read_b64_tr_b16 v[234:235], v7 offset:37632\n"
"v_mfma_f32_16x16x32_bf16 a[32:35], v[208:211], v[48:51], a[32:35]\n"
"ds_read_b64_tr_b16 v[236:237], v8 offset:33536\n"
"ds_read_b64_tr_b16 v[238:239], v8 offset:37632\n"
"v_mfma_f32_16x16x32_bf16 a[40:43], v[208:211], v[56:59], a[40:43]\n"
"ds_read_b128 v[192:195], v6 offset:32768\n"
"ds_read_b128 v[196:199], v6 offset:33024\n"
"v_mfma_f32_16x16x32_bf16 a[4:7], v[212:215], v[16:19], a[4:7]\n"
"ds_read_b128 v[200:203], v6 offset:33280\n"
"ds_read_b128 v[204:207], v6 offset:33536\n"
"v_mfma_f32_16x16x32_bf16 a[12:15], v[212:215], v[24:27], a[12:15]\n"
"v_max3_f32 v12, v64, v65, v66\n"
"v_max3_f32 v13, v67, v72, v73\n"
"v_mfma_f32_16x16x32_bf16 a[20:23], v[212:215], v[32:35], a[20:23]\n"
"v_max3_f32 v14, v74, v75, v80\n"
"v_max3_f32 v12, v12, v81, v82\n"
"v_mfma_f32_16x16x32_bf16 a[28:31], v[212:215], v[40:43], a[28:31]\n"
"v_max3_f32 v13, v13, v83, v88\n"
"v_max3_f32 v14, v14, v89, v90\n"
"v_mfma_f32_16x16x32_bf16 a[36:39], v[212:215], v[48:51], a[36:39]\n"
"v_max3_f32 v12, v12, v91, v96\n"
"v_max3_f32 v13, v13, v97, v98\n"
"v_mfma_f32_16x16x32_bf16 a[44:47], v[212:215], v[56:59], a[44:47]\n"
"buffer_load_dwordx4 v2, s[36:39], s66 offen offset:1024 nt lds\n"
"v_mfma_f32_16x16x32_bf16 a[48:51], v[216:219], v[16:19], a[48:51]\n"
"v_max3_f32 v14, v14, v99, v104\n"
"v_max3_f32 v12, v12, v105, v106\n"
"v_mfma_f32_16x16x32_bf16 a[56:59], v[216:219], v[24:27], a[56:59]\n"
"v_max3_f32 v13, v13, v107, v68\n"
"v_max3_f32 v14, v14, v69, v70\n"
"v_mfma_f32_16x16x32_bf16 a[64:67], v[216:219], v[32:35], a[64:67]\n"
"v_max3_f32 v12, v12, v71, v76\n"
"v_max3_f32 v13, v13, v77, v78\n"
"v_mfma_f32_16x16x32_bf16 a[72:75], v[216:219], v[40:43], a[72:75]\n"
"v_max3_f32 v14, v14, v79, v84\n"
"v_max3_f32 v12, v12, v85, v86\n"
"v_mfma_f32_16x16x32_bf16 a[80:83], v[216:219], v[48:51], a[80:83]\n"
"v_max3_f32 v13, v13, v87, v92\n"
"v_max3_f32 v14, v14, v93, v94\n"
"v_mfma_f32_16x16x32_bf16 a[88:91], v[216:219], v[56:59], a[88:91]\n"
"v_max3_f32 v12, v12, v95, v100\n"
"s_waitcnt lgkmcnt(0)\n"
"v_mfma_f32_16x16x32_bf16 a[52:55], v[220:223], v[16:19], a[52:55]\n"
"v_max3_f32 v13, v13, v101, v102\n"
"v_max3_f32 v14, v14, v103, v108\n"
"v_mfma_f32_16x16x32_bf16 a[60:63], v[220:223], v[24:27], a[60:63]\n"
"v_max3_f32 v12, v12, v109, v110\n"
"v_max3_f32 v12, v12, v13, v14\n"
"v_mfma_f32_16x16x32_bf16 a[68:71], v[220:223], v[32:35], a[68:71]\n"
"v_max_f32 v12, v12, v111\n"
"v_cmp_lt_f32 vcc, s78, v12\n"
"v_mfma_f32_16x16x32_bf16 a[76:79], v[220:223], v[40:43], a[76:79]\n"
"s_cbranch_vccnz LT4_%=\n"
"LR4_%=:\n"
"ds_read_b128 v[208:211], v6 offset:36864\n"
"v_mfma_f32_16x16x32_bf16 a[84:87], v[220:223], v[48:51], a[84:87]\n"
"buffer_load_dwordx4 v1, s[36:39], s66 offen offset:2048 nt lds\n"
"v_mfma_f32_16x16x32_bf16 a[92:95], v[220:223], v[56:59], a[92:95]\n"
"ds_read_b128 v[212:215], v6 offset:37120\n"
"ds_read_b128 v[216:219], v6 offset:37376\n"
"v_mfma_f32_16x16x32_bf16 a[96:99], v[224:227], v[16:19], a[96:99]\n"
"ds_read_b128 v[220:223], v6 offset:37632\n"
"v_add_u32 v7, 0x10000, v7\n"
"v_mfma_f32_16x16x32_bf16 a[104:107], v[224:227], v[24:27], a[104:107]\n"
"v_add_u32 v8, 0x10000, v8\n"
"v_add_u32 v6, 0x10000, v6\n"
"v_mfma_f32_16x16x32_bf16 a[112:115], v[224:227], v[32:35], a[112:115]\n"
"v_exp_f32 v64, v64\n"
"v_mfma_f32_16x16x32_bf16 a[120:123], v[224:227], v[40:43], a[120:123]\n"
"v_exp_f32 v65, v65\n"
"v_mfma_f32_16x16x32_bf16 a[128:131], v[224:227], v[48:51], a[128:131]\n"
"v_exp_f32 v66, v66\n"
"v_mfma_f32_16x16x32_bf16 a[136:139], v[224:227], v[56:59], a[136:139]\n"
"v_exp_f32 v67, v67\n"
"v_mfma_f32_16x16x32_bf16 a[100:103], v[228:231], v[16:19], a[100:103]\n"
"v_exp_f32 v68, v68\n"
"v_mfma_f32_16x16x32_bf16 a[108:111], v[228:231], v[24:27], a[108:111]\n"
"v_exp_f32 v69, v69\n"
"v_mfma_f32_16x16x32_bf16 a[116:119], v[228:231], v[32:35], a[116:119]\n"
"v_exp_f32 v70, v70\n"
"v_mfma_f32_16x16x32_bf16 a[124:127], v[228:231], v[40:43], a[124:127]\n"
"buffer_load_dwordx4 v2, s[36:39], s66 offen offset:3072 nt lds\n"
"v_mfma_f32_16x16x32_bf16 a[132:135], v[228:231], v[48:51], a[132:135]\n"
"v_exp_f32 v71, v71\n"
"v_mfma_f32_16x16x32_bf16 a[140:143], v[228:231], v[56:59], a[140:143]\n"
"v_cvt_pk_bf16_f32 v64, v64, v65\n"
"v_cvt_pk_bf16_f32 v65, v66, v67\n"
"v_mfma_f32_16x16x32_bf16 a[144:147], v[232:235], v[16:19], a[144:147]\n"
"v_exp_f32 v72, v72\n"
"v_mfma_f32_16x16x32_bf16 a[152:155], v[232:235], v[24:27], a[152:155]\n"
"v_exp_f32 v73, v73\n"
"v_mfma_f32_16x16x32_bf16 a[160:163], v[232:235], v[32:35], a[160:163]\n"
"v_cvt_pk_bf16_f32 v66, v68, v69\n"
"v_cvt_pk_bf16_f32 v67, v70, v71\n"
"v_mfma_f32_16x16x32_bf16 a[168:171], v[232:235], v[40:43], a[168:171]\n"
"v_exp_f32 v74, v74\n"
"v_mfma_f32_16x16x32_bf16 a[176:179], v[232:235], v[48:51], a[176:179]\n"
"v_exp_f32 v75, v75\n"
"v_mfma_f32_16x16x32_bf16 a[184:187], v[232:235], v[56:59], a[184:187]\n"
"v_exp_f32 v76, v76\n"
"v_mfma_f32_16x16x32_bf16 a[148:151], v[236:239], v[16:19], a[148:151]\n"
"v_exp_f32 v77, v77\n"
"v_mfma_f32_16x16x32_bf16 a[156:159], v[236:239], v[24:27], a[156:159]\n"
"v_exp_f32 v78, v78\n"
"v_mfma_f32_16x16x32_bf16 a[164:167], v[236:239], v[32:35], a[164:167]\n"
"v_exp_f32 v79, v79\n"
"v_mfma_f32_16x16x32_bf16 a[172:175], v[236:239], v[40:43], a[172:175]\n"
"v_cvt_pk_bf16_f32 v72, v72, v73\n"
"v_cvt_pk_bf16_f32 v73, v74, v75\n"
"v_mfma_f32_16x16x32_bf16 a[180:183], v[236:239], v[48:51], a[180:183]\n"
"v_exp_f32 v80, v80\n"
"v_mfma_f32_16x16x32_bf16 a[188:191], v[236:239], v[56:59], a[188:191]\n"
"v_exp_f32 v81, v81\n"
"v_mfma_f32_16x16x32_bf16 a[192:195], v[240:243], v[16:19], a[192:195]\n"
"v_cvt_pk_bf16_f32 v74, v76, v77\n"
"v_cvt_pk_bf16_f32 v75, v78, v79\n"
"v_mfma_f32_16x16x32_bf16 a[196:199], v[240:243], v[24:27], a[196:199]\n"
"v_exp_f32 v82, v82\n"
"v_mfma_f32_16x16x32_bf16 a[200:203], v[240:243], v[32:35], a[200:203]\n"
"s_add_u32 m0, s60, 147456\n"
"s_add_u32 s67, s56, 40960\n"
"v_mfma_f32_16x16x32_bf16 a[204:207], v[240:243], v[40:43], a[204:207]\n"
"buffer_load_dwordx4 v1, s[40:43], s67 offen offset:0 nt lds\n"
"v_mfma_f32_16x16x32_bf16 a[208:211], v[240:243], v[48:51], a[208:211]\n"
"v_exp_f32 v83, v83\n"
"v_mfma_f32_16x16x32_bf16 a[212:215], v[240:243], v[56:59], a[212:215]\n"
"s_cmp_lg_u32 s63, 0\n"
"s_cbranch_scc1 LT5_%=\n"
"LR5_%=:\n"
"v_mfma_f32_16x16x32_bf16 v[16:19], v[192:195], v[136:139], v[112:115]\n"
"v_exp_f32 v84, v84\n"
"v_mfma_f32_16x16x32_bf16 v[16:19], v[196:199], v[160:163], v[16:19]\n"
"v_exp_f32 v85, v85\n"
"v_mfma_f32_16x16x32_bf16 v[16:19], v[200:203], v[184:187], v[16:19]\n"
"v_exp_f32 v86, v86\n"
"v_mfma_f32_16x16x32_bf16 v[16:19], v[204:207], v[188:191], v[16:19]\n"
"v_exp_f32 v87, v87\n"
"v_mfma_f32_16x16x32_bf16 v[24:27], v[192:195], v[140:143], v[116:119]\n"
"v_cvt_pk_bf16_f32 v80, v80, v81\n"
"v_cvt_pk_bf16_f32 v81, v82, v83\n"
"v_mfma_f32_16x16x32_bf16 v[24:27], v[196:199], v[164:167], v[24:27]\n"
"v_exp_f32 v88, v88\n"
"v_mfma_f32_16x16x32_bf16 v[24:27], v[200:203], a[216:219], v[24:27]\n"
"v_exp_f32 v89, v89\n"
"v_mfma_f32_16x16x32_bf16 v[24:27], v[204:207], a[236:239], v[24:27]\n"
"v_cvt_pk_bf16_f32 v82, v84, v85\n"
"v_cvt_pk_bf16_f32 v83, v86, v87\n"
"v_mfma_f32_16x16x32_bf16 v[32:35], v[192:195], v[144:147], v[120:123]\n"
"v_exp_f32 v90, v90\n"
"v_mfma_f32_16x16x32_bf16 v[32:35], v[196:199], v[168:171], v[32:35]\n"
"v_exp_f32 v91, v91\n"
"v_mfma_f32_16x16x32_bf16 v[32:35], v[200:203], a[220:223], v[32:35]\n"
"v_exp_f32 v92, v92\n"
"v_mfma_f32_16x16x32_bf16 v[32:35], v[204:207], a[240:243], v[32:35]\n"
"v_exp_f32 v93, v93\n"
"v_mfma_f32_16x16x32_bf16 v[40:43], v[192:195], v[148:151], v[124:127]\n"
"v_exp_f32 v94, v94\n"
"v_mfma_f32_16x16x32_bf16 v[40:43], v[196:199], v[172:175], v[40:43]\n"
"buffer_load_dwordx4 v2, s[40:43], s67 offen offset:1024 nt lds\n"
"v_mfma_f32_16x16x32_bf16 v[40:43], v[200:203], a[224:227], v[40:43]\n"
"s_waitcnt vmcnt(10) lgkmcnt(0)\n"
"s_barrier\n"
"v_mfma_f32_16x16x32_bf16 v[40:43], v[204:207], a[244:247], v[40:43]\n"
"ds_read_b64_tr_b16 v[224:225], v7 offset:0\n"
"ds_read_b64_tr_b16 v[226:227], v7 offset:4096\n"
"v_mfma_f32_16x16x32_bf16 v[48:51], v[192:195], v[152:155], v[128:131]\n"
"ds_read_b64_tr_b16 v[228:229], v8 offset:0\n"
"ds_read_b64_tr_b16 v[230:231], v8 offset:4096\n"
"v_mfma_f32_16x16x32_bf16 v[48:51], v[196:199], v[176:179], v[48:51]\n"
"v_exp_f32 v95, v95\n"
"v_mfma_f32_16x16x32_bf16 v[48:51], v[200:203], a[228:231], v[48:51]\n"
"v_cvt_pk_bf16_f32 v88, v88, v89\n"
"v_cvt_pk_bf16_f32 v89, v90, v91\n"
"v_mfma_f32_16x16x32_bf16 v[48:51], v[204:207], a[248:251], v[48:51]\n"
"v_exp_f32 v96, v96\n"
"v_mfma_f32_16x16x32_bf16 v[56:59], v[192:195], v[156:159], v[132:135]\n"
"v_exp_f32 v97, v97\n"
"v_mfma_f32_16x16x32_bf16 v[56:59], v[196:199], v[180:183], v[56:59]\n"
"v_cvt_pk_bf16_f32 v90, v92, v93\n"
"v_cvt_pk_bf16_f32 v91, v94, v95\n"
"v_mfma_f32_16x16x32_bf16 v[56:59], v[200:203], a[232:235], v[56:59]\n"
"v_exp_f32 v98, v98\n"
"v_mfma_f32_16x16x32_bf16 v[56:59], v[204:207], a[252:255], v[56:59]\n"
"v_exp_f32 v99, v99\n"
"v_mfma_f32_16x16x32_bf16 v[20:23], v[208:211], v[136:139], v[112:115]\n"
"v_exp_f32 v100, v100\n"
"v_mfma_f32_16x16x32_bf16 v[20:23], v[212:215], v[160:163], v[20:23]\n"
"v_exp_f32 v101, v101\n"
"v_mfma_f32_16x16x32_bf16 v[20:23], v[216:219], v[184:187], v[20:23]\n"
"buffer_load_dwordx4 v1, s[40:43], s67 offen offset:2048 nt lds\n"
"v_mfma_f32_16x16x32_bf16 v[20:23], v[220:223], v[188:191], v[20:23]\n"
"v_exp_f32 v102, v102\n"
"v_mfma_f32_16x16x32_bf16 v[28:31], v[208:211], v[140:143], v[116:119]\n"
"v_exp_f32 v103, v103\n"
"v_mfma_f32_16x16x32_bf16 v[28:31], v[212:215], v[164:167], v[28:31]\n"
"ds_read_b64_tr_b16 v[232:233], v7 offset:256\n"
"ds_read_b64_tr_b16 v[234:235], v7 offset:4352\n"
"v_mfma_f32_16x16x32_bf16 v[28:31], v[216:219], a[216:219], v[28:31]\n"
"v_cvt_pk_bf16_f32 v96, v96, v97\n"
"v_cvt_pk_bf16_f32 v97, v98, v99\n"
"v_mfma_f32_16x16x32_bf16 v[28:31], v[220:223], a[236:239], v[28:31]\n"
"v_exp_f32 v104, v104\n"
"v_mfma_f32_16x16x32_bf16 v[36:39], v[208:211], v[144:147], v[120:123]\n"
"v_exp_f32 v105, v105\n"
"v_mfma_f32_16x16x32_bf16 v[36:39], v[212:215], v[168:171], v[36:39]\n"
"v_cvt_pk_bf16_f32 v98, v100, v101\n"
"v_cvt_pk_bf16_f32 v99, v102, v103\n"
"v_mfma_f32_16x16x32_bf16 v[36:39], v[216:219], a[220:223], v[36:39]\n"
"v_exp_f32 v106, v106\n"
"v_mfma_f32_16x16x32_bf16 v[36:39], v[220:223], a[240:243], v[36:39]\n"
"v_exp_f32 v107, v107\n"
"v_mfma_f32_16x16x32_bf16 v[44:47], v[208:211], v[148:151], v[124:127]\n"
"v_exp_f32 v108, v108\n"
"v_mfma_f32_16x16x32_bf16 v[44:47], v[212:215], v[172:175], v[44:47]\n"
"buffer_load_dwordx4 v2, s[40:43], s67 offen offset:3072 nt lds\n"
"v_mfma_f32_16x16x32_bf16 v[44:47], v[216:219], a[224:227], v[44:47]\n"
"v_exp_f32 v109, v109\n"
"v_mfma_f32_16x16x32_bf16 v[44:47], v[220:223], a[244:247], v[44:47]\n"
"v_exp_f32 v110, v110\n"
"v_mfma_f32_16x16x32_bf16 v[52:55], v[208:211], v[152:155], v[128:131]\n"
"v_exp_f32 v111, v111\n"
"v_mfma_f32_16x16x32_bf16 v[52:55], v[212:215], v[176:179], v[52:55]\n"
"v_cvt_pk_bf16_f32 v104, v104, v105\n"
"v_cvt_pk_bf16_f32 v105, v106, v107\n"
"v_mfma_f32_16x16x32_bf16 v[52:55], v[216:219], a[228:231], v[52:55]\n"
"v_cvt_pk_bf16_f32 v106, v108, v109\n"
"v_cvt_pk_bf16_f32 v107, v110, v111\n"
"v_mfma_f32_16x16x32_bf16 v[52:55], v[220:223], a[248:251], v[52:55]\n"
"ds_read_b64_tr_b16 v[236:237], v8 offset:256\n"
"ds_read_b64_tr_b16 v[238:239], v8 offset:4352\n"
"v_mfma_f32_16x16x32_bf16 v[60:63], v[208:211], v[156:159], v[132:135]\n"
"ds_read_b64_tr_b16 v[192:193], v7 offset:512\n"
"ds_read_b64_tr_b16 v[194:195], v7 offset:4608\n"
"v_mfma_f32_16x16x32_bf16 v[60:63], v[212:215], v[180:183], v[60:63]\n"
"ds_read_b64_tr_b16 v[196:197], v8 offset:512\n"
"ds_read_b64_tr_b16 v[198:199], v8 offset:4608\n"
"v_mfma_f32_16x16x32_bf16 v[60:63], v[216:219], a[232:235], v[60:63]\n"
"ds_read_b64_tr_b16 v[200:201], v7 offset:768\n"
"ds_read_b64_tr_b16 v[202:203], v7 offset:4864\n"
"v_mfma_f32_16x16x32_bf16 v[60:63], v[220:223], a[252:255], v[60:63]\n"
"ds_read_b64_tr_b16 v[204:205], v8 offset:768\n"
"s_waitcnt lgkmcnt(9)\n"
"v_mfma_f32_16x16x32_bf16 a[0:3], v[224:227], v[64:67], a[0:3]\n"
"s_add_u32 m0, s60, 131072\n"
"s_add_u32 s68, s56, 57344\n"
"v_mfma_f32_16x16x32_bf16 a[8:11], v[224:227], v[72:75], a[8:11]\n"
"buffer_load_dwordx4 v1, s[36:39], s68 offen offset:0 nt lds\n"
"v_mfma_f32_16x16x32_bf16 a[16:19], v[224:227], v[80:83], a[16:19]\n"
"ds_read_b64_tr_b16 v[206:207], v8 offset:4864\n"
"ds_read_b128 v[208:211], v6 offset:0\n"
"v_mfma_f32_16x16x32_bf16 a[24:27], v[224:227], v[88:91], a[24:27]\n"
"ds_read_b128 v[212:215], v6 offset:256\n"
"ds_read_b128 v[216:219], v6 offset:512\n"
"v_mfma_f32_16x16x32_bf16 a[32:35], v[224:227], v[96:99], a[32:35]\n"
"ds_read_b128 v[220:223], v6 offset:768\n"
"v_max3_f32 v12, v16, v17, v18\n"
"v_mfma_f32_16x16x32_bf16 a[40:43], v[224:227], v[104:107], a[40:43]\n"
"v_max3_f32 v13, v19, v24, v25\n"
"v_max3_f32 v14, v26, v27, v32\n"
"v_mfma_f32_16x16x32_bf16 a[4:7], v[228:231], v[64:67], a[4:7]\n"
"v_max3_f32 v12, v12, v33, v34\n"
"v_max3_f32 v13, v13, v35, v40\n"
"v_mfma_f32_16x16x32_bf16 a[12:15], v[228:231], v[72:75], a[12:15]\n"
"v_max3_f32 v14, v14, v41, v42\n"
"v_max3_f32 v12, v12, v43, v48\n"
"v_mfma_f32_16x16x32_bf16 a[20:23], v[228:231], v[80:83], a[20:23]\n"
"v_max3_f32 v13, v13, v49, v50\n"
"v_max3_f32 v14, v14, v51, v56\n"
"v_mfma_f32_16x16x32_bf16 a[28:31], v[228:231], v[88:91], a[28:31]\n"
"v_max3_f32 v12, v12, v57, v58\n"
"v_max3_f32 v13, v13, v59, v20\n"
"v_mfma_f32_16x16x32_bf16 a[36:39], v[228:231], v[96:99], a[36:39]\n"
"v_max3_f32 v14, v14, v21, v22\n"
"v_max3_f32 v12, v12, v23, v28\n"
"v_mfma_f32_16x16x32_bf16 a[44:47], v[228:231], v[104:107], a[44:47]\n"
"buffer_load_dwordx4 v2, s[36:39], s68 offen offset:1024 nt lds\n"
"v_mfma_f32_16x16x32_bf16 a[48:51], v[232:235], v[64:67], a[48:51]\n"
"v_max3_f32 v13, v13, v29, v30\n"
"v_max3_f32 v14, v14, v31, v36\n"
"v_mfma_f32_16x16x32_bf16 a[56:59], v[232:235], v[72:75], a[56:59]\n"
"v_max3_f32 v12, v12, v37, v38\n"
"v_max3_f32 v13, v13, v39, v44\n"
"v_mfma_f32_16x16x32_bf16 a[64:67], v[232:235], v[80:83], a[64:67]\n"
"v_max3_f32 v14, v14, v45, v46\n"
"v_max3_f32 v12, v12, v47, v52\n"
"v_mfma_f32_16x16x32_bf16 a[72:75], v[232:235], v[88:91], a[72:75]\n"
"v_max3_f32 v13, v13, v53, v54\n"
"v_max3_f32 v14, v14, v55, v60\n"
"v_mfma_f32_16x16x32_bf16 a[80:83], v[232:235], v[96:99], a[80:83]\n"
"v_max3_f32 v12, v12, v61, v62\n"
"v_max3_f32 v12, v12, v13, v14\n"
"v_mfma_f32_16x16x32_bf16 a[88:91], v[232:235], v[104:107], a[88:91]\n"
"v_max_f32 v12, v12, v63\n"
"s_waitcnt lgkmcnt(0)\n"
"v_mfma_f32_16x16x32_bf16 a[52:55], v[236:239], v[64:67], a[52:55]\n"
"v_cmp_lt_f32 vcc, s78, v12\n"
"s_cbranch_vccnz LT6_%=\n"
"LR6_%=:\n"
"v_mfma_f32_16x16x32_bf16 a[60:63], v[236:239], v[72:75], a[60:63]\n"
"ds_read_b128 v[224:227], v6 offset:4096\n"
"ds_read_b128 v[228:231], v6 offset:4352\n"
"v_mfma_f32_16x16x32_bf16 a[68:71], v[236:239], v[80:83], a[68:71]\n"
"ds_read_b128 v[232:235], v6 offset:4608\n"
"v_mfma_f32_16x16x32_bf16 a[76:79], v[236:239], v[88:91], a[76:79]\n"
"v_exp_f32 v16, v16\n"
"v_mfma_f32_16x16x32_bf16 a[84:87], v[236:239], v[96:99], a[84:87]\n"
"buffer_load_dwordx4 v1, s[36:39], s68 offen offset:2048 nt lds\n"
"v_mfma_f32_16x16x32_bf16 a[92:95], v[236:239], v[104:107], a[92:95]\n"
"ds_read_b128 v[236:239], v6 offset:4864\n"
"v_mfma_f32_16x16x32_bf16 a[96:99], v[192:195], v[64:67], a[96:99]\n"
"v_exp_f32 v17, v17\n"
"v_mfma_f32_16x16x32_bf16 a[104:107], v[192:195], v[72:75], a[104:107]\n"
"v_exp_f32 v18, v18\n"
"v_mfma_f32_16x16x32_bf16 a[112:115], v[192:195], v[80:83], a[112:115]\n"
"v_exp_f32 v19, v19\n"
"v_mfma_f32_16x16x32_bf16 a[120:123], v[192:195], v[88:91], a[120:123]\n"
"v_exp_f32 v20, v20\n"
"v_mfma_f32_16x16x32_bf16 a[128:131], v[192:195], v[96:99], a[128:131]\n"
"v_exp_f32 v21, v21\n"
"v_mfma_f32_16x16x32_bf16 a[136:139], v[192:195], v[104:107], a[136:139]\n"
"v_exp_f32 v22, v22\n"
"v_mfma_f32_16x16x32_bf16 a[100:103], v[196:199], v[64:67], a[100:103]\n"
"v_exp_f32 v23, v23\n"
"v_mfma_f32_16x16x32_bf16 a[108:111], v[196:199], v[72:75], a[108:111]\n"
"v_cvt_pk_bf16_f32 v16, v16, v17\n"
"v_cvt_pk_bf16_f32 v17, v18, v19\n"
"v_mfma_f32_16x16x32_bf16 a[116:119], v[196:199], v[80:83], a[116:119]\n"
"v_exp_f32 v24, v24\n"
"v_mfma_f32_16x16x32_bf16 a[124:127], v[196:199], v[88:91], a[124:127]\n"
"buffer_load_dwordx4 v2, s[36:39], s68 offen offset:3072 nt lds\n"
"v_mfma_f32_16x16x32_bf16 a[132:135], v[196:199], v[96:99], a[132:135]\n"
"v_exp_f32 v25, v25\n"
"v_mfma_f32_16x16x32_bf16 a[140:143], v[196:199], v[104:107], a[140:143]\n"
"v_cvt_pk_bf16_f32 v18, v20, v21\n"
"v_cvt_pk_bf16_f32 v19, v22, v23\n"
"v_mfma_f32_16x16x32_bf16 a[144:147], v[200:203], v[64:67], a[144:147]\n"
"v_exp_f32 v26, v26\n"
"v_mfma_f32_16x16x32_bf16 a[152:155], v[200:203], v[72:75], a[152:155]\n"
"v_exp_f32 v27, v27\n"
"v_mfma_f32_16x16x32_bf16 a[160:163], v[200:203], v[80:83], a[160:163]\n"
"v_exp_f32 v28, v28\n"
"v_mfma_f32_16x16x32_bf16 a[168:171], v[200:203], v[88:91], a[168:171]\n"
"v_exp_f32 v29, v29\n"
"v_mfma_f32_16x16x32_bf16 a[176:179], v[200:203], v[96:99], a[176:179]\n"
"v_exp_f32 v30, v30\n"
"v_mfma_f32_16x16x32_bf16 a[184:187], v[200:203], v[104:107], a[184:187]\n"
"v_exp_f32 v31, v31\n"
"v_mfma_f32_16x16x32_bf16 a[148:151], v[204:207], v[64:67], a[148:151]\n"
"v_cvt_pk_bf16_f32 v24, v24, v25\n"
"v_cvt_pk_bf16_f32 v25, v26, v27\n"
"v_mfma_f32_16x16x32_bf16 a[156:159], v[204:207], v[72:75], a[156:159]\n"
"v_exp_f32 v32, v32\n"
"v_mfma_f32_16x16x32_bf16 a[164:167], v[204:207], v[80:83], a[164:167]\n"
"v_exp_f32 v33, v33\n"
"v_mfma_f32_16x16x32_bf16 a[172:175], v[204:207], v[88:91], a[172:175]\n"
"v_cvt_pk_bf16_f32 v26, v28, v29\n"
"v_cvt_pk_bf16_f32 v27, v30, v31\n"
"v_mfma_f32_16x16x32_bf16 a[180:183], v[204:207], v[96:99], a[180:183]\n"
"v_exp_f32 v34, v34\n"
"v_mfma_f32_16x16x32_bf16 a[188:191], v[204:207], v[104:107], a[188:191]\n"
"v_exp_f32 v35, v35\n"
"v_mfma_f32_16x16x32_bf16 a[192:195], v[240:243], v[64:67], a[192:195]\n"
"v_exp_f32 v36, v36\n"
"v_mfma_f32_16x16x32_bf16 a[196:199], v[240:243], v[72:75], a[196:199]\n"
"v_exp_f32 v37, v37\n"
"v_mfma_f32_16x16x32_bf16 a[200:203], v[240:243], v[80:83], a[200:203]\n"
"s_add_u32 m0, s60, 16384\n"
"s_add_u32 s69, s56, 49152\n"
"v_mfma_f32_16x16x32_bf16 a[204:207], v[240:243], v[88:91], a[204:207]\n"
"buffer_load_dwordx4 v1, s[40:43], s69 offen offset:0 nt lds\n"
"v_mfma_f32_16x16x32_bf16 a[208:211], v[240:243], v[96:99], a[208:211]\n"
"v_exp_f32 v38, v38\n"
"v_mfma_f32_16x16x32_bf16 a[212:215], v[240:243], v[104:107], a[212:215]\n"
"s_cmp_lg_u32 s63, 0\n"
"s_cbranch_scc1 LT7_%=\n"
"LR7_%=:\n"
"v_mfma_f32_16x16x32_bf16 v[64:67], v[208:211], v[136:139], v[112:115]\n"
"v_exp_f32 v39, v39\n"
"v_mfma_f32_16x16x32_bf16 v[64:67], v[212:215], v[160:163], v[64:67]\n"
"v_cvt_pk_bf16_f32 v32, v32, v33\n"
"v_cvt_pk_bf16_f32 v33, v34, v35\n"
"v_mfma_f32_16x16x32_bf16 v[64:67], v[216:219], v[184:187], v[64:67]\n"
"v_exp_f32 v40, v40\n"
"v_mfma_f32_16x16x32_bf16 v[64:67], v[220:223], v[188:191], v[64:67]\n"
"v_exp_f32 v41, v41\n"
"v_mfma_f32_16x16x32_bf16 v[72:75], v[208:211], v[140:143], v[116:119]\n"
"v_cvt_pk_bf16_f32 v34, v36, v37\n"
"v_cvt_pk_bf16_f32 v35, v38, v39\n"
"v_mfma_f32_16x16x32_bf16 v[72:75], v[212:215], v[164:167], v[72:75]\n"
"v_exp_f32 v42, v42\n"
"v_mfma_f32_16x16x32_bf16 v[72:75], v[216:219], a[216:219], v[72:75]\n"
"v_exp_f32 v43, v43\n"
"v_mfma_f32_16x16x32_bf16 v[72:75], v[220:223], a[236:239], v[72:75]\n"
"v_exp_f32 v44, v44\n"
"v_mfma_f32_16x16x32_bf16 v[80:83], v[208:211], v[144:147], v[120:123]\n"
"v_exp_f32 v45, v45\n"
"v_mfma_f32_16x16x32_bf16 v[80:83], v[212:215], v[168:171], v[80:83]\n"
"v_exp_f32 v46, v46\n"
"v_mfma_f32_16x16x32_bf16 v[80:83], v[216:219], a[220:223], v[80:83]\n"
"v_exp_f32 v47, v47\n"
"v_mfma_f32_16x16x32_bf16 v[80:83], v[220:223], a[240:243], v[80:83]\n"
"v_cvt_pk_bf16_f32 v40, v40, v41\n"
"v_cvt_pk_bf16_f32 v41, v42, v43\n"
"v_mfma_f32_16x16x32_bf16 v[88:91], v[208:211], v[148:151], v[124:127]\n"
"v_exp_f32 v48, v48\n"
"v_mfma_f32_16x16x32_bf16 v[88:91], v[212:215], v[172:175], v[88:91]\n"
"buffer_load_dwordx4 v2, s[40:43], s69 offen offset:1024 nt lds\n"
"v_mfma_f32_16x16x32_bf16 v[88:91], v[216:219], a[224:227], v[88:91]\n"
"s_waitcnt vmcnt(10) lgkmcnt(0)\n"
"s_barrier\n"
"v_mfma_f32_16x16x32_bf16 v[88:91], v[220:223], a[244:247], v[88:91]\n"
"ds_read_b64_tr_b16 v[192:193], v7 offset:32768\n"
"ds_read_b64_tr_b16 v[194:195], v7 offset:36864\n"
"v_mfma_f32_16x16x32_bf16 v[96:99], v[208:211], v[152:155], v[128:131]\n"
"ds_read_b64_tr_b16 v[196:197], v8 offset:32768\n"
"ds_read_b64_tr_b16 v[198:199], v8 offset:36864\n"
"v_mfma_f32_16x16x32_bf16 v[96:99], v[212:215], v[176:179], v[96:99]\n"
"v_exp_f32 v49, v49\n"
"v_mfma_f32_16x16x32_bf16 v[96:99], v[216:219], a[228:231], v[96:99]\n"
"v_cvt_pk_bf16_f32 v42, v44, v45\n"
"v_cvt_pk_bf16_f32 v43, v46, v47\n"
"v_mfma_f32_16x16x32_bf16 v[96:99], v[220:223], a[248:251], v[96:99]\n"
"v_exp_f32 v50, v50\n"
"v_mfma_f32_16x16x32_bf16 v[104:107], v[208:211], v[156:159], v[132:135]\n"
"v_exp_f32 v51, v51\n"
"v_mfma_f32_16x16x32_bf16 v[104:107], v[212:215], v[180:183], v[104:107]\n"
"v_exp_f32 v52, v52\n"
"v_mfma_f32_16x16x32_bf16 v[104:107], v[216:219], a[232:235], v[104:107]\n"
"v_exp_f32 v53, v53\n"
"v_mfma_f32_16x16x32_bf16 v[104:107], v[220:223], a[252:255], v[104:107]\n"
"v_exp_f32 v54, v54\n"
"v_mfma_f32_16x16x32_bf16 v[68:71], v[224:227], v[136:139], v[112:115]\n"
"v_exp_f32 v55, v55\n"
"v_mfma_f32_16x16x32_bf16 v[68:71], v[228:231], v[160:163], v[68:71]\n"
"ds_read_b64_tr_b16 v[200:201], v7 offset:33024\n"
"ds_read_b64_tr_b16 v[202:203], v7 offset:37120\n"
"v_mfma_f32_16x16x32_bf16 v[68:71], v[232:235], v[184:187], v[68:71]\n"
"buffer_load_dwordx4 v1, s[40:43], s69 offen offset:2048 nt lds\n"
"v_mfma_f32_16x16x32_bf16 v[68:71], v[236:239], v[188:191], v[68:71]\n"
"v_cvt_pk_bf16_f32 v48, v48, v49\n"
"v_cvt_pk_bf16_f32 v49, v50, v51\n"
"v_mfma_f32_16x16x32_bf16 v[76:79], v[224:227], v[140:143], v[116:119]\n"
"v_exp_f32 v56, v56\n"
"v_mfma_f32_16x16x32_bf16 v[76:79], v[228:231], v[164:167], v[76:79]\n"
"v_exp_f32 v57, v57\n"
"v_mfma_f32_16x16x32_bf16 v[76:79], v[232:235], a[216:219], v[76:79]\n"
"v_cvt_pk_bf16_f32 v50, v52, v53\n"
"v_cvt_pk_bf16_f32 v51, v54, v55\n"
"v_mfma_f32_16x16x32_bf16 v[76:79], v[236:239], a[236:239], v[76:79]\n"
"v_exp_f32 v58, v58\n"
"v_mfma_f32_16x16x32_bf16 v[84:87], v[224:227], v[144:147], v[120:123]\n"
"v_exp_f32 v59, v59\n"
"v_mfma_f32_16x16x32_bf16 v[84:87], v[228:231], v[168:171], v[84:87]\n"
"v_exp_f32 v60, v60\n"
"v_mfma_f32_16x16x32_bf16 v[84:87], v[232:235], a[220:223], v[84:87]\n"
"v_exp_f32 v61, v61\n"
"v_mfma_f32_16x16x32_bf16 v[84:87], v[236:239], a[240:243], v[84:87]\n"
"v_exp_f32 v62, v62\n"
"v_mfma_f32_16x16x32_bf16 v[92:95], v[224:227], v[148:151], v[124:127]\n"
"v_exp_f32 v63, v63\n"
"v_mfma_f32_16x16x32_bf16 v[92:95], v[228:231], v[172:175], v[92:95]\n"
"buffer_load_dwordx4 v2, s[40:43], s69 offen offset:3072 nt lds\n"
"v_mfma_f32_16x16x32_bf16 v[92:95], v[232:235], a[224:227], v[92:95]\n"
"v_cvt_pk_bf16_f32 v56, v56, v57\n"
"v_cvt_pk_bf16_f32 v57, v58, v59\n"
"v_mfma_f32_16x16x32_bf16 v[92:95], v[236:239], a[244:247], v[92:95]\n"
"v_cvt_pk_bf16_f32 v58, v60, v61\n"
"v_cvt_pk_bf16_f32 v59, v62, v63\n"
"v_mfma_f32_16x16x32_bf16 v[100:103], v[224:227], v[152:155], v[128:131]\n"
"ds_read_b64_tr_b16 v[204:205], v8 offset:33024\n"
"ds_read_b64_tr_b16 v[206:207], v8 offset:37120\n"
"v_mfma_f32_16x16x32_bf16 v[100:103], v[228:231], v[176:179], v[100:103]\n"
"ds_read_b64_tr_b16 v[208:209], v7 offset:33280\n"
"ds_read_b64_tr_b16 v[210:211], v7 offset:37376\n"
"v_mfma_f32_16x16x32_bf16 v[100:103], v[232:235], a[228:231], v[100:103]\n"
"ds_read_b64_tr_b16 v[212:213], v8 offset:33280\n"
"ds_read_b64_tr_b16 v[214:215], v8 offset:37376\n"
"v_mfma_f32_16x16x32_bf16 v[100:103], v[236:239], a[248:251], v[100:103]\n"
"ds_read_b64_tr_b16 v[216:217], v7 offset:33536\n"
"ds_read_b64_tr_b16 v[218:219], v7 offset:37632\n"
"v_mfma_f32_16x16x32_bf16 v[108:111], v[224:227], v[156:159], v[132:135]\n"
"ds_read_b64_tr_b16 v[220:221], v8 offset:33536\n"
"ds_read_b64_tr_b16 v[222:223], v8 offset:37632\n"
"v_mfma_f32_16x16x32_bf16 v[108:111], v[228:231], v[180:183], v[108:111]\n"
"ds_read_b128 v[224:227], v6 offset:32768\n"
"ds_read_b128 v[228:231], v6 offset:33024\n"
"v_mfma_f32_16x16x32_bf16 v[108:111], v[232:235], a[232:235], v[108:111]\n"
"ds_read_b128 v[232:235], v6 offset:33280\n"
"v_max3_f32 v12, v64, v65, v66\n"
"v_mfma_f32_16x16x32_bf16 v[108:111], v[236:239], a[252:255], v[108:111]\n"
"ds_read_b128 v[236:239], v6 offset:33536\n"
"s_waitcnt lgkmcnt(14)\n"
"v_mfma_f32_16x16x32_bf16 a[0:3], v[192:195], v[16:19], a[0:3]\n"
"s_add_u32 m0, s60, 0\n"
"s_add_u32 s70, s56, 65536\n"
"v_mfma_f32_16x16x32_bf16 a[8:11], v[192:195], v[24:27], a[8:11]\n"
"buffer_load_dwordx4 v1, s[36:39], s70 offen offset:0 nt lds\n"
"v_mfma_f32_16x16x32_bf16 a[16:19], v[192:195], v[32:35], a[16:19]\n"
"v_max3_f32 v13, v67, v72, v73\n"
"v_max3_f32 v14, v74, v75, v80\n"
"v_mfma_f32_16x16x32_bf16 a[24:27], v[192:195], v[40:43], a[24:27]\n"
"v_max3_f32 v12, v12, v81, v82\n"
"v_max3_f32 v13, v13, v83, v88\n"
"v_mfma_f32_16x16x32_bf16 a[32:35], v[192:195], v[48:51], a[32:35]\n"
"v_max3_f32 v14, v14, v89, v90\n"
"v_max3_f32 v12, v12, v91, v96\n"
"v_mfma_f32_16x16x32_bf16 a[40:43], v[192:195], v[56:59], a[40:43]\n"
"v_max3_f32 v13, v13, v97, v98\n"
"v_max3_f32 v14, v14, v99, v104\n"
"v_mfma_f32_16x16x32_bf16 a[4:7], v[196:199], v[16:19], a[4:7]\n"
"v_max3_f32 v12, v12, v105, v106\n"
"v_max3_f32 v13, v13, v107, v68\n"
"v_mfma_f32_16x16x32_bf16 a[12:15], v[196:199], v[24:27], a[12:15]\n"
"v_max3_f32 v14, v14, v69, v70\n"
"v_max3_f32 v12, v12, v71, v76\n"
"v_mfma_f32_16x16x32_bf16 a[20:23], v[196:199], v[32:35], a[20:23]\n"
"v_max3_f32 v13, v13, v77, v78\n"
"v_max3_f32 v14, v14, v79, v84\n"
"v_mfma_f32_16x16x32_bf16 a[28:31], v[196:199], v[40:43], a[28:31]\n"
"v_max3_f32 v12, v12, v85, v86\n"
"v_max3_f32 v13, v13, v87, v92\n"
"v_mfma_f32_16x16x32_bf16 a[36:39], v[196:199], v[48:51], a[36:39]\n"
"v_max3_f32 v14, v14, v93, v94\n"
"v_max3_f32 v12, v12, v95, v100\n"
"v_mfma_f32_16x16x32_bf16 a[44:47], v[196:199], v[56:59], a[44:47]\n"
"buffer_load_dwordx4 v2, s[36:39], s70 offen offset:1024 nt lds\n"
"v_mfma_f32_16x16x32_bf16 a[48:51], v[200:203], v[16:19], a[48:51]\n"
"v_max3_f32 v13, v13, v101, v102\n"
"v_max3_f32 v14, v14, v103, v108\n"
"v_mfma_f32_16x16x32_bf16 a[56:59], v[200:203], v[24:27], a[56:59]\n"
"v_max3_f32 v12, v12, v109, v110\n"
"v_max3_f32 v12, v12, v13, v14\n"
"v_mfma_f32_16x16x32_bf16 a[64:67], v[200:203], v[32:35], a[64:67]\n"
"v_max_f32 v12, v12, v111\n"
"v_cmp_lt_f32 vcc, s78, v12\n"
"v_mfma_f32_16x16x32_bf16 a[72:75], v[200:203], v[40:43], a[72:75]\n"
"s_cbranch_vccnz LT8_%=\n"
"LR8_%=:\n"
"ds_read_b128 v[192:195], v6 offset:36864\n"
"v_mfma_f32_16x16x32_bf16 a[80:83], v[200:203], v[48:51], a[80:83]\n"
"ds_read_b128 v[196:199], v6 offset:37120\n"
"v_add_u32 v7, 0x10000, v7\n"
"v_mfma_f32_16x16x32_bf16 a[88:91], v[200:203], v[56:59], a[88:91]\n"
"ds_read_b128 v[200:203], v6 offset:37376\n"
"s_waitcnt lgkmcnt(3)\n"
"v_mfma_f32_16x16x32_bf16 a[52:55], v[204:207], v[16:19], a[52:55]\n"
"v_add_u32 v8, 0x10000, v8\n"
"v_mfma_f32_16x16x32_bf16 a[60:63], v[204:207], v[24:27], a[60:63]\n"
"v_exp_f32 v64, v64\n"
"v_mfma_f32_16x16x32_bf16 a[68:71], v[204:207], v[32:35], a[68:71]\n"
"v_exp_f32 v65, v65\n"
"v_mfma_f32_16x16x32_bf16 a[76:79], v[204:207], v[40:43], a[76:79]\n"
"v_exp_f32 v66, v66\n"
"v_mfma_f32_16x16x32_bf16 a[84:87], v[204:207], v[48:51], a[84:87]\n"
"buffer_load_dwordx4 v1, s[36:39], s70 offen offset:2048 nt lds\n"
"v_mfma_f32_16x16x32_bf16 a[92:95], v[204:207], v[56:59], a[92:95]\n"
"ds_read_b128 v[204:207], v6 offset:37632\n"
"v_cvt_pk_bf16_f32 v64, v64, v65\n"
"v_mfma_f32_16x16x32_bf16 a[96:99], v[208:211], v[16:19], a[96:99]\n"
"v_exp_f32 v67, v67\n"
"v_mfma_f32_16x16x32_bf16 a[104:107], v[208:211], v[24:27], a[104:107]\n"
"v_exp_f32 v68, v68\n"
"v_mfma_f32_16x16x32_bf16 a[112:115], v[208:211], v[32:35], a[112:115]\n"
"v_exp_f32 v69, v69\n"
"v_mfma_f32_16x16x32_bf16 a[120:123], v[208:211], v[40:43], a[120:123]\n"
"v_exp_f32 v70, v70\n"
"v_mfma_f32_16x16x32_bf16 a[128:131], v[208:211], v[48:51], a[128:131]\n"
"v_exp_f32 v71, v71\n"
"v_mfma_f32_16x16x32_bf16 a[136:139], v[208:211], v[56:59], a[136:139]\n"
"v_cvt_pk_bf16_f32 v65, v66, v67\n"
"v_cvt_pk_bf16_f32 v66, v68, v69\n"
"v_mfma_f32_16x16x32_bf16 a[100:103], v[212:215], v[16:19], a[100:103]\n"
"v_exp_f32 v72, v72\n"
"v_mfma_f32_16x16x32_bf16 a[108:111], v[212:215], v[24:27], a[108:111]\n"
"v_exp_f32 v73, v73\n"
"v_mfma_f32_16x16x32_bf16 a[116:119], v[212:215], v[32:35], a[116:119]\n"
"v_cvt_pk_bf16_f32 v67, v70, v71\n"
"v_cvt_pk_bf16_f32 v72, v72, v73\n"
"v_mfma_f32_16x16x32_bf16 a[124:127], v[212:215], v[40:43], a[124:127]\n"
"buffer_load_dwordx4 v2, s[36:39], s70 offen offset:3072 nt lds\n"
"v_mfma_f32_16x16x32_bf16 a[132:135], v[212:215], v[48:51], a[132:135]\n"
"v_exp_f32 v74, v74\n"
"v_mfma_f32_16x16x32_bf16 a[140:143], v[212:215], v[56:59], a[140:143]\n"
"v_exp_f32 v75, v75\n"
"v_mfma_f32_16x16x32_bf16 a[144:147], v[216:219], v[16:19], a[144:147]\n"
"v_exp_f32 v76, v76\n"
"v_mfma_f32_16x16x32_bf16 a[152:155], v[216:219], v[24:27], a[152:155]\n"
"v_exp_f32 v77, v77\n"
"v_mfma_f32_16x16x32_bf16 a[160:163], v[216:219], v[32:35], a[160:163]\n"
"v_exp_f32 v78, v78\n"
"v_mfma_f32_16x16x32_bf16 a[168:171], v[216:219], v[40:43], a[168:171]\n"
"v_exp_f32 v79, v79\n"
"v_mfma_f32_16x16x32_bf16 a[176:179], v[216:219], v[48:51], a[176:179]\n"
"v_cvt_pk_bf16_f32 v73, v74, v75\n"
"v_cvt_pk_bf16_f32 v74, v76, v77\n"
"v_mfma_f32_16x16x32_bf16 a[184:187], v[216:219], v[56:59], a[184:187]\n"
"v_exp_f32 v80, v80\n"
"v_mfma_f32_16x16x32_bf16 a[148:151], v[220:223], v[16:19], a[148:151]\n"
"v_exp_f32 v81, v81\n"
"v_mfma_f32_16x16x32_bf16 a[156:159], v[220:223], v[24:27], a[156:159]\n"
"v_cvt_pk_bf16_f32 v75, v78, v79\n"
"v_cvt_pk_bf16_f32 v80, v80, v81\n"
"v_mfma_f32_16x16x32_bf16 a[164:167], v[220:223], v[32:35], a[164:167]\n"
"v_exp_f32 v82, v82\n"
"v_mfma_f32_16x16x32_bf16 a[172:175], v[220:223], v[40:43], a[172:175]\n"
"v_exp_f32 v83, v83\n"
"v_mfma_f32_16x16x32_bf16 a[180:183], v[220:223], v[48:51], a[180:183]\n"
"v_exp_f32 v84, v84\n"
"v_mfma_f32_16x16x32_bf16 a[188:191], v[220:223], v[56:59], a[188:191]\n"
"v_exp_f32 v85, v85\n"
"v_mfma_f32_16x16x32_bf16 a[192:195], v[240:243], v[16:19], a[192:195]\n"
"v_exp_f32 v86, v86\n"
"v_mfma_f32_16x16x32_bf16 a[196:199], v[240:243], v[24:27], a[196:199]\n"
"v_exp_f32 v87, v87\n"
"v_mfma_f32_16x16x32_bf16 a[200:203], v[240:243], v[32:35], a[200:203]\n"
"s_add_u32 m0, s60, 49152\n"
"s_add_u32 s71, s56, 57344\n"
"v_mfma_f32_16x16x32_bf16 a[204:207], v[240:243], v[40:43], a[204:207]\n"
"buffer_load_dwordx4 v1, s[40:43], s71 offen offset:0 nt lds\n"
"v_mfma_f32_16x16x32_bf16 a[208:211], v[240:243], v[48:51], a[208:211]\n"
"v_cvt_pk_bf16_f32 v81, v82, v83\n"
"v_cvt_pk_bf16_f32 v82, v84, v85\n"
"v_mfma_f32_16x16x32_bf16 a[212:215], v[240:243], v[56:59], a[212:215]\n"
"s_cmp_lg_u32 s63, 0\n"
"s_cbranch_scc1 LT9_%=\n"
"LR9_%=:\n"
"v_mfma_f32_16x16x32_bf16 v[16:19], v[224:227], v[136:139], v[112:115]\n"
"v_exp_f32 v88, v88\n"
"v_mfma_f32_16x16x32_bf16 v[16:19], v[228:231], v[160:163], v[16:19]\n"
"v_exp_f32 v89, v89\n"
"v_mfma_f32_16x16x32_bf16 v[16:19], v[232:235], v[184:187], v[16:19]\n"
"v_cvt_pk_bf16_f32 v83, v86, v87\n"
"v_cvt_pk_bf16_f32 v88, v88, v89\n"
"v_mfma_f32_16x16x32_bf16 v[16:19], v[236:239], v[188:191], v[16:19]\n"
"v_exp_f32 v90, v90\n"
"v_mfma_f32_16x16x32_bf16 v[24:27], v[224:227], v[140:143], v[116:119]\n"
"v_exp_f32 v91, v91\n"
"v_mfma_f32_16x16x32_bf16 v[24:27], v[228:231], v[164:167], v[24:27]\n"
"v_exp_f32 v92, v92\n"
"v_mfma_f32_16x16x32_bf16 v[24:27], v[232:235], a[216:219], v[24:27]\n"
"v_exp_f32 v93, v93\n"
"v_mfma_f32_16x16x32_bf16 v[24:27], v[236:239], a[236:239], v[24:27]\n"
"v_exp_f32 v94, v94\n"
"v_mfma_f32_16x16x32_bf16 v[32:35], v[224:227], v[144:147], v[120:123]\n"
"v_exp_f32 v95, v95\n"
"v_mfma_f32_16x16x32_bf16 v[32:35], v[228:231], v[168:171], v[32:35]\n"
"v_cvt_pk_bf16_f32 v89, v90, v91\n"
"v_cvt_pk_bf16_f32 v90, v92, v93\n"
"v_mfma_f32_16x16x32_bf16 v[32:35], v[232:235], a[220:223], v[32:35]\n"
"v_exp_f32 v96, v96\n"
"v_mfma_f32_16x16x32_bf16 v[32:35], v[236:239], a[240:243], v[32:35]\n"
"v_exp_f32 v97, v97\n"
"v_mfma_f32_16x16x32_bf16 v[40:43], v[224:227], v[148:151], v[124:127]\n"
"v_cvt_pk_bf16_f32 v91, v94, v95\n"
"v_cvt_pk_bf16_f32 v96, v96, v97\n"
"v_mfma_f32_16x16x32_bf16 v[40:43], v[228:231], v[172:175], v[40:43]\n"
"buffer_load_dwordx4 v2, s[40:43], s71 offen offset:1024 nt lds\n"
"v_mfma_f32_16x16x32_bf16 v[40:43], v[232:235], a[224:227], v[40:43]\n"
"s_waitcnt vmcnt(10) lgkmcnt(0)\n"
"s_barrier\n"
"v_mfma_f32_16x16x32_bf16 v[40:43], v[236:239], a[244:247], v[40:43]\n"
"ds_read_b64_tr_b16 v[208:209], v7 offset:0\n"
"ds_read_b64_tr_b16 v[210:211], v7 offset:4096\n"
"v_mfma_f32_16x16x32_bf16 v[48:51], v[224:227], v[152:155], v[128:131]\n"
"ds_read_b64_tr_b16 v[212:213], v8 offset:0\n"
"ds_read_b64_tr_b16 v[214:215], v8 offset:4096\n"
"v_mfma_f32_16x16x32_bf16 v[48:51], v[228:231], v[176:179], v[48:51]\n"
"v_exp_f32 v98, v98\n"
"v_mfma_f32_16x16x32_bf16 v[48:51], v[232:235], a[228:231], v[48:51]\n"
"v_exp_f32 v99, v99\n"
"v_mfma_f32_16x16x32_bf16 v[48:51], v[236:239], a[248:251], v[48:51]\n"
"v_exp_f32 v100, v100\n"
"v_mfma_f32_16x16x32_bf16 v[56:59], v[224:227], v[156:159], v[132:135]\n"
"v_exp_f32 v101, v101\n"
"v_mfma_f32_16x16x32_bf16 v[56:59], v[228:231], v[180:183], v[56:59]\n"
"v_exp_f32 v102, v102\n"
"v_mfma_f32_16x16x32_bf16 v[56:59], v[232:235], a[232:235], v[56:59]\n"
"v_exp_f32 v103, v103\n"
"v_mfma_f32_16x16x32_bf16 v[56:59], v[236:239], a[252:255], v[56:59]\n"
"ds_read_b64_tr_b16 v[216:217], v7 offset:256\n"
"ds_read_b64_tr_b16 v[218:219], v7 offset:4352\n"
"v_mfma_f32_16x16x32_bf16 v[20:23], v[192:195], v[136:139], v[112:115]\n"
"v_cvt_pk_bf16_f32 v97, v98, v99\n"
"v_cvt_pk_bf16_f32 v98, v100, v101\n"
"v_mfma_f32_16x16x32_bf16 v[20:23], v[196:199], v[160:163], v[20:23]\n"
"v_exp_f32 v104, v104\n"
"v_mfma_f32_16x16x32_bf16 v[20:23], v[200:203], v[184:187], v[20:23]\n"
"buffer_load_dwordx4 v1, s[40:43], s71 offen offset:2048 nt lds\n"
"v_mfma_f32_16x16x32_bf16 v[20:23], v[204:207], v[188:191], v[20:23]\n"
"v_exp_f32 v105, v105\n"
"v_mfma_f32_16x16x32_bf16 v[28:31], v[192:195], v[140:143], v[116:119]\n"
"v_cvt_pk_bf16_f32 v99, v102, v103\n"
"v_cvt_pk_bf16_f32 v104, v104, v105\n"
"v_mfma_f32_16x16x32_bf16 v[28:31], v[196:199], v[164:167], v[28:31]\n"
"v_exp_f32 v106, v106\n"
"v_mfma_f32_16x16x32_bf16 v[28:31], v[200:203], a[216:219], v[28:31]\n"
"v_exp_f32 v107, v107\n"
"v_mfma_f32_16x16x32_bf16 v[28:31], v[204:207], a[236:239], v[28:31]\n"
"v_exp_f32 v108, v108\n"
"v_mfma_f32_16x16x32_bf16 v[36:39], v[192:195], v[144:147], v[120:123]\n"
"v_exp_f32 v109, v109\n"
"v_mfma_f32_16x16x32_bf16 v[36:39], v[196:199], v[168:171], v[36:39]\n"
"v_exp_f32 v110, v110\n"
"v_mfma_f32_16x16x32_bf16 v[36:39], v[200:203], a[220:223], v[36:39]\n"
"v_exp_f32 v111, v111\n"
"v_mfma_f32_16x16x32_bf16 v[36:39], v[204:207], a[240:243], v[36:39]\n"
"v_cvt_pk_bf16_f32 v105, v106, v107\n"
"v_cvt_pk_bf16_f32 v106, v108, v109\n"
"v_mfma_f32_16x16x32_bf16 v[44:47], v[192:195], v[148:151], v[124:127]\n"
"v_cvt_pk_bf16_f32 v107, v110, v111\n"
"ds_read_b64_tr_b16 v[220:221], v8 offset:256\n"
"v_mfma_f32_16x16x32_bf16 v[44:47], v[196:199], v[172:175], v[44:47]\n"
"buffer_load_dwordx4 v2, s[40:43], s71 offen offset:3072 nt lds\n"
"v_mfma_f32_16x16x32_bf16 v[44:47], v[200:203], a[224:227], v[44:47]\n"
"ds_read_b64_tr_b16 v[222:223], v8 offset:4352\n"
"ds_read_b64_tr_b16 v[224:225], v7 offset:512\n"
"v_mfma_f32_16x16x32_bf16 v[44:47], v[204:207], a[244:247], v[44:47]\n"
"ds_read_b64_tr_b16 v[226:227], v7 offset:4608\n"
"ds_read_b64_tr_b16 v[228:229], v8 offset:512\n"
"v_mfma_f32_16x16x32_bf16 v[52:55], v[192:195], v[152:155], v[128:131]\n"
"ds_read_b64_tr_b16 v[230:231], v8 offset:4608\n"
"ds_read_b64_tr_b16 v[232:233], v7 offset:768\n"
"v_mfma_f32_16x16x32_bf16 v[52:55], v[196:199], v[176:179], v[52:55]\n"
"ds_read_b64_tr_b16 v[234:235], v7 offset:4864\n"
"ds_read_b64_tr_b16 v[236:237], v8 offset:768\n"
"v_mfma_f32_16x16x32_bf16 v[52:55], v[200:203], a[228:231], v[52:55]\n"
"ds_read_b64_tr_b16 v[238:239], v8 offset:4864\n"
"v_add_u32 v6, 0x10000, v6\n"
"v_mfma_f32_16x16x32_bf16 v[52:55], v[204:207], a[248:251], v[52:55]\n"
"v_max3_f32 v12, v16, v17, v18\n"
"v_max3_f32 v13, v19, v24, v25\n"
"v_mfma_f32_16x16x32_bf16 v[60:63], v[192:195], v[156:159], v[132:135]\n"
"ds_read_b128 v[192:195], v6 offset:0\n"
"v_max3_f32 v14, v26, v27, v32\n"
"v_mfma_f32_16x16x32_bf16 v[60:63], v[196:199], v[180:183], v[60:63]\n"
"ds_read_b128 v[196:199], v6 offset:256\n"
"v_max3_f32 v12, v12, v33, v34\n"
"v_mfma_f32_16x16x32_bf16 v[60:63], v[200:203], a[232:235], v[60:63]\n"
"ds_read_b128 v[200:203], v6 offset:512\n"
"v_max3_f32 v13, v13, v35, v40\n"
"v_mfma_f32_16x16x32_bf16 v[60:63], v[204:207], a[252:255], v[60:63]\n"
"ds_read_b128 v[204:207], v6 offset:768\n"
"s_waitcnt lgkmcnt(12)\n"
"v_mfma_f32_16x16x32_bf16 a[0:3], v[208:211], v[64:67], a[0:3]\n"
"s_add_u32 m0, s60, 32768\n"
"s_add_u32 s64, s56, 73728\n"
"v_mfma_f32_16x16x32_bf16 a[8:11], v[208:211], v[72:75], a[8:11]\n"
"buffer_load_dwordx4 v1, s[36:39], s64 offen offset:0 nt lds\n"
"v_mfma_f32_16x16x32_bf16 a[16:19], v[208:211], v[80:83], a[16:19]\n"
"v_max3_f32 v14, v14, v41, v42\n"
"v_max3_f32 v12, v12, v43, v48\n"
"v_mfma_f32_16x16x32_bf16 a[24:27], v[208:211], v[88:91], a[24:27]\n"
"v_max3_f32 v13, v13, v49, v50\n"
"v_max3_f32 v14, v14, v51, v56\n"
"v_mfma_f32_16x16x32_bf16 a[32:35], v[208:211], v[96:99], a[32:35]\n"
"v_max3_f32 v12, v12, v57, v58\n"
"v_max3_f32 v13, v13, v59, v20\n"
"v_mfma_f32_16x16x32_bf16 a[40:43], v[208:211], v[104:107], a[40:43]\n"
"v_max3_f32 v14, v14, v21, v22\n"
"v_max3_f32 v12, v12, v23, v28\n"
"v_mfma_f32_16x16x32_bf16 a[4:7], v[212:215], v[64:67], a[4:7]\n"
"v_max3_f32 v13, v13, v29, v30\n"
"v_max3_f32 v14, v14, v31, v36\n"
"v_mfma_f32_16x16x32_bf16 a[12:15], v[212:215], v[72:75], a[12:15]\n"
"v_max3_f32 v12, v12, v37, v38\n"
"v_max3_f32 v13, v13, v39, v44\n"
"v_mfma_f32_16x16x32_bf16 a[20:23], v[212:215], v[80:83], a[20:23]\n"
"v_max3_f32 v14, v14, v45, v46\n"
"v_max3_f32 v12, v12, v47, v52\n"
"v_mfma_f32_16x16x32_bf16 a[28:31], v[212:215], v[88:91], a[28:31]\n"
"v_max3_f32 v13, v13, v53, v54\n"
"v_max3_f32 v14, v14, v55, v60\n"
"v_mfma_f32_16x16x32_bf16 a[36:39], v[212:215], v[96:99], a[36:39]\n"
"v_max3_f32 v12, v12, v61, v62\n"
"v_max3_f32 v12, v12, v13, v14\n"
"v_mfma_f32_16x16x32_bf16 a[44:47], v[212:215], v[104:107], a[44:47]\n"
"buffer_load_dwordx4 v2, s[36:39], s64 offen offset:1024 nt lds\n"
"v_mfma_f32_16x16x32_bf16 a[48:51], v[216:219], v[64:67], a[48:51]\n"
"v_max_f32 v12, v12, v63\n"
"v_cmp_lt_f32 vcc, s78, v12\n"
"v_mfma_f32_16x16x32_bf16 a[56:59], v[216:219], v[72:75], a[56:59]\n"
"s_cbranch_vccnz LT10_%=\n"
"LR10_%=:\n"
"ds_read_b128 v[208:211], v6 offset:4096\n"
"v_mfma_f32_16x16x32_bf16 a[64:67], v[216:219], v[80:83], a[64:67]\n"
"ds_read_b128 v[212:215], v6 offset:4352\n"
"v_add_u32 v7, 0xfffe0000, v7\n"
"v_mfma_f32_16x16x32_bf16 a[72:75], v[216:219], v[88:91], a[72:75]\n"
"v_add_u32 v8, 0xfffe0000, v8\n"
"v_mfma_f32_16x16x32_bf16 a[80:83], v[216:219], v[96:99], a[80:83]\n"
"v_exp_f32 v16, v16\n"
"v_mfma_f32_16x16x32_bf16 a[88:91], v[216:219], v[104:107], a[88:91]\n"
"ds_read_b128 v[216:219], v6 offset:4608\n"
"v_mfma_f32_16x16x32_bf16 a[52:55], v[220:223], v[64:67], a[52:55]\n"
"v_exp_f32 v17, v17\n"
"v_mfma_f32_16x16x32_bf16 a[60:63], v[220:223], v[72:75], a[60:63]\n"
"v_exp_f32 v18, v18\n"
"v_mfma_f32_16x16x32_bf16 a[68:71], v[220:223], v[80:83], a[68:71]\n"
"v_exp_f32 v19, v19\n"
"v_mfma_f32_16x16x32_bf16 a[76:79], v[220:223], v[88:91], a[76:79]\n"
"v_exp_f32 v20, v20\n"
"v_mfma_f32_16x16x32_bf16 a[84:87], v[220:223], v[96:99], a[84:87]\n"
"buffer_load_dwordx4 v1, s[36:39], s64 offen offset:2048 nt lds\n"
"v_mfma_f32_16x16x32_bf16 a[92:95], v[220:223], v[104:107], a[92:95]\n"
"ds_read_b128 v[220:223], v6 offset:4864\n"
"v_cvt_pk_bf16_f32 v16, v16, v17\n"
"s_waitcnt lgkmcnt(2)\n"
"v_mfma_f32_16x16x32_bf16 a[96:99], v[224:227], v[64:67], a[96:99]\n"
"v_exp_f32 v21, v21\n"
"v_mfma_f32_16x16x32_bf16 a[104:107], v[224:227], v[72:75], a[104:107]\n"
"v_exp_f32 v22, v22\n"
"v_mfma_f32_16x16x32_bf16 a[112:115], v[224:227], v[80:83], a[112:115]\n"
"v_exp_f32 v23, v23\n"
"v_mfma_f32_16x16x32_bf16 a[120:123], v[224:227], v[88:91], a[120:123]\n"
"v_cvt_pk_bf16_f32 v17, v18, v19\n"
"v_cvt_pk_bf16_f32 v18, v20, v21\n"
"v_mfma_f32_16x16x32_bf16 a[128:131], v[224:227], v[96:99], a[128:131]\n"
"v_exp_f32 v24, v24\n"
"v_mfma_f32_16x16x32_bf16 a[136:139], v[224:227], v[104:107], a[136:139]\n"
"v_exp_f32 v25, v25\n"
"v_mfma_f32_16x16x32_bf16 a[100:103], v[228:231], v[64:67], a[100:103]\n"
"v_cvt_pk_bf16_f32 v19, v22, v23\n"
"v_cvt_pk_bf16_f32 v24, v24, v25\n"
"v_mfma_f32_16x16x32_bf16 a[108:111], v[228:231], v[72:75], a[108:111]\n"
"v_exp_f32 v26, v26\n"
"v_mfma_f32_16x16x32_bf16 a[116:119], v[228:231], v[80:83], a[116:119]\n"
"v_exp_f32 v27, v27\n"
"v_mfma_f32_16x16x32_bf16 a[124:127], v[228:231], v[88:91], a[124:127]\n"
"buffer_load_dwordx4 v2, s[36:39], s64 offen offset:3072 nt lds\n"
"v_mfma_f32_16x16x32_bf16 a[132:135], v[228:231], v[96:99], a[132:135]\n"
"v_exp_f32 v28, v28\n"
"v_mfma_f32_16x16x32_bf16 a[140:143], v[228:231], v[104:107], a[140:143]\n"
"v_exp_f32 v29, v29\n"
"v_mfma_f32_16x16x32_bf16 a[144:147], v[232:235], v[64:67], a[144:147]\n"
"v_exp_f32 v30, v30\n"
"v_mfma_f32_16x16x32_bf16 a[152:155], v[232:235], v[72:75], a[152:155]\n"
"v_exp_f32 v31, v31\n"
"v_mfma_f32_16x16x32_bf16 a[160:163], v[232:235], v[80:83], a[160:163]\n"
"v_cvt_pk_bf16_f32 v25, v26, v27\n"
"v_cvt_pk_bf16_f32 v26, v28, v29\n"
"v_mfma_f32_16x16x32_bf16 a[168:171], v[232:235], v[88:91], a[168:171]\n"
"v_exp_f32 v32, v32\n"
"v_mfma_f32_16x16x32_bf16 a[176:179], v[232:235], v[96:99], a[176:179]\n"
"v_exp_f32 v33, v33\n"
"v_mfma_f32_16x16x32_bf16 a[184:187], v[232:235], v[104:107], a[184:187]\n"
"v_cvt_pk_bf16_f32 v27, v30, v31\n"
"v_cvt_pk_bf16_f32 v32, v32, v33\n"
"v_mfma_f32_16x16x32_bf16 a[148:151], v[236:239], v[64:67], a[148:151]\n"
"v_exp_f32 v34, v34\n"
"v_mfma_f32_16x16x32_bf16 a[156:159], v[236:239], v[72:75], a[156:159]\n"
"v_exp_f32 v35, v35\n"
"v_mfma_f32_16x16x32_bf16 a[164:167], v[236:239], v[80:83], a[164:167]\n"
"v_exp_f32 v36, v36\n"
"v_mfma_f32_16x16x32_bf16 a[172:175], v[236:239], v[88:91], a[172:175]\n"
"v_exp_f32 v37, v37\n"
"v_mfma_f32_16x16x32_bf16 a[180:183], v[236:239], v[96:99], a[180:183]\n"
"v_exp_f32 v38, v38\n"
"v_mfma_f32_16x16x32_bf16 a[188:191], v[236:239], v[104:107], a[188:191]\n"
"v_exp_f32 v39, v39\n"
"v_mfma_f32_16x16x32_bf16 a[192:195], v[240:243], v[64:67], a[192:195]\n"
"v_cvt_pk_bf16_f32 v33, v34, v35\n"
"v_cvt_pk_bf16_f32 v34, v36, v37\n"
"v_mfma_f32_16x16x32_bf16 a[196:199], v[240:243], v[72:75], a[196:199]\n"
"v_exp_f32 v40, v40\n"
"v_mfma_f32_16x16x32_bf16 a[200:203], v[240:243], v[80:83], a[200:203]\n"
"s_add_u32 m0, s60, 81920\n"
"s_add_u32 s65, s56, 65536\n"
"v_mfma_f32_16x16x32_bf16 a[204:207], v[240:243], v[88:91], a[204:207]\n"
"buffer_load_dwordx4 v1, s[40:43], s65 offen offset:0 nt lds\n"
"v_mfma_f32_16x16x32_bf16 a[208:211], v[240:243], v[96:99], a[208:211]\n"
"v_exp_f32 v41, v41\n"
"v_mfma_f32_16x16x32_bf16 a[212:215], v[240:243], v[104:107], a[212:215]\n"
"s_cmp_lg_u32 s63, 0\n"
"s_cbranch_scc1 LT11_%=\n"
"LR11_%=:\n"
"v_mfma_f32_16x16x32_bf16 v[64:67], v[192:195], v[136:139], v[112:115]\n"
"v_cvt_pk_bf16_f32 v35, v38, v39\n"
"v_cvt_pk_bf16_f32 v40, v40, v41\n"
"v_mfma_f32_16x16x32_bf16 v[64:67], v[196:199], v[160:163], v[64:67]\n"
"v_exp_f32 v42, v42\n"
"v_mfma_f32_16x16x32_bf16 v[64:67], v[200:203], v[184:187], v[64:67]\n"
"v_exp_f32 v43, v43\n"
"v_mfma_f32_16x16x32_bf16 v[64:67], v[204:207], v[188:191], v[64:67]\n"
"v_exp_f32 v44, v44\n"
"v_mfma_f32_16x16x32_bf16 v[72:75], v[192:195], v[140:143], v[116:119]\n"
"v_exp_f32 v45, v45\n"
"v_mfma_f32_16x16x32_bf16 v[72:75], v[196:199], v[164:167], v[72:75]\n"
"v_exp_f32 v46, v46\n"
"v_mfma_f32_16x16x32_bf16 v[72:75], v[200:203], a[216:219], v[72:75]\n"
"v_exp_f32 v47, v47\n"
"v_mfma_f32_16x16x32_bf16 v[72:75], v[204:207], a[236:239], v[72:75]\n"
"v_cvt_pk_bf16_f32 v41, v42, v43\n"
"v_cvt_pk_bf16_f32 v42, v44, v45\n"
"v_mfma_f32_16x16x32_bf16 v[80:83], v[192:195], v[144:147], v[120:123]\n"
"v_exp_f32 v48, v48\n"
"v_mfma_f32_16x16x32_bf16 v[80:83], v[196:199], v[168:171], v[80:83]\n"
"v_exp_f32 v49, v49\n"
"v_mfma_f32_16x16x32_bf16 v[80:83], v[200:203], a[220:223], v[80:83]\n"
"v_cvt_pk_bf16_f32 v43, v46, v47\n"
"v_cvt_pk_bf16_f32 v48, v48, v49\n"
"v_mfma_f32_16x16x32_bf16 v[80:83], v[204:207], a[240:243], v[80:83]\n"
"v_exp_f32 v50, v50\n"
"v_mfma_f32_16x16x32_bf16 v[88:91], v[192:195], v[148:151], v[124:127]\n"
"v_exp_f32 v51, v51\n"
"v_mfma_f32_16x16x32_bf16 v[88:91], v[196:199], v[172:175], v[88:91]\n"
"buffer_load_dwordx4 v2, s[40:43], s65 offen offset:1024 nt lds\n"
"v_mfma_f32_16x16x32_bf16 v[88:91], v[200:203], a[224:227], v[88:91]\n"
"s_waitcnt vmcnt(10) lgkmcnt(0)\n"
"s_barrier\n"
"v_mfma_f32_16x16x32_bf16 v[88:91], v[204:207], a[244:247], v[88:91]\n"
"ds_read_b64_tr_b16 v[224:225], v7 offset:0\n"
"ds_read_b64_tr_b16 v[226:227], v7 offset:4096\n"
"v_mfma_f32_16x16x32_bf16 v[96:99], v[192:195], v[152:155], v[128:131]\n"
"ds_read_b64_tr_b16 v[228:229], v8 offset:0\n"
"ds_read_b64_tr_b16 v[230:231], v8 offset:4096\n"
"v_mfma_f32_16x16x32_bf16 v[96:99], v[196:199], v[176:179], v[96:99]\n"
"v_exp_f32 v52, v52\n"
"v_mfma_f32_16x16x32_bf16 v[96:99], v[200:203], a[228:231], v[96:99]\n"
"v_exp_f32 v53, v53\n"
"v_mfma_f32_16x16x32_bf16 v[96:99], v[204:207], a[248:251], v[96:99]\n"
"v_exp_f32 v54, v54\n"
"v_mfma_f32_16x16x32_bf16 v[104:107], v[192:195], v[156:159], v[132:135]\n"
"v_exp_f32 v55, v55\n"
"v_mfma_f32_16x16x32_bf16 v[104:107], v[196:199], v[180:183], v[104:107]\n"
"ds_read_b64_tr_b16 v[232:233], v7 offset:256\n"
"ds_read_b64_tr_b16 v[234:235], v7 offset:4352\n"
"v_mfma_f32_16x16x32_bf16 v[104:107], v[200:203], a[232:235], v[104:107]\n"
"v_cvt_pk_bf16_f32 v49, v50, v51\n"
"v_cvt_pk_bf16_f32 v50, v52, v53\n"
"v_mfma_f32_16x16x32_bf16 v[104:107], v[204:207], a[252:255], v[104:107]\n"
"v_exp_f32 v56, v56\n"
"v_mfma_f32_16x16x32_bf16 v[68:71], v[208:211], v[136:139], v[112:115]\n"
"v_exp_f32 v57, v57\n"
"v_mfma_f32_16x16x32_bf16 v[68:71], v[212:215], v[160:163], v[68:71]\n"
"v_cvt_pk_bf16_f32 v51, v54, v55\n"
"v_cvt_pk_bf16_f32 v56, v56, v57\n"
"v_mfma_f32_16x16x32_bf16 v[68:71], v[216:219], v[184:187], v[68:71]\n"
"buffer_load_dwordx4 v1, s[40:43], s65 offen offset:2048 nt lds\n"
"v_mfma_f32_16x16x32_bf16 v[68:71], v[220:223], v[188:191], v[68:71]\n"
"v_exp_f32 v58, v58\n"
"v_mfma_f32_16x16x32_bf16 v[76:79], v[208:211], v[140:143], v[116:119]\n"
"v_exp_f32 v59, v59\n"
"v_mfma_f32_16x16x32_bf16 v[76:79], v[212:215], v[164:167], v[76:79]\n"
"v_exp_f32 v60, v60\n"
"v_mfma_f32_16x16x32_bf16 v[76:79], v[216:219], a[216:219], v[76:79]\n"
"v_exp_f32 v61, v61\n"
"v_mfma_f32_16x16x32_bf16 v[76:79], v[220:223], a[236:239], v[76:79]\n"
"v_exp_f32 v62, v62\n"
"v_mfma_f32_16x16x32_bf16 v[84:87], v[208:211], v[144:147], v[120:123]\n"
"v_exp_f32 v63, v63\n"
"v_mfma_f32_16x16x32_bf16 v[84:87], v[212:215], v[168:171], v[84:87]\n"
"v_cvt_pk_bf16_f32 v57, v58, v59\n"
"v_cvt_pk_bf16_f32 v58, v60, v61\n"
"v_mfma_f32_16x16x32_bf16 v[84:87], v[216:219], a[220:223], v[84:87]\n"
"v_cvt_pk_bf16_f32 v59, v62, v63\n"
"ds_read_b64_tr_b16 v[236:237], v8 offset:256\n"
"v_mfma_f32_16x16x32_bf16 v[84:87], v[220:223], a[240:243], v[84:87]\n"
"ds_read_b64_tr_b16 v[238:239], v8 offset:4352\n"
"ds_read_b64_tr_b16 v[192:193], v7 offset:512\n"
"v_mfma_f32_16x16x32_bf16 v[92:95], v[208:211], v[148:151], v[124:127]\n"
"ds_read_b64_tr_b16 v[194:195], v7 offset:4608\n"
"ds_read_b64_tr_b16 v[196:197], v8 offset:512\n"
"v_mfma_f32_16x16x32_bf16 v[92:95], v[212:215], v[172:175], v[92:95]\n"
"buffer_load_dwordx4 v2, s[40:43], s65 offen offset:3072 nt lds\n"
"v_mfma_f32_16x16x32_bf16 v[92:95], v[216:219], a[224:227], v[92:95]\n"
"ds_read_b64_tr_b16 v[198:199], v8 offset:4608\n"
"ds_read_b64_tr_b16 v[200:201], v7 offset:768\n"
"v_mfma_f32_16x16x32_bf16 v[92:95], v[220:223], a[244:247], v[92:95]\n"
"ds_read_b64_tr_b16 v[202:203], v7 offset:4864\n"
"ds_read_b64_tr_b16 v[204:205], v8 offset:768\n"
"v_mfma_f32_16x16x32_bf16 v[100:103], v[208:211], v[152:155], v[128:131]\n"
"ds_read_b64_tr_b16 v[206:207], v8 offset:4864\n"
"v_add_u32 v6, 0xfffe0000, v6\n"
"v_mfma_f32_16x16x32_bf16 v[100:103], v[212:215], v[176:179], v[100:103]\n"
"v_max3_f32 v12, v64, v65, v66\n"
"v_max3_f32 v13, v67, v72, v73\n"
"v_mfma_f32_16x16x32_bf16 v[100:103], v[216:219], a[228:231], v[100:103]\n"
"v_max3_f32 v14, v74, v75, v80\n"
"v_max3_f32 v12, v12, v81, v82\n"
"v_mfma_f32_16x16x32_bf16 v[100:103], v[220:223], a[248:251], v[100:103]\n"
"v_max3_f32 v13, v13, v83, v88\n"
"v_max3_f32 v14, v14, v89, v90\n"
"v_mfma_f32_16x16x32_bf16 v[108:111], v[208:211], v[156:159], v[132:135]\n"
"ds_read_b128 v[208:211], v6 offset:0\n"
"v_max3_f32 v12, v12, v91, v96\n"
"v_mfma_f32_16x16x32_bf16 v[108:111], v[212:215], v[180:183], v[108:111]\n"
"ds_read_b128 v[212:215], v6 offset:256\n"
"v_max3_f32 v13, v13, v97, v98\n"
"v_mfma_f32_16x16x32_bf16 v[108:111], v[216:219], a[232:235], v[108:111]\n"
"ds_read_b128 v[216:219], v6 offset:512\n"
"v_max3_f32 v14, v14, v99, v104\n"
"v_mfma_f32_16x16x32_bf16 v[108:111], v[220:223], a[252:255], v[108:111]\n"
"ds_read_b128 v[220:223], v6 offset:768\n"
"s_waitcnt lgkmcnt(8)\n"
"v_mfma_f32_16x16x32_bf16 a[0:3], v[224:227], v[16:19], a[0:3]\n"
"s_add_u32 m0, s60, 65536\n"
"s_add_u32 s66, s56, 81920\n"
"v_mfma_f32_16x16x32_bf16 a[8:11], v[224:227], v[24:27], a[8:11]\n"
"buffer_load_dwordx4 v1, s[36:39], s66 offen offset:0 nt lds\n"
"v_mfma_f32_16x16x32_bf16 a[16:19], v[224:227], v[32:35], a[16:19]\n"
"v_max3_f32 v12, v12, v105, v106\n"
"v_max3_f32 v13, v13, v107, v68\n"
"v_mfma_f32_16x16x32_bf16 a[24:27], v[224:227], v[40:43], a[24:27]\n"
"v_max3_f32 v14, v14, v69, v70\n"
"v_max3_f32 v12, v12, v71, v76\n"
"v_mfma_f32_16x16x32_bf16 a[32:35], v[224:227], v[48:51], a[32:35]\n"
"v_max3_f32 v13, v13, v77, v78\n"
"v_max3_f32 v14, v14, v79, v84\n"
"v_mfma_f32_16x16x32_bf16 a[40:43], v[224:227], v[56:59], a[40:43]\n"
"v_max3_f32 v12, v12, v85, v86\n"
"v_max3_f32 v13, v13, v87, v92\n"
"v_mfma_f32_16x16x32_bf16 a[4:7], v[228:231], v[16:19], a[4:7]\n"
"v_max3_f32 v14, v14, v93, v94\n"
"v_max3_f32 v12, v12, v95, v100\n"
"v_mfma_f32_16x16x32_bf16 a[12:15], v[228:231], v[24:27], a[12:15]\n"
"v_max3_f32 v13, v13, v101, v102\n"
"v_max3_f32 v14, v14, v103, v108\n"
"v_mfma_f32_16x16x32_bf16 a[20:23], v[228:231], v[32:35], a[20:23]\n"
"v_max3_f32 v12, v12, v109, v110\n"
"v_max3_f32 v12, v12, v13, v14\n"
"v_mfma_f32_16x16x32_bf16 a[28:31], v[228:231], v[40:43], a[28:31]\n"
"v_max_f32 v12, v12, v111\n"
"v_cmp_lt_f32 vcc, s78, v12\n"
"v_mfma_f32_16x16x32_bf16 a[36:39], v[228:231], v[48:51], a[36:39]\n"
"s_cbranch_vccnz LT12_%=\n"
"LR12_%=:\n"
"ds_read_b128 v[224:227], v6 offset:4096\n"
"v_mfma_f32_16x16x32_bf16 a[44:47], v[228:231], v[56:59], a[44:47]\n"
"buffer_load_dwordx4 v2, s[36:39], s66 offen offset:1024 nt lds\n"
"v_mfma_f32_16x16x32_bf16 a[48:51], v[232:235], v[16:19], a[48:51]\n"
"ds_read_b128 v[228:231], v6 offset:4352\n"
"v_mfma_f32_16x16x32_bf16 a[56:59], v[232:235], v[24:27], a[56:59]\n"
"v_exp_f32 v64, v64\n"
"v_mfma_f32_16x16x32_bf16 a[64:67], v[232:235], v[32:35], a[64:67]\n"
"v_exp_f32 v65, v65\n"
"v_mfma_f32_16x16x32_bf16 a[72:75], v[232:235], v[40:43], a[72:75]\n"
"v_exp_f32 v66, v66\n"
"v_mfma_f32_16x16x32_bf16 a[80:83], v[232:235], v[48:51], a[80:83]\n"
"v_exp_f32 v67, v67\n"
"v_mfma_f32_16x16x32_bf16 a[88:91], v[232:235], v[56:59], a[88:91]\n"
"ds_read_b128 v[232:235], v6 offset:4608\n"
"v_cvt_pk_bf16_f32 v64, v64, v65\n"
"v_mfma_f32_16x16x32_bf16 a[52:55], v[236:239], v[16:19], a[52:55]\n"
"v_exp_f32 v68, v68\n"
"v_mfma_f32_16x16x32_bf16 a[60:63], v[236:239], v[24:27], a[60:63]\n"
"v_exp_f32 v69, v69\n"
"v_mfma_f32_16x16x32_bf16 a[68:71], v[236:239], v[32:35], a[68:71]\n"
"v_exp_f32 v70, v70\n"
"v_mfma_f32_16x16x32_bf16 a[76:79], v[236:239], v[40:43], a[76:79]\n"
"v_exp_f32 v71, v71\n"
"v_mfma_f32_16x16x32_bf16 a[84:87], v[236:239], v[48:51], a[84:87]\n"
"buffer_load_dwordx4 v1, s[36:39], s66 offen offset:2048 nt lds\n"
"v_mfma_f32_16x16x32_bf16 a[92:95], v[236:239], v[56:59], a[92:95]\n"
"ds_read_b128 v[236:239], v6 offset:4864\n"
"v_cvt_pk_bf16_f32 v65, v66, v67\n"
"v_mfma_f32_16x16x32_bf16 a[96:99], v[192:195], v[16:19], a[96:99]\n"
"v_exp_f32 v72, v72\n"
"v_mfma_f32_16x16x32_bf16 a[104:107], v[192:195], v[24:27], a[104:107]\n"
"v_exp_f32 v73, v73\n"
"v_mfma_f32_16x16x32_bf16 a[112:115], v[192:195], v[32:35], a[112:115]\n"
"v_cvt_pk_bf16_f32 v66, v68, v69\n"
"v_cvt_pk_bf16_f32 v67, v70, v71\n"
"v_mfma_f32_16x16x32_bf16 a[120:123], v[192:195], v[40:43], a[120:123]\n"
"v_exp_f32 v74, v74\n"
"v_mfma_f32_16x16x32_bf16 a[128:131], v[192:195], v[48:51], a[128:131]\n"
"v_exp_f32 v75, v75\n"
"v_mfma_f32_16x16x32_bf16 a[136:139], v[192:195], v[56:59], a[136:139]\n"
"v_cvt_pk_bf16_f32 v72, v72, v73\n"
"v_mfma_f32_16x16x32_bf16 a[100:103], v[196:199], v[16:19], a[100:103]\n"
"v_exp_f32 v76, v76\n"
"v_mfma_f32_16x16x32_bf16 a[108:111], v[196:199], v[24:27], a[108:111]\n"
"v_exp_f32 v77, v77\n"
"v_mfma_f32_16x16x32_bf16 a[116:119], v[196:199], v[32:35], a[116:119]\n"
"v_exp_f32 v78, v78\n"
"v_mfma_f32_16x16x32_bf16 a[124:127], v[196:199], v[40:43], a[124:127]\n"
"buffer_load_dwordx4 v2, s[36:39], s66 offen offset:3072 nt lds\n"
"v_mfma_f32_16x16x32_bf16 a[132:135], v[196:199], v[48:51], a[132:135]\n"
"v_exp_f32 v79, v79\n"
"v_mfma_f32_16x16x32_bf16 a[140:143], v[196:199], v[56:59], a[140:143]\n"
"v_cvt_pk_bf16_f32 v73, v74, v75\n"
"v_cvt_pk_bf16_f32 v74, v76, v77\n"
"s_waitcnt lgkmcnt(0)\n"
"v_mfma_f32_16x16x32_bf16 a[144:147], v[200:203], v[16:19], a[144:147]\n"
"v_exp_f32 v80, v80\n"
"v_mfma_f32_16x16x32_bf16 a[152:155], v[200:203], v[24:27], a[152:155]\n"
"v_exp_f32 v81, v81\n"
"v_mfma_f32_16x16x32_bf16 a[160:163], v[200:203], v[32:35], a[160:163]\n"
"v_cvt_pk_bf16_f32 v75, v78, v79\n"
"v_cvt_pk_bf16_f32 v80, v80, v81\n"
"v_mfma_f32_16x16x32_bf16 a[168:171], v[200:203], v[40:43], a[168:171]\n"
"v_exp_f32 v82, v82\n"
"v_mfma_f32_16x16x32_bf16 a[176:179], v[200:203], v[48:51], a[176:179]\n"
"v_exp_f32 v83, v83\n"
"v_mfma_f32_16x16x32_bf16 a[184:187], v[200:203], v[56:59], a[184:187]\n"
"v_exp_f32 v84, v84\n"
"v_mfma_f32_16x16x32_bf16 a[148:151], v[204:207], v[16:19], a[148:151]\n"
"v_exp_f32 v85, v85\n"
"v_mfma_f32_16x16x32_bf16 a[156:159], v[204:207], v[24:27], a[156:159]\n"
"v_exp_f32 v86, v86\n"
"v_mfma_f32_16x16x32_bf16 a[164:167], v[204:207], v[32:35], a[164:167]\n"
"v_exp_f32 v87, v87\n"
"v_mfma_f32_16x16x32_bf16 a[172:175], v[204:207], v[40:43], a[172:175]\n"
"v_cvt_pk_bf16_f32 v81, v82, v83\n"
"v_cvt_pk_bf16_f32 v82, v84, v85\n"
"v_mfma_f32_16x16x32_bf16 a[180:183], v[204:207], v[48:51], a[180:183]\n"
"v_exp_f32 v88, v88\n"
"v_mfma_f32_16x16x32_bf16 a[188:191], v[204:207], v[56:59], a[188:191]\n"
"v_exp_f32 v89, v89\n"
"v_mfma_f32_16x16x32_bf16 a[192:195], v[240:243], v[16:19], a[192:195]\n"
"v_cvt_pk_bf16_f32 v83, v86, v87\n"
"v_cvt_pk_bf16_f32 v88, v88, v89\n"
"v_mfma_f32_16x16x32_bf16 a[196:199], v[240:243], v[24:27], a[196:199]\n"
"v_exp_f32 v90, v90\n"
"v_mfma_f32_16x16x32_bf16 a[200:203], v[240:243], v[32:35], a[200:203]\n"
"s_add_u32 m0, s60, 114688\n"
"s_add_u32 s67, s56, 73728\n"
"v_mfma_f32_16x16x32_bf16 a[204:207], v[240:243], v[40:43], a[204:207]\n"
"buffer_load_dwordx4 v1, s[40:43], s67 offen offset:0 nt lds\n"
"v_mfma_f32_16x16x32_bf16 a[208:211], v[240:243], v[48:51], a[208:211]\n"
"v_exp_f32 v91, v91\n"
"v_mfma_f32_16x16x32_bf16 a[212:215], v[240:243], v[56:59], a[212:215]\n"
"s_cmp_lg_u32 s63, 0\n"
"s_cbranch_scc1 LT13_%=\n"
"LR13_%=:\n"
"v_mfma_f32_16x16x32_bf16 v[16:19], v[208:211], v[136:139], v[112:115]\n"
"v_exp_f32 v92, v92\n"
"v_mfma_f32_16x16x32_bf16 v[16:19], v[212:215], v[160:163], v[16:19]\n"
"v_exp_f32 v93, v93\n"
"v_mfma_f32_16x16x32_bf16 v[16:19], v[216:219], v[184:187], v[16:19]\n"
"v_exp_f32 v94, v94\n"
"v_mfma_f32_16x16x32_bf16 v[16:19], v[220:223], v[188:191], v[16:19]\n"
"v_exp_f32 v95, v95\n"
"v_mfma_f32_16x16x32_bf16 v[24:27], v[208:211], v[140:143], v[116:119]\n"
"v_cvt_pk_bf16_f32 v89, v90, v91\n"
"v_cvt_pk_bf16_f32 v90, v92, v93\n"
"v_mfma_f32_16x16x32_bf16 v[24:27], v[212:215], v[164:167], v[24:27]\n"
"v_exp_f32 v96, v96\n"
"v_mfma_f32_16x16x32_bf16 v[24:27], v[216:219], a[216:219], v[24:27]\n"
"v_exp_f32 v97, v97\n"
"v_mfma_f32_16x16x32_bf16 v[24:27], v[220:223], a[236:239], v[24:27]\n"
"v_cvt_pk_bf16_f32 v91, v94, v95\n"
"v_cvt_pk_bf16_f32 v96, v96, v97\n"
"v_mfma_f32_16x16x32_bf16 v[32:35], v[208:211], v[144:147], v[120:123]\n"
"v_exp_f32 v98, v98\n"
"v_mfma_f32_16x16x32_bf16 v[32:35], v[212:215], v[168:171], v[32:35]\n"
"v_exp_f32 v99, v99\n"
"v_mfma_f32_16x16x32_bf16 v[32:35], v[216:219], a[220:223], v[32:35]\n"
"v_exp_f32 v100, v100\n"
"v_mfma_f32_16x16x32_bf16 v[32:35], v[220:223], a[240:243], v[32:35]\n"
"v_exp_f32 v101, v101\n"
"v_mfma_f32_16x16x32_bf16 v[40:43], v[208:211], v[148:151], v[124:127]\n"
"v_exp_f32 v102, v102\n"
"v_mfma_f32_16x16x32_bf16 v[40:43], v[212:215], v[172:175], v[40:43]\n"
"buffer_load_dwordx4 v2, s[40:43], s67 offen offset:1024 nt lds\n"
"v_mfma_f32_16x16x32_bf16 v[40:43], v[216:219], a[224:227], v[40:43]\n"
"s_waitcnt vmcnt(10)\n"
"s_barrier\n"
"v_mfma_f32_16x16x32_bf16 v[40:43], v[220:223], a[244:247], v[40:43]\n"
"ds_read_b64_tr_b16 v[192:193], v7 offset:32768\n"
"ds_read_b64_tr_b16 v[194:195], v7 offset:36864\n"
"v_mfma_f32_16x16x32_bf16 v[48:51], v[208:211], v[152:155], v[128:131]\n"
"ds_read_b64_tr_b16 v[196:197], v8 offset:32768\n"
"ds_read_b64_tr_b16 v[198:199], v8 offset:36864\n"
"v_mfma_f32_16x16x32_bf16 v[48:51], v[212:215], v[176:179], v[48:51]\n"
"v_exp_f32 v103, v103\n"
"v_mfma_f32_16x16x32_bf16 v[48:51], v[216:219], a[228:231], v[48:51]\n"
"ds_read_b64_tr_b16 v[200:201], v7 offset:33024\n"
"ds_read_b64_tr_b16 v[202:203], v7 offset:37120\n"
"v_mfma_f32_16x16x32_bf16 v[48:51], v[220:223], a[248:251], v[48:51]\n"
"v_cvt_pk_bf16_f32 v97, v98, v99\n"
"v_cvt_pk_bf16_f32 v98, v100, v101\n"
"v_mfma_f32_16x16x32_bf16 v[56:59], v[208:211], v[156:159], v[132:135]\n"
"v_exp_f32 v104, v104\n"
"v_mfma_f32_16x16x32_bf16 v[56:59], v[212:215], v[180:183], v[56:59]\n"
"v_exp_f32 v105, v105\n"
"v_mfma_f32_16x16x32_bf16 v[56:59], v[216:219], a[232:235], v[56:59]\n"
"v_cvt_pk_bf16_f32 v99, v102, v103\n"
"v_cvt_pk_bf16_f32 v104, v104, v105\n"
"v_mfma_f32_16x16x32_bf16 v[56:59], v[220:223], a[252:255], v[56:59]\n"
"v_exp_f32 v106, v106\n"
"v_mfma_f32_16x16x32_bf16 v[20:23], v[224:227], v[136:139], v[112:115]\n"
"v_exp_f32 v107, v107\n"
"v_mfma_f32_16x16x32_bf16 v[20:23], v[228:231], v[160:163], v[20:23]\n"
"v_exp_f32 v108, v108\n"
"v_mfma_f32_16x16x32_bf16 v[20:23], v[232:235], v[184:187], v[20:23]\n"
"buffer_load_dwordx4 v1, s[40:43], s67 offen offset:2048 nt lds\n"
"v_mfma_f32_16x16x32_bf16 v[20:23], v[236:239], v[188:191], v[20:23]\n"
"v_exp_f32 v109, v109\n"
"v_mfma_f32_16x16x32_bf16 v[28:31], v[224:227], v[140:143], v[116:119]\n"
"v_exp_f32 v110, v110\n"
"v_mfma_f32_16x16x32_bf16 v[28:31], v[228:231], v[164:167], v[28:31]\n"
"v_exp_f32 v111, v111\n"
"v_mfma_f32_16x16x32_bf16 v[28:31], v[232:235], a[216:219], v[28:31]\n"
"v_cvt_pk_bf16_f32 v105, v106, v107\n"
"v_cvt_pk_bf16_f32 v106, v108, v109\n"
"v_mfma_f32_16x16x32_bf16 v[28:31], v[236:239], a[236:239], v[28:31]\n"
"v_cvt_pk_bf16_f32 v107, v110, v111\n"
"ds_read_b64_tr_b16 v[204:205], v8 offset:33024\n"
"v_mfma_f32_16x16x32_bf16 v[36:39], v[224:227], v[144:147], v[120:123]\n"
"ds_read_b64_tr_b16 v[206:207], v8 offset:37120\n"
"ds_read_b64_tr_b16 v[208:209], v7 offset:33280\n"
"v_mfma_f32_16x16x32_bf16 v[36:39], v[228:231], v[168:171], v[36:39]\n"
"ds_read_b64_tr_b16 v[210:211], v7 offset:37376\n"
"ds_read_b64_tr_b16 v[212:213], v8 offset:33280\n"
"v_mfma_f32_16x16x32_bf16 v[36:39], v[232:235], a[220:223], v[36:39]\n"
"ds_read_b64_tr_b16 v[214:215], v8 offset:37376\n"
"ds_read_b64_tr_b16 v[216:217], v7 offset:33536\n"
"v_mfma_f32_16x16x32_bf16 v[36:39], v[236:239], a[240:243], v[36:39]\n"
"ds_read_b64_tr_b16 v[218:219], v7 offset:37632\n"
"ds_read_b64_tr_b16 v[220:221], v8 offset:33536\n"
"v_mfma_f32_16x16x32_bf16 v[44:47], v[224:227], v[148:151], v[124:127]\n"
"ds_read_b64_tr_b16 v[222:223], v8 offset:37632\n"
"v_max3_f32 v12, v16, v17, v18\n"
"v_mfma_f32_16x16x32_bf16 v[44:47], v[228:231], v[172:175], v[44:47]\n"
"buffer_load_dwordx4 v2, s[40:43], s67 offen offset:3072 nt lds\n"
"v_mfma_f32_16x16x32_bf16 v[44:47], v[232:235], a[224:227], v[44:47]\n"
"v_max3_f32 v13, v19, v24, v25\n"
"v_max3_f32 v14, v26, v27, v32\n"
"v_mfma_f32_16x16x32_bf16 v[44:47], v[236:239], a[244:247], v[44:47]\n"
"v_max3_f32 v12, v12, v33, v34\n"
"v_max3_f32 v13, v13, v35, v40\n"
"v_mfma_f32_16x16x32_bf16 v[52:55], v[224:227], v[152:155], v[128:131]\n"
"v_max3_f32 v14, v14, v41, v42\n"
"v_max3_f32 v12, v12, v43, v48\n"
"v_mfma_f32_16x16x32_bf16 v[52:55], v[228:231], v[176:179], v[52:55]\n"
"v_max3_f32 v13, v13, v49, v50\n"
"v_max3_f32 v14, v14, v51, v56\n"
"v_mfma_f32_16x16x32_bf16 v[52:55], v[232:235], a[228:231], v[52:55]\n"
"v_max3_f32 v12, v12, v57, v58\n"
"v_max3_f32 v13, v13, v59, v20\n"
"v_mfma_f32_16x16x32_bf16 v[52:55], v[236:239], a[248:251], v[52:55]\n"
"v_max3_f32 v14, v14, v21, v22\n"
"v_max3_f32 v12, v12, v23, v28\n"
"v_mfma_f32_16x16x32_bf16 v[60:63], v[224:227], v[156:159], v[132:135]\n"
"ds_read_b128 v[224:227], v6 offset:32768\n"
"v_max3_f32 v13, v13, v29, v30\n"
"v_mfma_f32_16x16x32_bf16 v[60:63], v[228:231], v[180:183], v[60:63]\n"
"ds_read_b128 v[228:231], v6 offset:33024\n"
"v_max3_f32 v14, v14, v31, v36\n"
"v_mfma_f32_16x16x32_bf16 v[60:63], v[232:235], a[232:235], v[60:63]\n"
"ds_read_b128 v[232:235], v6 offset:33280\n"
"v_max3_f32 v12, v12, v37, v38\n"
"v_mfma_f32_16x16x32_bf16 v[60:63], v[236:239], a[252:255], v[60:63]\n"
"ds_read_b128 v[236:239], v6 offset:33536\n"
"s_waitcnt lgkmcnt(4)\n"
"v_mfma_f32_16x16x32_bf16 a[0:3], v[192:195], v[64:67], a[0:3]\n"
"s_add_u32 m0, s60, 98304\n"
"s_add_u32 s68, s56, 90112\n"
"v_mfma_f32_16x16x32_bf16 a[8:11], v[192:195], v[72:75], a[8:11]\n"
"buffer_load_dwordx4 v1, s[36:39], s68 offen offset:0 nt lds\n"
"v_mfma_f32_16x16x32_bf16 a[16:19], v[192:195], v[80:83], a[16:19]\n"
"v_max3_f32 v13, v13, v39, v44\n"
"v_max3_f32 v14, v14, v45, v46\n"
"v_mfma_f32_16x16x32_bf16 a[24:27], v[192:195], v[88:91], a[24:27]\n"
"v_max3_f32 v12, v12, v47, v52\n"
"v_max3_f32 v13, v13, v53, v54\n"
"v_mfma_f32_16x16x32_bf16 a[32:35], v[192:195], v[96:99], a[32:35]\n"
"v_max3_f32 v14, v14, v55, v60\n"
"v_max3_f32 v12, v12, v61, v62\n"
"v_mfma_f32_16x16x32_bf16 a[40:43], v[192:195], v[104:107], a[40:43]\n"
"v_max3_f32 v12, v12, v13, v14\n"
"v_max_f32 v12, v12, v63\n"
"v_mfma_f32_16x16x32_bf16 a[4:7], v[196:199], v[64:67], a[4:7]\n"
"v_cmp_lt_f32 vcc, s78, v12\n"
"s_cbranch_vccnz LT14_%=\n"
"LR14_%=:\n"
"v_mfma_f32_16x16x32_bf16 a[12:15], v[196:199], v[72:75], a[12:15]\n"
"ds_read_b128 v[192:195], v6 offset:36864\n"
"v_add_u32 v7, 0x10000, v7\n"
"v_mfma_f32_16x16x32_bf16 a[20:23], v[196:199], v[80:83], a[20:23]\n"
"v_add_u32 v8, 0x10000, v8\n"
"v_mfma_f32_16x16x32_bf16 a[28:31], v[196:199], v[88:91], a[28:31]\n"
"v_exp_f32 v16, v16\n"
"v_mfma_f32_16x16x32_bf16 a[36:39], v[196:199], v[96:99], a[36:39]\n"
"v_exp_f32 v17, v17\n"
"v_mfma_f32_16x16x32_bf16 a[44:47], v[196:199], v[104:107], a[44:47]\n"
"buffer_load_dwordx4 v2, s[36:39], s68 offen offset:1024 nt lds\n"
"v_mfma_f32_16x16x32_bf16 a[48:51], v[200:203], v[64:67], a[48:51]\n"
"ds_read_b128 v[196:199], v6 offset:37120\n"
"v_cvt_pk_bf16_f32 v16, v16, v17\n"
"v_mfma_f32_16x16x32_bf16 a[56:59], v[200:203], v[72:75], a[56:59]\n"
"v_exp_f32 v18, v18\n"
"v_mfma_f32_16x16x32_bf16 a[64:67], v[200:203], v[80:83], a[64:67]\n"
"v_exp_f32 v19, v19\n"
"v_mfma_f32_16x16x32_bf16 a[72:75], v[200:203], v[88:91], a[72:75]\n"
"v_exp_f32 v20, v20\n"
"v_mfma_f32_16x16x32_bf16 a[80:83], v[200:203], v[96:99], a[80:83]\n"
"v_exp_f32 v21, v21\n"
"v_mfma_f32_16x16x32_bf16 a[88:91], v[200:203], v[104:107], a[88:91]\n"
"ds_read_b128 v[200:203], v6 offset:37376\n"
"v_cvt_pk_bf16_f32 v17, v18, v19\n"
"v_mfma_f32_16x16x32_bf16 a[52:55], v[204:207], v[64:67], a[52:55]\n"
"v_exp_f32 v22, v22\n"
"v_mfma_f32_16x16x32_bf16 a[60:63], v[204:207], v[72:75], a[60:63]\n"
"v_exp_f32 v23, v23\n"
"v_mfma_f32_16x16x32_bf16 a[68:71], v[204:207], v[80:83], a[68:71]\n"
"v_exp_f32 v24, v24\n"
"v_mfma_f32_16x16x32_bf16 a[76:79], v[204:207], v[88:91], a[76:79]\n"
"v_exp_f32 v25, v25\n"
"v_mfma_f32_16x16x32_bf16 a[84:87], v[204:207], v[96:99], a[84:87]\n"
"buffer_load_dwordx4 v1, s[36:39], s68 offen offset:2048 nt lds\n"
"v_mfma_f32_16x16x32_bf16 a[92:95], v[204:207], v[104:107], a[92:95]\n"
"ds_read_b128 v[204:207], v6 offset:37632\n"
"v_cvt_pk_bf16_f32 v18, v20, v21\n"
"v_mfma_f32_16x16x32_bf16 a[96:99], v[208:211], v[64:67], a[96:99]\n"
"v_cvt_pk_bf16_f32 v19, v22, v23\n"
"v_cvt_pk_bf16_f32 v24, v24, v25\n"
"v_mfma_f32_16x16x32_bf16 a[104:107], v[208:211], v[72:75], a[104:107]\n"
"v_exp_f32 v26, v26\n"
"v_mfma_f32_16x16x32_bf16 a[112:115], v[208:211], v[80:83], a[112:115]\n"
"v_exp_f32 v27, v27\n"
"v_mfma_f32_16x16x32_bf16 a[120:123], v[208:211], v[88:91], a[120:123]\n"
"v_exp_f32 v28, v28\n"
"v_mfma_f32_16x16x32_bf16 a[128:131], v[208:211], v[96:99], a[128:131]\n"
"v_exp_f32 v29, v29\n"
"v_mfma_f32_16x16x32_bf16 a[136:139], v[208:211], v[104:107], a[136:139]\n"
"v_exp_f32 v30, v30\n"
"v_mfma_f32_16x16x32_bf16 a[100:103], v[212:215], v[64:67], a[100:103]\n"
"v_exp_f32 v31, v31\n"
"v_mfma_f32_16x16x32_bf16 a[108:111], v[212:215], v[72:75], a[108:111]\n"
"v_cvt_pk_bf16_f32 v25, v26, v27\n"
"v_cvt_pk_bf16_f32 v26, v28, v29\n"
"v_mfma_f32_16x16x32_bf16 a[116:119], v[212:215], v[80:83], a[116:119]\n"
"v_exp_f32 v32, v32\n"
"v_mfma_f32_16x16x32_bf16 a[124:127], v[212:215], v[88:91], a[124:127]\n"
"buffer_load_dwordx4 v2, s[36:39], s68 offen offset:3072 nt lds\n"
"v_mfma_f32_16x16x32_bf16 a[132:135], v[212:215], v[96:99], a[132:135]\n"
"v_exp_f32 v33, v33\n"
"v_mfma_f32_16x16x32_bf16 a[140:143], v[212:215], v[104:107], a[140:143]\n"
"v_cvt_pk_bf16_f32 v27, v30, v31\n"
"v_cvt_pk_bf16_f32 v32, v32, v33\n"
"v_mfma_f32_16x16x32_bf16 a[144:147], v[216:219], v[64:67], a[144:147]\n"
"v_exp_f32 v34, v34\n"
"v_mfma_f32_16x16x32_bf16 a[152:155], v[216:219], v[72:75], a[152:155]\n"
"v_exp_f32 v35, v35\n"
"v_mfma_f32_16x16x32_bf16 a[160:163], v[216:219], v[80:83], a[160:163]\n"
"v_exp_f32 v36, v36\n"
"v_mfma_f32_16x16x32_bf16 a[168:171], v[216:219], v[88:91], a[168:171]\n"
"v_exp_f32 v37, v37\n"
"v_mfma_f32_16x16x32_bf16 a[176:179], v[216:219], v[96:99], a[176:179]\n"
"v_exp_f32 v38, v38\n"
"v_mfma_f32_16x16x32_bf16 a[184:187], v[216:219], v[104:107], a[184:187]\n"
"v_exp_f32 v39, v39\n"
"v_mfma_f32_16x16x32_bf16 a[148:151], v[220:223], v[64:67], a[148:151]\n"
"v_cvt_pk_bf16_f32 v33, v34, v35\n"
"v_cvt_pk_bf16_f32 v34, v36, v37\n"
"v_mfma_f32_16x16x32_bf16 a[156:159], v[220:223], v[72:75], a[156:159]\n"
"v_exp_f32 v40, v40\n"
"v_mfma_f32_16x16x32_bf16 a[164:167], v[220:223], v[80:83], a[164:167]\n"
"v_exp_f32 v41, v41\n"
"v_mfma_f32_16x16x32_bf16 a[172:175], v[220:223], v[88:91], a[172:175]\n"
"v_cvt_pk_bf16_f32 v35, v38, v39\n"
"v_cvt_pk_bf16_f32 v40, v40, v41\n"
"v_mfma_f32_16x16x32_bf16 a[180:183], v[220:223], v[96:99], a[180:183]\n"
"v_exp_f32 v42, v42\n"
"v_mfma_f32_16x16x32_bf16 a[188:191], v[220:223], v[104:107], a[188:191]\n"
"v_exp_f32 v43, v43\n"
"v_mfma_f32_16x16x32_bf16 a[192:195], v[240:243], v[64:67], a[192:195]\n"
"v_exp_f32 v44, v44\n"
"v_mfma_f32_16x16x32_bf16 a[196:199], v[240:243], v[72:75], a[196:199]\n"
"v_exp_f32 v45, v45\n"
"v_mfma_f32_16x16x32_bf16 a[200:203], v[240:243], v[80:83], a[200:203]\n"
"s_add_u32 m0, s60, 147456\n"
"s_add_u32 s69, s56, 81920\n"
"v_mfma_f32_16x16x32_bf16 a[204:207], v[240:243], v[88:91], a[204:207]\n"
"buffer_load_dwordx4 v1, s[40:43], s69 offen offset:0 nt lds\n"
"v_mfma_f32_16x16x32_bf16 a[208:211], v[240:243], v[96:99], a[208:211]\n"
"v_exp_f32 v46, v46\n"
"v_mfma_f32_16x16x32_bf16 a[212:215], v[240:243], v[104:107], a[212:215]\n"
"v_cvt_pk_bf16_f32 v41, v42, v43\n"
"s_waitcnt lgkmcnt(0)\n"
"v_mfma_f32_16x16x32_bf16 v[64:67], v[224:227], v[136:139], v[112:115]\n"
"s_cmp_lg_u32 s63, 0\n"
"s_cbranch_scc1 LT15_%=\n"
"LR15_%=:\n"
"v_mfma_f32_16x16x32_bf16 v[64:67], v[228:231], v[160:163], v[64:67]\n"
"v_exp_f32 v47, v47\n"
"v_mfma_f32_16x16x32_bf16 v[64:67], v[232:235], v[184:187], v[64:67]\n"
"v_exp_f32 v48, v48\n"
"v_mfma_f32_16x16x32_bf16 v[64:67], v[236:239], v[188:191], v[64:67]\n"
"v_exp_f32 v49, v49\n"
"v_mfma_f32_16x16x32_bf16 v[72:75], v[224:227], v[140:143], v[116:119]\n"
"v_cvt_pk_bf16_f32 v42, v44, v45\n"
"v_cvt_pk_bf16_f32 v43, v46, v47\n"
"v_mfma_f32_16x16x32_bf16 v[72:75], v[228:231], v[164:167], v[72:75]\n"
"v_exp_f32 v50, v50\n"
"v_mfma_f32_16x16x32_bf16 v[72:75], v[232:235], a[216:219], v[72:75]\n"
"v_exp_f32 v51, v51\n"
"v_mfma_f32_16x16x32_bf16 v[72:75], v[236:239], a[236:239], v[72:75]\n"
"v_exp_f32 v52, v52\n"
"v_mfma_f32_16x16x32_bf16 v[80:83], v[224:227], v[144:147], v[120:123]\n"
"v_exp_f32 v53, v53\n"
"v_mfma_f32_16x16x32_bf16 v[80:83], v[228:231], v[168:171], v[80:83]\n"
"v_exp_f32 v54, v54\n"
"v_mfma_f32_16x16x32_bf16 v[80:83], v[232:235], a[220:223], v[80:83]\n"
"v_exp_f32 v55, v55\n"
"v_mfma_f32_16x16x32_bf16 v[80:83], v[236:239], a[240:243], v[80:83]\n"
"v_cvt_pk_bf16_f32 v48, v48, v49\n"
"v_cvt_pk_bf16_f32 v49, v50, v51\n"
"v_mfma_f32_16x16x32_bf16 v[88:91], v[224:227], v[148:151], v[124:127]\n"
"v_exp_f32 v56, v56\n"
"v_mfma_f32_16x16x32_bf16 v[88:91], v[228:231], v[172:175], v[88:91]\n"
"buffer_load_dwordx4 v2, s[40:43], s69 offen offset:1024 nt lds\n"
"v_mfma_f32_16x16x32_bf16 v[88:91], v[232:235], a[224:227], v[88:91]\n"
"s_waitcnt vmcnt(10)\n"
"s_barrier\n"
"v_mfma_f32_16x16x32_bf16 v[88:91], v[236:239], a[244:247], v[88:91]\n"
"ds_read_b64_tr_b16 v[208:209], v7 offset:0\n"
"ds_read_b64_tr_b16 v[210:211], v7 offset:4096\n"
"v_mfma_f32_16x16x32_bf16 v[96:99], v[224:227], v[152:155], v[128:131]\n"
"ds_read_b64_tr_b16 v[212:213], v8 offset:0\n"
"ds_read_b64_tr_b16 v[214:215], v8 offset:4096\n"
"v_mfma_f32_16x16x32_bf16 v[96:99], v[228:231], v[176:179], v[96:99]\n"
"ds_read_b64_tr_b16 v[216:217], v7 offset:256\n"
"ds_read_b64_tr_b16 v[218:219], v7 offset:4352\n"
"v_mfma_f32_16x16x32_bf16 v[96:99], v[232:235], a[228:231], v[96:99]\n"
"v_exp_f32 v57, v57\n"
"v_mfma_f32_16x16x32_bf16 v[96:99], v[236:239], a[248:251], v[96:99]\n"
"v_cvt_pk_bf16_f32 v50, v52, v53\n"
"v_cvt_pk_bf16_f32 v51, v54, v55\n"
"v_mfma_f32_16x16x32_bf16 v[104:107], v[224:227], v[156:159], v[132:135]\n"
"v_exp_f32 v58, v58\n"
"v_mfma_f32_16x16x32_bf16 v[104:107], v[228:231], v[180:183], v[104:107]\n"
"v_exp_f32 v59, v59\n"
"v_mfma_f32_16x16x32_bf16 v[104:107], v[232:235], a[232:235], v[104:107]\n"
"v_exp_f32 v60, v60\n"
"v_mfma_f32_16x16x32_bf16 v[104:107], v[236:239], a[252:255], v[104:107]\n"
"v_exp_f32 v61, v61\n"
"v_mfma_f32_16x16x32_bf16 v[68:71], v[192:195], v[136:139], v[112:115]\n"
"v_exp_f32 v62, v62\n"
"v_mfma_f32_16x16x32_bf16 v[68:71], v[196:199], v[160:163], v[68:71]\n"
"v_exp_f32 v63, v63\n"
"v_mfma_f32_16x16x32_bf16 v[68:71], v[200:203], v[184:187], v[68:71]\n"
"buffer_load_dwordx4 v1, s[40:43], s69 offen offset:2048 nt lds\n"
"v_mfma_f32_16x16x32_bf16 v[68:71], v[204:207], v[188:191], v[68:71]\n"
"v_cvt_pk_bf16_f32 v56, v56, v57\n"
"v_cvt_pk_bf16_f32 v57, v58, v59\n"
"v_mfma_f32_16x16x32_bf16 v[76:79], v[192:195], v[140:143], v[116:119]\n"
"v_cvt_pk_bf16_f32 v58, v60, v61\n"
"v_cvt_pk_bf16_f32 v59, v62, v63\n"
"v_mfma_f32_16x16x32_bf16 v[76:79], v[196:199], v[164:167], v[76:79]\n"
"ds_read_b64_tr_b16 v[220:221], v8 offset:256\n"
"ds_read_b64_tr_b16 v[222:223], v8 offset:4352\n"
"v_mfma_f32_16x16x32_bf16 v[76:79], v[200:203], a[216:219], v[76:79]\n"
"ds_read_b64_tr_b16 v[224:225], v7 offset:512\n"
"ds_read_b64_tr_b16 v[226:227], v7 offset:4608\n"
"v_mfma_f32_16x16x32_bf16 v[76:79], v[204:207], a[236:239], v[76:79]\n"
"ds_read_b64_tr_b16 v[228:229], v8 offset:512\n"
"ds_read_b64_tr_b16 v[230:231], v8 offset:4608\n"
"v_mfma_f32_16x16x32_bf16 v[84:87], v[192:195], v[144:147], v[120:123]\n"
"ds_read_b64_tr_b16 v[232:233], v7 offset:768\n"
"ds_read_b64_tr_b16 v[234:235], v7 offset:4864\n"
"v_mfma_f32_16x16x32_bf16 v[84:87], v[196:199], v[168:171], v[84:87]\n"
"ds_read_b64_tr_b16 v[236:237], v8 offset:768\n"
"ds_read_b64_tr_b16 v[238:239], v8 offset:4864\n"
"v_mfma_f32_16x16x32_bf16 v[84:87], v[200:203], a[220:223], v[84:87]\n"
"v_add_u32 v6, 0x10000, v6\n"
"v_max3_f32 v12, v64, v65, v66\n"
"v_mfma_f32_16x16x32_bf16 v[84:87], v[204:207], a[240:243], v[84:87]\n"
"v_max3_f32 v13, v67, v72, v73\n"
"v_max3_f32 v14, v74, v75, v80\n"
"v_mfma_f32_16x16x32_bf16 v[92:95], v[192:195], v[148:151], v[124:127]\n"
"v_max3_f32 v12, v12, v81, v82\n"
"v_max3_f32 v13, v13, v83, v88\n"
"v_mfma_f32_16x16x32_bf16 v[92:95], v[196:199], v[172:175], v[92:95]\n"
"buffer_load_dwordx4 v2, s[40:43], s69 offen offset:3072 nt lds\n"
"v_mfma_f32_16x16x32_bf16 v[92:95], v[200:203], a[224:227], v[92:95]\n"
"v_max3_f32 v14, v14, v89, v90\n"
"v_max3_f32 v12, v12, v91, v96\n"
"v_mfma_f32_16x16x32_bf16 v[92:95], v[204:207], a[244:247], v[92:95]\n"
"v_max3_f32 v13, v13, v97, v98\n"
"v_max3_f32 v14, v14, v99, v104\n"
"v_mfma_f32_16x16x32_bf16 v[100:103], v[192:195], v[152:155], v[128:131]\n"
"v_max3_f32 v12, v12, v105, v106\n"
"v_max3_f32 v13, v13, v107, v68\n"
"v_mfma_f32_16x16x32_bf16 v[100:103], v[196:199], v[176:179], v[100:103]\n"
"v_max3_f32 v14, v14, v69, v70\n"
"v_max3_f32 v12, v12, v71, v76\n"
"v_mfma_f32_16x16x32_bf16 v[100:103], v[200:203], a[228:231], v[100:103]\n"
"v_max3_f32 v13, v13, v77, v78\n"
"v_max3_f32 v14, v14, v79, v84\n"
"v_mfma_f32_16x16x32_bf16 v[100:103], v[204:207], a[248:251], v[100:103]\n"
"v_max3_f32 v12, v12, v85, v86\n"
"v_max3_f32 v13, v13, v87, v92\n"
"v_mfma_f32_16x16x32_bf16 v[108:111], v[192:195], v[156:159], v[132:135]\n"
"ds_read_b128 v[192:195], v6 offset:0\n"
"v_max3_f32 v14, v14, v93, v94\n"
"v_mfma_f32_16x16x32_bf16 v[108:111], v[196:199], v[180:183], v[108:111]\n"
"ds_read_b128 v[196:199], v6 offset:256\n"
"v_mfma_f32_16x16x32_bf16 v[108:111], v[200:203], a[232:235], v[108:111]\n"
"ds_read_b128 v[200:203], v6 offset:512\n"
"v_max3_f32 v12, v12, v95, v100\n"
"v_mfma_f32_16x16x32_bf16 v[108:111], v[204:207], a[252:255], v[108:111]\n"
"ds_read_b128 v[204:207], v6 offset:768\n"
"s_waitcnt lgkmcnt(4)\n"
"v_mfma_f32_16x16x32_bf16 a[0:3], v[208:211], v[16:19], a[0:3]\n"
"s_add_u32 m0, s60, 131072\n"
"s_add_u32 s70, s56, 98304\n"
"v_mfma_f32_16x16x32_bf16 a[8:11], v[208:211], v[24:27], a[8:11]\n"
"buffer_load_dwordx4 v1, s[36:39], s70 offen offset:0 nt lds\n"
"v_mfma_f32_16x16x32_bf16 a[16:19], v[208:211], v[32:35], a[16:19]\n"
"v_max3_f32 v13, v13, v101, v102\n"
"v_max3_f32 v14, v14, v103, v108\n"
"v_mfma_f32_16x16x32_bf16 a[24:27], v[208:211], v[40:43], a[24:27]\n"
"v_max3_f32 v12, v12, v109, v110\n"
"v_max3_f32 v12, v12, v13, v14\n"
"v_mfma_f32_16x16x32_bf16 a[32:35], v[208:211], v[48:51], a[32:35]\n"
"v_max_f32 v12, v12, v111\n"
"v_cmp_lt_f32 vcc, s78, v12\n"
"v_mfma_f32_16x16x32_bf16 a[40:43], v[208:211], v[56:59], a[40:43]\n"
"s_cbranch_vccnz LT16_%=\n"
"LR16_%=:\n"
"ds_read_b128 v[208:211], v6 offset:4096\n"
"v_mfma_f32_16x16x32_bf16 a[4:7], v[212:215], v[16:19], a[4:7]\n"
"v_exp_f32 v64, v64\n"
"v_mfma_f32_16x16x32_bf16 a[12:15], v[212:215], v[24:27], a[12:15]\n"
"v_exp_f32 v65, v65\n"
"v_mfma_f32_16x16x32_bf16 a[20:23], v[212:215], v[32:35], a[20:23]\n"
"v_exp_f32 v66, v66\n"
"v_mfma_f32_16x16x32_bf16 a[28:31], v[212:215], v[40:43], a[28:31]\n"
"v_exp_f32 v67, v67\n"
"v_mfma_f32_16x16x32_bf16 a[36:39], v[212:215], v[48:51], a[36:39]\n"
"v_exp_f32 v68, v68\n"
"v_mfma_f32_16x16x32_bf16 a[44:47], v[212:215], v[56:59], a[44:47]\n"
"buffer_load_dwordx4 v2, s[36:39], s70 offen offset:1024 nt lds\n"
"v_mfma_f32_16x16x32_bf16 a[48:51], v[216:219], v[16:19], a[48:51]\n"
"ds_read_b128 v[212:215], v6 offset:4352\n"
"v_cvt_pk_bf16_f32 v64, v64, v65\n"
"v_mfma_f32_16x16x32_bf16 a[56:59], v[216:219], v[24:27], a[56:59]\n"
"v_exp_f32 v69, v69\n"
"v_mfma_f32_16x16x32_bf16 a[64:67], v[216:219], v[32:35], a[64:67]\n"
"v_exp_f32 v70, v70\n"
"v_mfma_f32_16x16x32_bf16 a[72:75], v[216:219], v[40:43], a[72:75]\n"
"v_exp_f32 v71, v71\n"
"v_mfma_f32_16x16x32_bf16 a[80:83], v[216:219], v[48:51], a[80:83]\n"
"v_cvt_pk_bf16_f32 v65, v66, v67\n"
"v_cvt_pk_bf16_f32 v66, v68, v69\n"
"v_mfma_f32_16x16x32_bf16 a[88:91], v[216:219], v[56:59], a[88:91]\n"
"ds_read_b128 v[216:219], v6 offset:4608\n"
"v_cvt_pk_bf16_f32 v67, v70, v71\n"
"v_mfma_f32_16x16x32_bf16 a[52:55], v[220:223], v[16:19], a[52:55]\n"
"v_exp_f32 v72, v72\n"
"v_mfma_f32_16x16x32_bf16 a[60:63], v[220:223], v[24:27], a[60:63]\n"
"v_exp_f32 v73, v73\n"
"v_mfma_f32_16x16x32_bf16 a[68:71], v[220:223], v[32:35], a[68:71]\n"
"v_exp_f32 v74, v74\n"
"v_mfma_f32_16x16x32_bf16 a[76:79], v[220:223], v[40:43], a[76:79]\n"
"v_exp_f32 v75, v75\n"
"v_mfma_f32_16x16x32_bf16 a[84:87], v[220:223], v[48:51], a[84:87]\n"
"buffer_load_dwordx4 v1, s[36:39], s70 offen offset:2048 nt lds\n"
"v_mfma_f32_16x16x32_bf16 a[92:95], v[220:223], v[56:59], a[92:95]\n"
"ds_read_b128 v[220:223], v6 offset:4864\n"
"v_cvt_pk_bf16_f32 v72, v72, v73\n"
"v_mfma_f32_16x16x32_bf16 a[96:99], v[224:227], v[16:19], a[96:99]\n"
"v_exp_f32 v76, v76\n"
"v_mfma_f32_16x16x32_bf16 a[104:107], v[224:227], v[24:27], a[104:107]\n"
"v_exp_f32 v77, v77\n"
"v_mfma_f32_16x16x32_bf16 a[112:115], v[224:227], v[32:35], a[112:115]\n"
"v_exp_f32 v78, v78\n"
"v_mfma_f32_16x16x32_bf16 a[120:123], v[224:227], v[40:43], a[120:123]\n"
"v_exp_f32 v79, v79\n"
"v_mfma_f32_16x16x32_bf16 a[128:131], v[224:227], v[48:51], a[128:131]\n"
"v_cvt_pk_bf16_f32 v73, v74, v75\n"
"v_cvt_pk_bf16_f32 v74, v76, v77\n"
"v_mfma_f32_16x16x32_bf16 a[136:139], v[224:227], v[56:59], a[136:139]\n"
"v_exp_f32 v80, v80\n"
"v_mfma_f32_16x16x32_bf16 a[100:103], v[228:231], v[16:19], a[100:103]\n"
"v_exp_f32 v81, v81\n"
"v_mfma_f32_16x16x32_bf16 a[108:111], v[228:231], v[24:27], a[108:111]\n"
"v_cvt_pk_bf16_f32 v75, v78, v79\n"
"v_cvt_pk_bf16_f32 v80, v80, v81\n"
"v_mfma_f32_16x16x32_bf16 a[116:119], v[228:231], v[32:35], a[116:119]\n"
"v_exp_f32 v82, v82\n"
"v_mfma_f32_16x16x32_bf16 a[124:127], v[228:231], v[40:43], a[124:127]\n"
"buffer_load_dwordx4 v2, s[36:39], s70 offen offset:3072 nt lds\n"
"v_mfma_f32_16x16x32_bf16 a[132:135], v[228:231], v[48:51], a[132:135]\n"
"v_exp_f32 v83, v83\n"
"v_mfma_f32_16x16x32_bf16 a[140:143], v[228:231], v[56:59], a[140:143]\n"
"v_exp_f32 v84, v84\n"
"v_mfma_f32_16x16x32_bf16 a[144:147], v[232:235], v[16:19], a[144:147]\n"
"v_exp_f32 v85, v85\n"
"v_mfma_f32_16x16x32_bf16 a[152:155], v[232:235], v[24:27], a[152:155]\n"
"v_exp_f32 v86, v86\n"
"v_mfma_f32_16x16x32_bf16 a[160:163], v[232:235], v[32:35], a[160:163]\n"
"v_exp_f32 v87, v87\n"
"v_mfma_f32_16x16x32_bf16 a[168:171], v[232:235], v[40:43], a[168:171]\n"
"v_cvt_pk_bf16_f32 v81, v82, v83\n"
"v_cvt_pk_bf16_f32 v82, v84, v85\n"
"v_mfma_f32_16x16x32_bf16 a[176:179], v[232:235], v[48:51], a[176:179]\n"
"v_exp_f32 v88, v88\n"
"v_mfma_f32_16x16x32_bf16 a[184:187], v[232:235], v[56:59], a[184:187]\n"
"v_exp_f32 v89, v89\n"
"v_mfma_f32_16x16x32_bf16 a[148:151], v[236:239], v[16:19], a[148:151]\n"
"v_cvt_pk_bf16_f32 v83, v86, v87\n"
"v_cvt_pk_bf16_f32 v88, v88, v89\n"
"v_mfma_f32_16x16x32_bf16 a[156:159], v[236:239], v[24:27], a[156:159]\n"
"v_exp_f32 v90, v90\n"
"v_mfma_f32_16x16x32_bf16 a[164:167], v[236:239], v[32:35], a[164:167]\n"
"v_exp_f32 v91, v91\n"
"v_mfma_f32_16x16x32_bf16 a[172:175], v[236:239], v[40:43], a[172:175]\n"
"v_exp_f32 v92, v92\n"
"v_mfma_f32_16x16x32_bf16 a[180:183], v[236:239], v[48:51], a[180:183]\n"
"v_exp_f32 v93, v93\n"
"v_mfma_f32_16x16x32_bf16 a[188:191], v[236:239], v[56:59], a[188:191]\n"
"v_exp_f32 v94, v94\n"
"v_mfma_f32_16x16x32_bf16 a[192:195], v[240:243], v[16:19], a[192:195]\n"
"v_exp_f32 v95, v95\n"
"v_mfma_f32_16x16x32_bf16 a[196:199], v[240:243], v[24:27], a[196:199]\n"
"v_cvt_pk_bf16_f32 v89, v90, v91\n"
"v_cvt_pk_bf16_f32 v90, v92, v93\n"
"v_mfma_f32_16x16x32_bf16 a[200:203], v[240:243], v[32:35], a[200:203]\n"
"s_add_u32 m0, s60, 16384\n"
"s_add_u32 s71, s56, 90112\n"
"v_mfma_f32_16x16x32_bf16 a[204:207], v[240:243], v[40:43], a[204:207]\n"
"buffer_load_dwordx4 v1, s[40:43], s71 offen offset:0 nt lds\n"
"v_mfma_f32_16x16x32_bf16 a[208:211], v[240:243], v[48:51], a[208:211]\n"
"v_exp_f32 v96, v96\n"
"v_mfma_f32_16x16x32_bf16 a[212:215], v[240:243], v[56:59], a[212:215]\n"
"v_cvt_pk_bf16_f32 v91, v94, v95\n"
"s_waitcnt lgkmcnt(0)\n"
"v_mfma_f32_16x16x32_bf16 v[16:19], v[192:195], v[136:139], v[112:115]\n"
"s_cmp_lg_u32 s63, 0\n"
"s_cbranch_scc1 LT17_%=\n"
"LR17_%=:\n"
"v_mfma_f32_16x16x32_bf16 v[16:19], v[196:199], v[160:163], v[16:19]\n"
"v_exp_f32 v97, v97\n"
"v_mfma_f32_16x16x32_bf16 v[16:19], v[200:203], v[184:187], v[16:19]\n"
"v_exp_f32 v98, v98\n"
"v_mfma_f32_16x16x32_bf16 v[16:19], v[204:207], v[188:191], v[16:19]\n"
"v_exp_f32 v99, v99\n"
"v_mfma_f32_16x16x32_bf16 v[24:27], v[192:195], v[140:143], v[116:119]\n"
"v_exp_f32 v100, v100\n"
"v_mfma_f32_16x16x32_bf16 v[24:27], v[196:199], v[164:167], v[24:27]\n"
"v_exp_f32 v101, v101\n"
"v_mfma_f32_16x16x32_bf16 v[24:27], v[200:203], a[216:219], v[24:27]\n"
"v_exp_f32 v102, v102\n"
"v_mfma_f32_16x16x32_bf16 v[24:27], v[204:207], a[236:239], v[24:27]\n"
"v_exp_f32 v103, v103\n"
"v_mfma_f32_16x16x32_bf16 v[32:35], v[192:195], v[144:147], v[120:123]\n"
"v_cvt_pk_bf16_f32 v96, v96, v97\n"
"v_cvt_pk_bf16_f32 v97, v98, v99\n"
"v_mfma_f32_16x16x32_bf16 v[32:35], v[196:199], v[168:171], v[32:35]\n"
"v_exp_f32 v104, v104\n"
"v_mfma_f32_16x16x32_bf16 v[32:35], v[200:203], a[220:223], v[32:35]\n"
"v_exp_f32 v105, v105\n"
"v_mfma_f32_16x16x32_bf16 v[32:35], v[204:207], a[240:243], v[32:35]\n"
"v_cvt_pk_bf16_f32 v98, v100, v101\n"
"v_cvt_pk_bf16_f32 v99, v102, v103\n"
"v_mfma_f32_16x16x32_bf16 v[40:43], v[192:195], v[148:151], v[124:127]\n"
"v_exp_f32 v106, v106\n"
"v_mfma_f32_16x16x32_bf16 v[40:43], v[196:199], v[172:175], v[40:43]\n"
"buffer_load_dwordx4 v2, s[40:43], s71 offen offset:1024 nt lds\n"
"v_mfma_f32_16x16x32_bf16 v[40:43], v[200:203], a[224:227], v[40:43]\n"
"s_waitcnt vmcnt(10)\n"
"s_barrier\n"
"v_mfma_f32_16x16x32_bf16 v[40:43], v[204:207], a[244:247], v[40:43]\n"
"ds_read_b64_tr_b16 v[224:225], v7 offset:32768\n"
"ds_read_b64_tr_b16 v[226:227], v7 offset:36864\n"
"v_mfma_f32_16x16x32_bf16 v[48:51], v[192:195], v[152:155], v[128:131]\n"
"ds_read_b64_tr_b16 v[228:229], v8 offset:32768\n"
"ds_read_b64_tr_b16 v[230:231], v8 offset:36864\n"
"v_mfma_f32_16x16x32_bf16 v[48:51], v[196:199], v[176:179], v[48:51]\n"
"ds_read_b64_tr_b16 v[232:233], v7 offset:33024\n"
"ds_read_b64_tr_b16 v[234:235], v7 offset:37120\n"
"v_mfma_f32_16x16x32_bf16 v[48:51], v[200:203], a[228:231], v[48:51]\n"
"v_exp_f32 v107, v107\n"
"v_mfma_f32_16x16x32_bf16 v[48:51], v[204:207], a[248:251], v[48:51]\n"
"v_exp_f32 v108, v108\n"
"v_mfma_f32_16x16x32_bf16 v[56:59], v[192:195], v[156:159], v[132:135]\n"
"v_exp_f32 v109, v109\n"
"v_mfma_f32_16x16x32_bf16 v[56:59], v[196:199], v[180:183], v[56:59]\n"
"v_exp_f32 v110, v110\n"
"v_mfma_f32_16x16x32_bf16 v[56:59], v[200:203], a[232:235], v[56:59]\n"
"v_exp_f32 v111, v111\n"
"v_mfma_f32_16x16x32_bf16 v[56:59], v[204:207], a[252:255], v[56:59]\n"
"v_cvt_pk_bf16_f32 v104, v104, v105\n"
"v_cvt_pk_bf16_f32 v105, v106, v107\n"
"v_mfma_f32_16x16x32_bf16 v[20:23], v[208:211], v[136:139], v[112:115]\n"
"v_cvt_pk_bf16_f32 v106, v108, v109\n"
"v_cvt_pk_bf16_f32 v107, v110, v111\n"
"v_mfma_f32_16x16x32_bf16 v[20:23], v[212:215], v[160:163], v[20:23]\n"
"ds_read_b64_tr_b16 v[236:237], v8 offset:33024\n"
"ds_read_b64_tr_b16 v[238:239], v8 offset:37120\n"
"v_mfma_f32_16x16x32_bf16 v[20:23], v[216:219], v[184:187], v[20:23]\n"
"buffer_load_dwordx4 v1, s[40:43], s71 offen offset:2048 nt lds\n"
"v_mfma_f32_16x16x32_bf16 v[20:23], v[220:223], v[188:191], v[20:23]\n"
"ds_read_b64_tr_b16 v[192:193], v7 offset:33280\n"
"ds_read_b64_tr_b16 v[194:195], v7 offset:37376\n"
"v_mfma_f32_16x16x32_bf16 v[28:31], v[208:211], v[140:143], v[116:119]\n"
"ds_read_b64_tr_b16 v[196:197], v8 offset:33280\n"
"ds_read_b64_tr_b16 v[198:199], v8 offset:37376\n"
"v_mfma_f32_16x16x32_bf16 v[28:31], v[212:215], v[164:167], v[28:31]\n"
"ds_read_b64_tr_b16 v[200:201], v7 offset:33536\n"
"ds_read_b64_tr_b16 v[202:203], v7 offset:37632\n"
"v_mfma_f32_16x16x32_bf16 v[28:31], v[216:219], a[216:219], v[28:31]\n"
"ds_read_b64_tr_b16 v[204:205], v8 offset:33536\n"
"ds_read_b64_tr_b16 v[206:207], v8 offset:37632\n"
"v_mfma_f32_16x16x32_bf16 v[28:31], v[220:223], a[236:239], v[28:31]\n"
"v_max3_f32 v12, v16, v17, v18\n"
"v_max3_f32 v13, v19, v24, v25\n"
"v_mfma_f32_16x16x32_bf16 v[36:39], v[208:211], v[144:147], v[120:123]\n"
"v_max3_f32 v14, v26, v27, v32\n"
"v_max3_f32 v12, v12, v33, v34\n"
"v_mfma_f32_16x16x32_bf16 v[36:39], v[212:215], v[168:171], v[36:39]\n"
"v_max3_f32 v13, v13, v35, v40\n"
"v_max3_f32 v14, v14, v41, v42\n"
"v_mfma_f32_16x16x32_bf16 v[36:39], v[216:219], a[220:223], v[36:39]\n"
"v_max3_f32 v12, v12, v43, v48\n"
"v_max3_f32 v13, v13, v49, v50\n"
"v_mfma_f32_16x16x32_bf16 v[36:39], v[220:223], a[240:243], v[36:39]\n"
"v_max3_f32 v14, v14, v51, v56\n"
"v_max3_f32 v12, v12, v57, v58\n"
"v_mfma_f32_16x16x32_bf16 v[44:47], v[208:211], v[148:151], v[124:127]\n"
"v_max3_f32 v13, v13, v59, v20\n"
"v_max3_f32 v14, v14, v21, v22\n"
"v_mfma_f32_16x16x32_bf16 v[44:47], v[212:215], v[172:175], v[44:47]\n"
"buffer_load_dwordx4 v2, s[40:43], s71 offen offset:3072 nt lds\n"
"v_mfma_f32_16x16x32_bf16 v[44:47], v[216:219], a[224:227], v[44:47]\n"
"v_max3_f32 v12, v12, v23, v28\n"
"v_max3_f32 v13, v13, v29, v30\n"
"v_mfma_f32_16x16x32_bf16 v[44:47], v[220:223], a[244:247], v[44:47]\n"
"v_max3_f32 v14, v14, v31, v36\n"
"v_max3_f32 v12, v12, v37, v38\n"
"v_mfma_f32_16x16x32_bf16 v[52:55], v[208:211], v[152:155], v[128:131]\n"
"v_add_u32 v7, 0x10000, v7\n"
"v_add_u32 v8, 0x10000, v8\n"
"v_mfma_f32_16x16x32_bf16 v[52:55], v[212:215], v[176:179], v[52:55]\n"
"v_mfma_f32_16x16x32_bf16 v[52:55], v[216:219], a[228:231], v[52:55]\n"
"s_nop 0\n"
"v_max3_f32 v13, v13, v39, v44\n"
"v_max3_f32 v14, v14, v45, v46\n"
"v_mfma_f32_16x16x32_bf16 v[52:55], v[220:223], a[248:251], v[52:55]\n"
"v_mfma_f32_16x16x32_bf16 v[60:63], v[208:211], v[156:159], v[132:135]\n"
"ds_read_b128 v[208:211], v6 offset:32768\n"
"v_mfma_f32_16x16x32_bf16 v[60:63], v[212:215], v[180:183], v[60:63]\n"
"ds_read_b128 v[212:215], v6 offset:33024\n"
"v_mfma_f32_16x16x32_bf16 v[60:63], v[216:219], a[232:235], v[60:63]\n"
"ds_read_b128 v[216:219], v6 offset:33280\n"
"s_nop 1\n"
"v_max3_f32 v12, v12, v47, v52\n"
"v_mfma_f32_16x16x32_bf16 v[60:63], v[220:223], a[252:255], v[60:63]\n"
"ds_read_b128 v[220:223], v6 offset:33536\n"
"s_waitcnt lgkmcnt(4)\n"
"v_mfma_f32_16x16x32_bf16 a[0:3], v[224:227], v[64:67], a[0:3]\n"
"s_add_u32 m0, s60, 0\n"
"s_add_u32 s64, s56, 106496\n"
"v_mfma_f32_16x16x32_bf16 a[8:11], v[224:227], v[72:75], a[8:11]\n"
"buffer_load_dwordx4 v1, s[36:39], s64 offen offset:0 nt lds\n"
"v_mfma_f32_16x16x32_bf16 a[16:19], v[224:227], v[80:83], a[16:19]\n"
"v_max3_f32 v13, v13, v53, v54\n"
"v_max3_f32 v14, v14, v55, v60\n"
"v_mfma_f32_16x16x32_bf16 a[24:27], v[224:227], v[88:91], a[24:27]\n"
"v_max3_f32 v12, v12, v61, v62\n"
"v_max3_f32 v12, v12, v13, v14\n"
"v_mfma_f32_16x16x32_bf16 a[32:35], v[224:227], v[96:99], a[32:35]\n"
"v_max_f32 v12, v12, v63\n"
"v_cmp_lt_f32 vcc, s78, v12\n"
"v_mfma_f32_16x16x32_bf16 a[40:43], v[224:227], v[104:107], a[40:43]\n"
"s_cbranch_vccnz LT18_%=\n"
"LR18_%=:\n"
"ds_read_b128 v[224:227], v6 offset:36864\n"
"v_mfma_f32_16x16x32_bf16 a[4:7], v[228:231], v[64:67], a[4:7]\n"
"v_exp_f32 v16, v16\n"
"v_mfma_f32_16x16x32_bf16 a[12:15], v[228:231], v[72:75], a[12:15]\n"
"v_exp_f32 v17, v17\n"
"v_mfma_f32_16x16x32_bf16 a[20:23], v[228:231], v[80:83], a[20:23]\n"
"v_exp_f32 v18, v18\n"
"v_mfma_f32_16x16x32_bf16 a[28:31], v[228:231], v[88:91], a[28:31]\n"
"v_exp_f32 v19, v19\n"
"v_mfma_f32_16x16x32_bf16 a[36:39], v[228:231], v[96:99], a[36:39]\n"
"v_exp_f32 v20, v20\n"
"v_mfma_f32_16x16x32_bf16 a[44:47], v[228:231], v[104:107], a[44:47]\n"
"buffer_load_dwordx4 v2, s[36:39], s64 offen offset:1024 nt lds\n"
"v_mfma_f32_16x16x32_bf16 a[48:51], v[232:235], v[64:67], a[48:51]\n"
"ds_read_b128 v[228:231], v6 offset:37120\n"
"v_cvt_pk_bf16_f32 v16, v16, v17\n"
"v_mfma_f32_16x16x32_bf16 a[56:59], v[232:235], v[72:75], a[56:59]\n"
"v_exp_f32 v21, v21\n"
"v_mfma_f32_16x16x32_bf16 a[64:67], v[232:235], v[80:83], a[64:67]\n"
"v_exp_f32 v22, v22\n"
"v_mfma_f32_16x16x32_bf16 a[72:75], v[232:235], v[88:91], a[72:75]\n"
"v_exp_f32 v23, v23\n"
"v_mfma_f32_16x16x32_bf16 a[80:83], v[232:235], v[96:99], a[80:83]\n"
"v_cvt_pk_bf16_f32 v17, v18, v19\n"
"v_cvt_pk_bf16_f32 v18, v20, v21\n"
"v_mfma_f32_16x16x32_bf16 a[88:91], v[232:235], v[104:107], a[88:91]\n"
"ds_read_b128 v[232:235], v6 offset:37376\n"
"v_cvt_pk_bf16_f32 v19, v22, v23\n"
"v_mfma_f32_16x16x32_bf16 a[52:55], v[236:239], v[64:67], a[52:55]\n"
"v_exp_f32 v24, v24\n"
"v_mfma_f32_16x16x32_bf16 a[60:63], v[236:239], v[72:75], a[60:63]\n"
"v_exp_f32 v25, v25\n"
"v_mfma_f32_16x16x32_bf16 a[68:71], v[236:239], v[80:83], a[68:71]\n"
"v_exp_f32 v26, v26\n"
"v_mfma_f32_16x16x32_bf16 a[76:79], v[236:239], v[88:91], a[76:79]\n"
"v_exp_f32 v27, v27\n"
"v_mfma_f32_16x16x32_bf16 a[84:87], v[236:239], v[96:99], a[84:87]\n"
"buffer_load_dwordx4 v1, s[36:39], s64 offen offset:2048 nt lds\n"
"v_mfma_f32_16x16x32_bf16 a[92:95], v[236:239], v[104:107], a[92:95]\n"
"ds_read_b128 v[236:239], v6 offset:37632\n"
"v_cvt_pk_bf16_f32 v24, v24, v25\n"
"v_mfma_f32_16x16x32_bf16 a[96:99], v[192:195], v[64:67], a[96:99]\n"
"v_exp_f32 v28, v28\n"
"v_mfma_f32_16x16x32_bf16 a[104:107], v[192:195], v[72:75], a[104:107]\n"
"v_exp_f32 v29, v29\n"
"v_mfma_f32_16x16x32_bf16 a[112:115], v[192:195], v[80:83], a[112:115]\n"
"v_exp_f32 v30, v30\n"
"v_mfma_f32_16x16x32_bf16 a[120:123], v[192:195], v[88:91], a[120:123]\n"
"v_exp_f32 v31, v31\n"
"v_mfma_f32_16x16x32_bf16 a[128:131], v[192:195], v[96:99], a[128:131]\n"
"v_cvt_pk_bf16_f32 v25, v26, v27\n"
"v_cvt_pk_bf16_f32 v26, v28, v29\n"
"v_mfma_f32_16x16x32_bf16 a[136:139], v[192:195], v[104:107], a[136:139]\n"
"v_exp_f32 v32, v32\n"
"v_mfma_f32_16x16x32_bf16 a[100:103], v[196:199], v[64:67], a[100:103]\n"
"v_exp_f32 v33, v33\n"
"v_mfma_f32_16x16x32_bf16 a[108:111], v[196:199], v[72:75], a[108:111]\n"
"v_cvt_pk_bf16_f32 v27, v30, v31\n"
"v_cvt_pk_bf16_f32 v32, v32, v33\n"
"v_mfma_f32_16x16x32_bf16 a[116:119], v[196:199], v[80:83], a[116:119]\n"
"v_exp_f32 v34, v34\n"
"v_mfma_f32_16x16x32_bf16 a[124:127], v[196:199], v[88:91], a[124:127]\n"
"buffer_load_dwordx4 v2, s[36:39], s64 offen offset:3072 nt lds\n"
"v_mfma_f32_16x16x32_bf16 a[132:135], v[196:199], v[96:99], a[132:135]\n"
"v_exp_f32 v35, v35\n"
"v_mfma_f32_16x16x32_bf16 a[140:143], v[196:199], v[104:107], a[140:143]\n"
"v_exp_f32 v36, v36\n"
"v_mfma_f32_16x16x32_bf16 a[144:147], v[200:203], v[64:67], a[144:147]\n"
"v_exp_f32 v37, v37\n"
"v_mfma_f32_16x16x32_bf16 a[152:155], v[200:203], v[72:75], a[152:155]\n"
"v_exp_f32 v38, v38\n"
"v_mfma_f32_16x16x32_bf16 a[160:163], v[200:203], v[80:83], a[160:163]\n"
"v_exp_f32 v39, v39\n"
"v_mfma_f32_16x16x32_bf16 a[168:171], v[200:203], v[88:91], a[168:171]\n"
"v_cvt_pk_bf16_f32 v33, v34, v35\n"
"v_cvt_pk_bf16_f32 v34, v36, v37\n"
"v_mfma_f32_16x16x32_bf16 a[176:179], v[200:203], v[96:99], a[176:179]\n"
"v_exp_f32 v40, v40\n"
"v_mfma_f32_16x16x32_bf16 a[184:187], v[200:203], v[104:107], a[184:187]\n"
"v_exp_f32 v41, v41\n"
"v_mfma_f32_16x16x32_bf16 a[148:151], v[204:207], v[64:67], a[148:151]\n"
"v_cvt_pk_bf16_f32 v35, v38, v39\n"
"v_cvt_pk_bf16_f32 v40, v40, v41\n"
"v_mfma_f32_16x16x32_bf16 a[156:159], v[204:207], v[72:75], a[156:159]\n"
"v_exp_f32 v42, v42\n"
"v_mfma_f32_16x16x32_bf16 a[164:167], v[204:207], v[80:83], a[164:167]\n"
"v_exp_f32 v43, v43\n"
"v_mfma_f32_16x16x32_bf16 a[172:175], v[204:207], v[88:91], a[172:175]\n"
"v_exp_f32 v44, v44\n"
"v_mfma_f32_16x16x32_bf16 a[180:183], v[204:207], v[96:99], a[180:183]\n"
"v_exp_f32 v45, v45\n"
"v_mfma_f32_16x16x32_bf16 a[188:191], v[204:207], v[104:107], a[188:191]\n"
"v_exp_f32 v46, v46\n"
"v_mfma_f32_16x16x32_bf16 a[192:195], v[240:243], v[64:67], a[192:195]\n"
"v_exp_f32 v47, v47\n"
"v_mfma_f32_16x16x32_bf16 a[196:199], v[240:243], v[72:75], a[196:199]\n"
"v_cvt_pk_bf16_f32 v41, v42, v43\n"
"v_cvt_pk_bf16_f32 v42, v44, v45\n"
"v_mfma_f32_16x16x32_bf16 a[200:203], v[240:243], v[80:83], a[200:203]\n"
"s_add_u32 m0, s60, 49152\n"
"s_add_u32 s65, s56, 98304\n"
"v_mfma_f32_16x16x32_bf16 a[204:207], v[240:243], v[88:91], a[204:207]\n"
"buffer_load_dwordx4 v1, s[40:43], s65 offen offset:0 nt lds\n"
"v_mfma_f32_16x16x32_bf16 a[208:211], v[240:243], v[96:99], a[208:211]\n"
"v_exp_f32 v48, v48\n"
"v_mfma_f32_16x16x32_bf16 a[212:215], v[240:243], v[104:107], a[212:215]\n"
"v_cvt_pk_bf16_f32 v43, v46, v47\n"
"s_waitcnt lgkmcnt(0)\n"
"v_mfma_f32_16x16x32_bf16 v[64:67], v[208:211], v[136:139], v[112:115]\n"
"s_cmp_lg_u32 s63, 0\n"
"s_cbranch_scc1 LT19_%=\n"
"LR19_%=:\n"
"v_mfma_f32_16x16x32_bf16 v[64:67], v[212:215], v[160:163], v[64:67]\n"
"v_exp_f32 v49, v49\n"
"v_mfma_f32_16x16x32_bf16 v[64:67], v[216:219], v[184:187], v[64:67]\n"
"v_exp_f32 v50, v50\n"
"v_mfma_f32_16x16x32_bf16 v[64:67], v[220:223], v[188:191], v[64:67]\n"
"v_exp_f32 v51, v51\n"
"v_mfma_f32_16x16x32_bf16 v[72:75], v[208:211], v[140:143], v[116:119]\n"
"v_exp_f32 v52, v52\n"
"v_mfma_f32_16x16x32_bf16 v[72:75], v[212:215], v[164:167], v[72:75]\n"
"v_exp_f32 v53, v53\n"
"v_mfma_f32_16x16x32_bf16 v[72:75], v[216:219], a[216:219], v[72:75]\n"
"v_exp_f32 v54, v54\n"
"v_mfma_f32_16x16x32_bf16 v[72:75], v[220:223], a[236:239], v[72:75]\n"
"v_exp_f32 v55, v55\n"
"v_mfma_f32_16x16x32_bf16 v[80:83], v[208:211], v[144:147], v[120:123]\n"
"v_cvt_pk_bf16_f32 v48, v48, v49\n"
"v_cvt_pk_bf16_f32 v49, v50, v51\n"
"v_mfma_f32_16x16x32_bf16 v[80:83], v[212:215], v[168:171], v[80:83]\n"
"v_exp_f32 v56, v56\n"
"v_mfma_f32_16x16x32_bf16 v[80:83], v[216:219], a[220:223], v[80:83]\n"
"v_exp_f32 v57, v57\n"
"v_mfma_f32_16x16x32_bf16 v[80:83], v[220:223], a[240:243], v[80:83]\n"
"v_cvt_pk_bf16_f32 v50, v52, v53\n"
"v_cvt_pk_bf16_f32 v51, v54, v55\n"
"v_mfma_f32_16x16x32_bf16 v[88:91], v[208:211], v[148:151], v[124:127]\n"
"v_exp_f32 v58, v58\n"
"v_mfma_f32_16x16x32_bf16 v[88:91], v[212:215], v[172:175], v[88:91]\n"
"buffer_load_dwordx4 v2, s[40:43], s65 offen offset:1024 nt lds\n"
"v_mfma_f32_16x16x32_bf16 v[88:91], v[216:219], a[224:227], v[88:91]\n"
"s_waitcnt vmcnt(10)\n"
"s_barrier\n"
"v_mfma_f32_16x16x32_bf16 v[88:91], v[220:223], a[244:247], v[88:91]\n"
"ds_read_b64_tr_b16 v[192:193], v7 offset:0\n"
"ds_read_b64_tr_b16 v[194:195], v7 offset:4096\n"
"v_mfma_f32_16x16x32_bf16 v[96:99], v[208:211], v[152:155], v[128:131]\n"
"ds_read_b64_tr_b16 v[196:197], v8 offset:0\n"
"ds_read_b64_tr_b16 v[198:199], v8 offset:4096\n"
"v_mfma_f32_16x16x32_bf16 v[96:99], v[212:215], v[176:179], v[96:99]\n"
"ds_read_b64_tr_b16 v[200:201], v7 offset:256\n"
"ds_read_b64_tr_b16 v[202:203], v7 offset:4352\n"
"v_mfma_f32_16x16x32_bf16 v[96:99], v[216:219], a[228:231], v[96:99]\n"
"v_exp_f32 v59, v59\n"
"v_mfma_f32_16x16x32_bf16 v[96:99], v[220:223], a[248:251], v[96:99]\n"
"v_exp_f32 v60, v60\n"
"v_mfma_f32_16x16x32_bf16 v[104:107], v[208:211], v[156:159], v[132:135]\n"
"v_exp_f32 v61, v61\n"
"v_mfma_f32_16x16x32_bf16 v[104:107], v[212:215], v[180:183], v[104:107]\n"
"v_exp_f32 v62, v62\n"
"v_mfma_f32_16x16x32_bf16 v[104:107], v[216:219], a[232:235], v[104:107]\n"
"v_exp_f32 v63, v63\n"
"v_mfma_f32_16x16x32_bf16 v[104:107], v[220:223], a[252:255], v[104:107]\n"
"v_cvt_pk_bf16_f32 v56, v56, v57\n"
"v_cvt_pk_bf16_f32 v57, v58, v59\n"
"v_mfma_f32_16x16x32_bf16 v[68:71], v[224:227], v[136:139], v[112:115]\n"
"v_cvt_pk_bf16_f32 v58, v60, v61\n"
"v_cvt_pk_bf16_f32 v59, v62, v63\n"
"v_mfma_f32_16x16x32_bf16 v[68:71], v[228:231], v[160:163], v[68:71]\n"
"ds_read_b64_tr_b16 v[204:205], v8 offset:256\n"
"ds_read_b64_tr_b16 v[206:207], v8 offset:4352\n"
"v_mfma_f32_16x16x32_bf16 v[68:71], v[232:235], v[184:187], v[68:71]\n"
"buffer_load_dwordx4 v1, s[40:43], s65 offen offset:2048 nt lds\n"
"v_mfma_f32_16x16x32_bf16 v[68:71], v[236:239], v[188:191], v[68:71]\n"
"ds_read_b64_tr_b16 v[208:209], v7 offset:512\n"
"ds_read_b64_tr_b16 v[210:211], v7 offset:4608\n"
"v_mfma_f32_16x16x32_bf16 v[76:79], v[224:227], v[140:143], v[116:119]\n"
"ds_read_b64_tr_b16 v[212:213], v8 offset:512\n"
"ds_read_b64_tr_b16 v[214:215], v8 offset:4608\n"
"v_mfma_f32_16x16x32_bf16 v[76:79], v[228:231], v[164:167], v[76:79]\n"
"ds_read_b64_tr_b16 v[216:217], v7 offset:768\n"
"ds_read_b64_tr_b16 v[218:219], v7 offset:4864\n"
"v_mfma_f32_16x16x32_bf16 v[76:79], v[232:235], a[216:219], v[76:79]\n"
"ds_read_b64_tr_b16 v[220:221], v8 offset:768\n"
"ds_read_b64_tr_b16 v[222:223], v8 offset:4864\n"
"v_mfma_f32_16x16x32_bf16 v[76:79], v[236:239], a[236:239], v[76:79]\n"
"v_add_u32 v6, 0x10000, v6\n"
"v_max3_f32 v12, v64, v65, v66\n"
"v_mfma_f32_16x16x32_bf16 v[84:87], v[224:227], v[144:147], v[120:123]\n"
"v_max3_f32 v13, v67, v72, v73\n"
"v_max3_f32 v14, v74, v75, v80\n"
"v_mfma_f32_16x16x32_bf16 v[84:87], v[228:231], v[168:171], v[84:87]\n"
"v_max3_f32 v12, v12, v81, v82\n"
"v_max3_f32 v13, v13, v83, v88\n"
"v_mfma_f32_16x16x32_bf16 v[84:87], v[232:235], a[220:223], v[84:87]\n"
"v_max3_f32 v14, v14, v89, v90\n"
"v_max3_f32 v12, v12, v91, v96\n"
"v_mfma_f32_16x16x32_bf16 v[84:87], v[236:239], a[240:243], v[84:87]\n"
"v_max3_f32 v13, v13, v97, v98\n"
"v_max3_f32 v14, v14, v99, v104\n"
"v_mfma_f32_16x16x32_bf16 v[92:95], v[224:227], v[148:151], v[124:127]\n"
"v_max3_f32 v12, v12, v105, v106\n"
"v_max3_f32 v13, v13, v107, v68\n"
"v_mfma_f32_16x16x32_bf16 v[92:95], v[228:231], v[172:175], v[92:95]\n"
"buffer_load_dwordx4 v2, s[40:43], s65 offen offset:3072 nt lds\n"
"v_mfma_f32_16x16x32_bf16 v[92:95], v[232:235], a[224:227], v[92:95]\n"
"v_max3_f32 v14, v14, v69, v70\n"
"v_max3_f32 v12, v12, v71, v76\n"
"v_mfma_f32_16x16x32_bf16 v[92:95], v[236:239], a[244:247], v[92:95]\n"
"v_max3_f32 v13, v13, v77, v78\n"
"v_max3_f32 v14, v14, v79, v84\n"
"v_mfma_f32_16x16x32_bf16 v[100:103], v[224:227], v[152:155], v[128:131]\n"
"v_max3_f32 v12, v12, v85, v86\n"
"v_add_u32 v7, 0xfffe0000, v7\n"
"v_mfma_f32_16x16x32_bf16 v[100:103], v[228:231], v[176:179], v[100:103]\n"
"v_add_u32 v8, 0xfffe0000, v8\n"
"v_mfma_f32_16x16x32_bf16 v[100:103], v[232:235], a[228:231], v[100:103]\n"
"v_max3_f32 v13, v13, v87, v92\n"
"v_max3_f32 v14, v14, v93, v94\n"
"v_mfma_f32_16x16x32_bf16 v[100:103], v[236:239], a[248:251], v[100:103]\n"
"v_mfma_f32_16x16x32_bf16 v[108:111], v[224:227], v[156:159], v[132:135]\n"
"ds_read_b128 v[224:227], v6 offset:0\n"
"v_mfma_f32_16x16x32_bf16 v[108:111], v[228:231], v[180:183], v[108:111]\n"
"ds_read_b128 v[228:231], v6 offset:256\n"
"v_mfma_f32_16x16x32_bf16 v[108:111], v[232:235], a[232:235], v[108:111]\n"
"ds_read_b128 v[232:235], v6 offset:512\n"
"s_nop 1\n"
"v_max3_f32 v12, v12, v95, v100\n"
"v_mfma_f32_16x16x32_bf16 v[108:111], v[236:239], a[252:255], v[108:111]\n"
"ds_read_b128 v[236:239], v6 offset:768\n"
"s_waitcnt lgkmcnt(4)\n"
"v_mfma_f32_16x16x32_bf16 a[0:3], v[192:195], v[16:19], a[0:3]\n"
"s_add_u32 m0, s60, 32768\n"
"s_add_u32 s66, s56, 114688\n"
"v_mfma_f32_16x16x32_bf16 a[8:11], v[192:195], v[24:27], a[8:11]\n"
"buffer_load_dwordx4 v1, s[36:39], s66 offen offset:0 nt lds\n"
"v_mfma_f32_16x16x32_bf16 a[16:19], v[192:195], v[32:35], a[16:19]\n"
"v_max3_f32 v13, v13, v101, v102\n"
"v_max3_f32 v14, v14, v103, v108\n"
"v_mfma_f32_16x16x32_bf16 a[24:27], v[192:195], v[40:43], a[24:27]\n"
"v_max3_f32 v12, v12, v109, v110\n"
"v_max3_f32 v12, v12, v13, v14\n"
"v_mfma_f32_16x16x32_bf16 a[32:35], v[192:195], v[48:51], a[32:35]\n"
"v_max_f32 v12, v12, v111\n"
"v_cmp_lt_f32 vcc, s78, v12\n"
"v_mfma_f32_16x16x32_bf16 a[40:43], v[192:195], v[56:59], a[40:43]\n"
"s_cbranch_vccnz LT20_%=\n"
"LR20_%=:\n"
"ds_read_b128 v[192:195], v6 offset:4096\n"
"v_mfma_f32_16x16x32_bf16 a[4:7], v[196:199], v[16:19], a[4:7]\n"
"v_exp_f32 v64, v64\n"
"v_mfma_f32_16x16x32_bf16 a[12:15], v[196:199], v[24:27], a[12:15]\n"
"v_exp_f32 v65, v65\n"
"v_mfma_f32_16x16x32_bf16 a[20:23], v[196:199], v[32:35], a[20:23]\n"
"v_exp_f32 v66, v66\n"
"v_mfma_f32_16x16x32_bf16 a[28:31], v[196:199], v[40:43], a[28:31]\n"
"v_exp_f32 v67, v67\n"
"v_mfma_f32_16x16x32_bf16 a[36:39], v[196:199], v[48:51], a[36:39]\n"
"v_exp_f32 v68, v68\n"
"v_mfma_f32_16x16x32_bf16 a[44:47], v[196:199], v[56:59], a[44:47]\n"
"buffer_load_dwordx4 v2, s[36:39], s66 offen offset:1024 nt lds\n"
"v_mfma_f32_16x16x32_bf16 a[48:51], v[200:203], v[16:19], a[48:51]\n"
"ds_read_b128 v[196:199], v6 offset:4352\n"
"v_cvt_pk_bf16_f32 v64, v64, v65\n"
"v_mfma_f32_16x16x32_bf16 a[56:59], v[200:203], v[24:27], a[56:59]\n"
"v_exp_f32 v69, v69\n"
"v_mfma_f32_16x16x32_bf16 a[64:67], v[200:203], v[32:35], a[64:67]\n"
"v_exp_f32 v70, v70\n"
"v_mfma_f32_16x16x32_bf16 a[72:75], v[200:203], v[40:43], a[72:75]\n"
"v_exp_f32 v71, v71\n"
"v_mfma_f32_16x16x32_bf16 a[80:83], v[200:203], v[48:51], a[80:83]\n"
"v_cvt_pk_bf16_f32 v65, v66, v67\n"
"v_cvt_pk_bf16_f32 v66, v68, v69\n"
"v_mfma_f32_16x16x32_bf16 a[88:91], v[200:203], v[56:59], a[88:91]\n"
"ds_read_b128 v[200:203], v6 offset:4608\n"
"v_cvt_pk_bf16_f32 v67, v70, v71\n"
"v_mfma_f32_16x16x32_bf16 a[52:55], v[204:207], v[16:19], a[52:55]\n"
"v_exp_f32 v72, v72\n"
"v_mfma_f32_16x16x32_bf16 a[60:63], v[204:207], v[24:27], a[60:63]\n"
"v_exp_f32 v73, v73\n"
"v_mfma_f32_16x16x32_bf16 a[68:71], v[204:207], v[32:35], a[68:71]\n"
"v_exp_f32 v74, v74\n"
"v_mfma_f32_16x16x32_bf16 a[76:79], v[204:207], v[40:43], a[76:79]\n"
"v_exp_f32 v75, v75\n"
"v_mfma_f32_16x16x32_bf16 a[84:87], v[204:207], v[48:51], a[84:87]\n"
"buffer_load_dwordx4 v1, s[36:39], s66 offen offset:2048 nt lds\n"
"v_mfma_f32_16x16x32_bf16 a[92:95], v[204:207], v[56:59], a[92:95]\n"
"ds_read_b128 v[204:207], v6 offset:4864\n"
"v_cvt_pk_bf16_f32 v72, v72, v73\n"
"v_mfma_f32_16x16x32_bf16 a[96:99], v[208:211], v[16:19], a[96:99]\n"
"v_exp_f32 v76, v76\n"
"v_mfma_f32_16x16x32_bf16 a[104:107], v[208:211], v[24:27], a[104:107]\n"
"v_exp_f32 v77, v77\n"
"v_mfma_f32_16x16x32_bf16 a[112:115], v[208:211], v[32:35], a[112:115]\n"
"v_exp_f32 v78, v78\n"
"v_mfma_f32_16x16x32_bf16 a[120:123], v[208:211], v[40:43], a[120:123]\n"
"v_exp_f32 v79, v79\n"
"v_mfma_f32_16x16x32_bf16 a[128:131], v[208:211], v[48:51], a[128:131]\n"
"v_cvt_pk_bf16_f32 v73, v74, v75\n"
"v_cvt_pk_bf16_f32 v74, v76, v77\n"
"v_mfma_f32_16x16x32_bf16 a[136:139], v[208:211], v[56:59], a[136:139]\n"
"v_exp_f32 v80, v80\n"
"v_mfma_f32_16x16x32_bf16 a[100:103], v[212:215], v[16:19], a[100:103]\n"
"v_exp_f32 v81, v81\n"
"v_mfma_f32_16x16x32_bf16 a[108:111], v[212:215], v[24:27], a[108:111]\n"
"v_cvt_pk_bf16_f32 v75, v78, v79\n"
"v_cvt_pk_bf16_f32 v80, v80, v81\n"
"v_mfma_f32_16x16x32_bf16 a[116:119], v[212:215], v[32:35], a[116:119]\n"
"v_exp_f32 v82, v82\n"
"v_mfma_f32_16x16x32_bf16 a[124:127], v[212:215], v[40:43], a[124:127]\n"
"buffer_load_dwordx4 v2, s[36:39], s66 offen offset:3072 nt lds\n"
"v_mfma_f32_16x16x32_bf16 a[132:135], v[212:215], v[48:51], a[132:135]\n"
"v_exp_f32 v83, v83\n"
"v_mfma_f32_16x16x32_bf16 a[140:143], v[212:215], v[56:59], a[140:143]\n"
"v_exp_f32 v84, v84\n"
"v_mfma_f32_16x16x32_bf16 a[144:147], v[216:219], v[16:19], a[144:147]\n"
"v_exp_f32 v85, v85\n"
"v_mfma_f32_16x16x32_bf16 a[152:155], v[216:219], v[24:27], a[152:155]\n"
"v_exp_f32 v86, v86\n"
"v_mfma_f32_16x16x32_bf16 a[160:163], v[216:219], v[32:35], a[160:163]\n"
"v_exp_f32 v87, v87\n"
"v_mfma_f32_16x16x32_bf16 a[168:171], v[216:219], v[40:43], a[168:171]\n"
"v_cvt_pk_bf16_f32 v81, v82, v83\n"
"v_cvt_pk_bf16_f32 v82, v84, v85\n"
"v_mfma_f32_16x16x32_bf16 a[176:179], v[216:219], v[48:51], a[176:179]\n"
"v_exp_f32 v88, v88\n"
"v_mfma_f32_16x16x32_bf16 a[184:187], v[216:219], v[56:59], a[184:187]\n"
"v_exp_f32 v89, v89\n"
"v_mfma_f32_16x16x32_bf16 a[148:151], v[220:223], v[16:19], a[148:151]\n"
"v_cvt_pk_bf16_f32 v83, v86, v87\n"
"v_cvt_pk_bf16_f32 v88, v88, v89\n"
"v_mfma_f32_16x16x32_bf16 a[156:159], v[220:223], v[24:27], a[156:159]\n"
"v_exp_f32 v90, v90\n"
"v_mfma_f32_16x16x32_bf16 a[164:167], v[220:223], v[32:35], a[164:167]\n"
"v_exp_f32 v91, v91\n"
"v_mfma_f32_16x16x32_bf16 a[172:175], v[220:223], v[40:43], a[172:175]\n"
"v_exp_f32 v92, v92\n"
"v_mfma_f32_16x16x32_bf16 a[180:183], v[220:223], v[48:51], a[180:183]\n"
"v_exp_f32 v93, v93\n"
"v_mfma_f32_16x16x32_bf16 a[188:191], v[220:223], v[56:59], a[188:191]\n"
"v_exp_f32 v94, v94\n"
"v_mfma_f32_16x16x32_bf16 a[192:195], v[240:243], v[16:19], a[192:195]\n"
"v_exp_f32 v95, v95\n"
"v_mfma_f32_16x16x32_bf16 a[196:199], v[240:243], v[24:27], a[196:199]\n"
"v_cvt_pk_bf16_f32 v89, v90, v91\n"
"v_cvt_pk_bf16_f32 v90, v92, v93\n"
"v_mfma_f32_16x16x32_bf16 a[200:203], v[240:243], v[32:35], a[200:203]\n"
"s_add_u32 m0, s60, 81920\n"
"s_add_u32 s67, s56, 106496\n"
"v_mfma_f32_16x16x32_bf16 a[204:207], v[240:243], v[40:43], a[204:207]\n"
"buffer_load_dwordx4 v1, s[40:43], s67 offen offset:0 nt lds\n"
"v_mfma_f32_16x16x32_bf16 a[208:211], v[240:243], v[48:51], a[208:211]\n"
"v_exp_f32 v96, v96\n"
"v_mfma_f32_16x16x32_bf16 a[212:215], v[240:243], v[56:59], a[212:215]\n"
"v_cvt_pk_bf16_f32 v91, v94, v95\n"
"s_waitcnt lgkmcnt(0)\n"
"v_mfma_f32_16x16x32_bf16 v[16:19], v[224:227], v[136:139], v[112:115]\n"
"s_cmp_lg_u32 s63, 0\n"
"s_cbranch_scc1 LT21_%=\n"
"LR21_%=:\n"
"v_mfma_f32_16x16x32_bf16 v[16:19], v[228:231], v[160:163], v[16:19]\n"
"v_exp_f32 v97, v97\n"
"v_mfma_f32_16x16x32_bf16 v[16:19], v[232:235], v[184:187], v[16:19]\n"
"v_exp_f32 v98, v98\n"
"v_mfma_f32_16x16x32_bf16 v[16:19], v[236:239], v[188:191], v[16:19]\n"
"v_exp_f32 v99, v99\n"
"v_mfma_f32_16x16x32_bf16 v[24:27], v[224:227], v[140:143], v[116:119]\n"
"v_exp_f32 v100, v100\n"
"v_mfma_f32_16x16x32_bf16 v[24:27], v[228:231], v[164:167], v[24:27]\n"
"v_exp_f32 v101, v101\n"
"v_mfma_f32_16x16x32_bf16 v[24:27], v[232:235], a[216:219], v[24:27]\n"
"v_exp_f32 v102, v102\n"
"v_mfma_f32_16x16x32_bf16 v[24:27], v[236:239], a[236:239], v[24:27]\n"
"v_exp_f32 v103, v103\n"
"v_mfma_f32_16x16x32_bf16 v[32:35], v[224:227], v[144:147], v[120:123]\n"
"v_cvt_pk_bf16_f32 v96, v96, v97\n"
"v_cvt_pk_bf16_f32 v97, v98, v99\n"
"v_mfma_f32_16x16x32_bf16 v[32:35], v[228:231], v[168:171], v[32:35]\n"
"v_exp_f32 v104, v104\n"
"v_mfma_f32_16x16x32_bf16 v[32:35], v[232:235], a[220:223], v[32:35]\n"
"v_exp_f32 v105, v105\n"
"v_mfma_f32_16x16x32_bf16 v[32:35], v[236:239], a[240:243], v[32:35]\n"
"v_cvt_pk_bf16_f32 v98, v100, v101\n"
"v_cvt_pk_bf16_f32 v99, v102, v103\n"
"v_mfma_f32_16x16x32_bf16 v[40:43], v[224:227], v[148:151], v[124:127]\n"
"v_exp_f32 v106, v106\n"
"v_mfma_f32_16x16x32_bf16 v[40:43], v[228:231], v[172:175], v[40:43]\n"
"buffer_load_dwordx4 v2, s[40:43], s67 offen offset:1024 nt lds\n"
"v_mfma_f32_16x16x32_bf16 v[40:43], v[232:235], a[224:227], v[40:43]\n"
"s_waitcnt vmcnt(10)\n"
"s_barrier\n"
"v_mfma_f32_16x16x32_bf16 v[40:43], v[236:239], a[244:247], v[40:43]\n"
"ds_read_b64_tr_b16 v[208:209], v7 offset:0\n"
"ds_read_b64_tr_b16 v[210:211], v7 offset:4096\n"
"v_mfma_f32_16x16x32_bf16 v[48:51], v[224:227], v[152:155], v[128:131]\n"
"ds_read_b64_tr_b16 v[212:213], v8 offset:0\n"
"ds_read_b64_tr_b16 v[214:215], v8 offset:4096\n"
"v_mfma_f32_16x16x32_bf16 v[48:51], v[228:231], v[176:179], v[48:51]\n"
"ds_read_b64_tr_b16 v[216:217], v7 offset:256\n"
"ds_read_b64_tr_b16 v[218:219], v7 offset:4352\n"
"v_mfma_f32_16x16x32_bf16 v[48:51], v[232:235], a[228:231], v[48:51]\n"
"v_exp_f32 v107, v107\n"
"v_mfma_f32_16x16x32_bf16 v[48:51], v[236:239], a[248:251], v[48:51]\n"
"v_exp_f32 v108, v108\n"
"v_mfma_f32_16x16x32_bf16 v[56:59], v[224:227], v[156:159], v[132:135]\n"
"v_exp_f32 v109, v109\n"
"v_mfma_f32_16x16x32_bf16 v[56:59], v[228:231], v[180:183], v[56:59]\n"
"v_exp_f32 v110, v110\n"
"v_mfma_f32_16x16x32_bf16 v[56:59], v[232:235], a[232:235], v[56:59]\n"
"v_exp_f32 v111, v111\n"
"v_mfma_f32_16x16x32_bf16 v[56:59], v[236:239], a[252:255], v[56:59]\n"
"v_cvt_pk_bf16_f32 v104, v104, v105\n"
"v_cvt_pk_bf16_f32 v105, v106, v107\n"
"v_mfma_f32_16x16x32_bf16 v[20:23], v[192:195], v[136:139], v[112:115]\n"
"v_cvt_pk_bf16_f32 v106, v108, v109\n"
"v_cvt_pk_bf16_f32 v107, v110, v111\n"
"v_mfma_f32_16x16x32_bf16 v[20:23], v[196:199], v[160:163], v[20:23]\n"
"ds_read_b64_tr_b16 v[220:221], v8 offset:256\n"
"ds_read_b64_tr_b16 v[222:223], v8 offset:4352\n"
"v_mfma_f32_16x16x32_bf16 v[20:23], v[200:203], v[184:187], v[20:23]\n"
"buffer_load_dwordx4 v1, s[40:43], s67 offen offset:2048 nt lds\n"
"v_mfma_f32_16x16x32_bf16 v[20:23], v[204:207], v[188:191], v[20:23]\n"
"ds_read_b64_tr_b16 v[224:225], v7 offset:512\n"
"ds_read_b64_tr_b16 v[226:227], v7 offset:4608\n"
"v_mfma_f32_16x16x32_bf16 v[28:31], v[192:195], v[140:143], v[116:119]\n"
"ds_read_b64_tr_b16 v[228:229], v8 offset:512\n"
"ds_read_b64_tr_b16 v[230:231], v8 offset:4608\n"
"v_mfma_f32_16x16x32_bf16 v[28:31], v[196:199], v[164:167], v[28:31]\n"
"ds_read_b64_tr_b16 v[232:233], v7 offset:768\n"
"ds_read_b64_tr_b16 v[234:235], v7 offset:4864\n"
"v_mfma_f32_16x16x32_bf16 v[28:31], v[200:203], a[216:219], v[28:31]\n"
"ds_read_b64_tr_b16 v[236:237], v8 offset:768\n"
"ds_read_b64_tr_b16 v[238:239], v8 offset:4864\n"
"v_mfma_f32_16x16x32_bf16 v[28:31], v[204:207], a[236:239], v[28:31]\n"
"v_add_u32 v6, 0xfffe0000, v6\n"
"v_max3_f32 v12, v16, v17, v18\n"
"v_mfma_f32_16x16x32_bf16 v[36:39], v[192:195], v[144:147], v[120:123]\n"
"v_max3_f32 v13, v19, v24, v25\n"
"v_max3_f32 v14, v26, v27, v32\n"
"v_mfma_f32_16x16x32_bf16 v[36:39], v[196:199], v[168:171], v[36:39]\n"
"v_max3_f32 v12, v12, v33, v34\n"
"v_max3_f32 v13, v13, v35, v40\n"
"v_mfma_f32_16x16x32_bf16 v[36:39], v[200:203], a[220:223], v[36:39]\n"
"v_max3_f32 v14, v14, v41, v42\n"
"v_max3_f32 v12, v12, v43, v48\n"
"v_mfma_f32_16x16x32_bf16 v[36:39], v[204:207], a[240:243], v[36:39]\n"
"v_max3_f32 v13, v13, v49, v50\n"
"v_max3_f32 v14, v14, v51, v56\n"
"v_mfma_f32_16x16x32_bf16 v[44:47], v[192:195], v[148:151], v[124:127]\n"
"v_max3_f32 v12, v12, v57, v58\n"
"v_max3_f32 v13, v13, v59, v20\n"
"v_mfma_f32_16x16x32_bf16 v[44:47], v[196:199], v[172:175], v[44:47]\n"
"buffer_load_dwordx4 v2, s[40:43], s67 offen offset:3072 nt lds\n"
"v_mfma_f32_16x16x32_bf16 v[44:47], v[200:203], a[224:227], v[44:47]\n"
"v_max3_f32 v14, v14, v21, v22\n"
"v_max3_f32 v12, v12, v23, v28\n"
"v_mfma_f32_16x16x32_bf16 v[44:47], v[204:207], a[244:247], v[44:47]\n"
"v_max3_f32 v13, v13, v29, v30\n"
"v_max3_f32 v14, v14, v31, v36\n"
"v_mfma_f32_16x16x32_bf16 v[52:55], v[192:195], v[152:155], v[128:131]\n"
"v_max3_f32 v12, v12, v37, v38\n"
"v_mfma_f32_16x16x32_bf16 v[52:55], v[196:199], v[176:179], v[52:55]\n"
"v_mfma_f32_16x16x32_bf16 v[52:55], v[200:203], a[228:231], v[52:55]\n"
"s_nop 1\n"
"v_max3_f32 v13, v13, v39, v44\n"
"v_max3_f32 v14, v14, v45, v46\n"
"v_mfma_f32_16x16x32_bf16 v[52:55], v[204:207], a[248:251], v[52:55]\n"
"v_mfma_f32_16x16x32_bf16 v[60:63], v[192:195], v[156:159], v[132:135]\n"
"ds_read_b128 v[192:195], v6 offset:0\n"
"v_mfma_f32_16x16x32_bf16 v[60:63], v[196:199], v[180:183], v[60:63]\n"
"ds_read_b128 v[196:199], v6 offset:256\n"
"v_mfma_f32_16x16x32_bf16 v[60:63], v[200:203], a[232:235], v[60:63]\n"
"ds_read_b128 v[200:203], v6 offset:512\n"
"s_nop 1\n"
"v_max3_f32 v12, v12, v47, v52\n"
"v_mfma_f32_16x16x32_bf16 v[60:63], v[204:207], a[252:255], v[60:63]\n"
"ds_read_b128 v[204:207], v6 offset:768\n"
"s_waitcnt lgkmcnt(4)\n"
"v_mfma_f32_16x16x32_bf16 a[0:3], v[208:211], v[64:67], a[0:3]\n"
"s_add_u32 m0, s60, 65536\n"
"s_add_u32 s68, s56, 122880\n"
"v_mfma_f32_16x16x32_bf16 a[8:11], v[208:211], v[72:75], a[8:11]\n"
"buffer_load_dwordx4 v1, s[36:39], s68 offen offset:0 nt lds\n"
"v_mfma_f32_16x16x32_bf16 a[16:19], v[208:211], v[80:83], a[16:19]\n"
"v_max3_f32 v13, v13, v53, v54\n"
"v_max3_f32 v14, v14, v55, v60\n"
"v_mfma_f32_16x16x32_bf16 a[24:27], v[208:211], v[88:91], a[24:27]\n"
"v_max3_f32 v12, v12, v61, v62\n"
"v_max3_f32 v12, v12, v13, v14\n"
"v_mfma_f32_16x16x32_bf16 a[32:35], v[208:211], v[96:99], a[32:35]\n"
"v_max_f32 v12, v12, v63\n"
"v_cmp_lt_f32 vcc, s78, v12\n"
"v_mfma_f32_16x16x32_bf16 a[40:43], v[208:211], v[104:107], a[40:43]\n"
"s_cbranch_vccnz LT22_%=\n"
"LR22_%=:\n"
"ds_read_b128 v[208:211], v6 offset:4096\n"
"v_mfma_f32_16x16x32_bf16 a[4:7], v[212:215], v[64:67], a[4:7]\n"
"v_exp_f32 v16, v16\n"
"v_mfma_f32_16x16x32_bf16 a[12:15], v[212:215], v[72:75], a[12:15]\n"
"v_exp_f32 v17, v17\n"
"v_mfma_f32_16x16x32_bf16 a[20:23], v[212:215], v[80:83], a[20:23]\n"
"v_exp_f32 v18, v18\n"
"v_mfma_f32_16x16x32_bf16 a[28:31], v[212:215], v[88:91], a[28:31]\n"
"v_exp_f32 v19, v19\n"
"v_mfma_f32_16x16x32_bf16 a[36:39], v[212:215], v[96:99], a[36:39]\n"
"v_exp_f32 v20, v20\n"
"v_mfma_f32_16x16x32_bf16 a[44:47], v[212:215], v[104:107], a[44:47]\n"
"buffer_load_dwordx4 v2, s[36:39], s68 offen offset:1024 nt lds\n"
"v_mfma_f32_16x16x32_bf16 a[48:51], v[216:219], v[64:67], a[48:51]\n"
"ds_read_b128 v[212:215], v6 offset:4352\n"
"v_cvt_pk_bf16_f32 v16, v16, v17\n"
"v_mfma_f32_16x16x32_bf16 a[56:59], v[216:219], v[72:75], a[56:59]\n"
"v_exp_f32 v21, v21\n"
"v_mfma_f32_16x16x32_bf16 a[64:67], v[216:219], v[80:83], a[64:67]\n"
"v_exp_f32 v22, v22\n"
"v_mfma_f32_16x16x32_bf16 a[72:75], v[216:219], v[88:91], a[72:75]\n"
"v_exp_f32 v23, v23\n"
"v_mfma_f32_16x16x32_bf16 a[80:83], v[216:219], v[96:99], a[80:83]\n"
"v_cvt_pk_bf16_f32 v17, v18, v19\n"
"v_cvt_pk_bf16_f32 v18, v20, v21\n"
"v_mfma_f32_16x16x32_bf16 a[88:91], v[216:219], v[104:107], a[88:91]\n"
"ds_read_b128 v[216:219], v6 offset:4608\n"
"v_cvt_pk_bf16_f32 v19, v22, v23\n"
"v_mfma_f32_16x16x32_bf16 a[52:55], v[220:223], v[64:67], a[52:55]\n"
"v_exp_f32 v24, v24\n"
"v_mfma_f32_16x16x32_bf16 a[60:63], v[220:223], v[72:75], a[60:63]\n"
"v_exp_f32 v25, v25\n"
"v_mfma_f32_16x16x32_bf16 a[68:71], v[220:223], v[80:83], a[68:71]\n"
"v_exp_f32 v26, v26\n"
"v_mfma_f32_16x16x32_bf16 a[76:79], v[220:223], v[88:91], a[76:79]\n"
"v_exp_f32 v27, v27\n"
"v_mfma_f32_16x16x32_bf16 a[84:87], v[220:223], v[96:99], a[84:87]\n"
"buffer_load_dwordx4 v1, s[36:39], s68 offen offset:2048 nt lds\n"
"v_mfma_f32_16x16x32_bf16 a[92:95], v[220:223], v[104:107], a[92:95]\n"
"ds_read_b128 v[220:223], v6 offset:4864\n"
"v_cvt_pk_bf16_f32 v24, v24, v25\n"
"v_mfma_f32_16x16x32_bf16 a[96:99], v[224:227], v[64:67], a[96:99]\n"
"v_exp_f32 v28, v28\n"
"v_mfma_f32_16x16x32_bf16 a[104:107], v[224:227], v[72:75], a[104:107]\n"
"v_exp_f32 v29, v29\n"
"v_mfma_f32_16x16x32_bf16 a[112:115], v[224:227], v[80:83], a[112:115]\n"
"v_exp_f32 v30, v30\n"
"v_mfma_f32_16x16x32_bf16 a[120:123], v[224:227], v[88:91], a[120:123]\n"
"v_exp_f32 v31, v31\n"
"v_mfma_f32_16x16x32_bf16 a[128:131], v[224:227], v[96:99], a[128:131]\n"
"v_cvt_pk_bf16_f32 v25, v26, v27\n"
"v_cvt_pk_bf16_f32 v26, v28, v29\n"
"v_mfma_f32_16x16x32_bf16 a[136:139], v[224:227], v[104:107], a[136:139]\n"
"v_exp_f32 v32, v32\n"
"v_mfma_f32_16x16x32_bf16 a[100:103], v[228:231], v[64:67], a[100:103]\n"
"v_exp_f32 v33, v33\n"
"v_mfma_f32_16x16x32_bf16 a[108:111], v[228:231], v[72:75], a[108:111]\n"
"v_cvt_pk_bf16_f32 v27, v30, v31\n"
"v_cvt_pk_bf16_f32 v32, v32, v33\n"
"v_mfma_f32_16x16x32_bf16 a[116:119], v[228:231], v[80:83], a[116:119]\n"
"v_exp_f32 v34, v34\n"
"v_mfma_f32_16x16x32_bf16 a[124:127], v[228:231], v[88:91], a[124:127]\n"
"buffer_load_dwordx4 v2, s[36:39], s68 offen offset:3072 nt lds\n"
"v_mfma_f32_16x16x32_bf16 a[132:135], v[228:231], v[96:99], a[132:135]\n"
"v_exp_f32 v35, v35\n"
"v_mfma_f32_16x16x32_bf16 a[140:143], v[228:231], v[104:107], a[140:143]\n"
"v_exp_f32 v36, v36\n"
"v_mfma_f32_16x16x32_bf16 a[144:147], v[232:235], v[64:67], a[144:147]\n"
"v_exp_f32 v37, v37\n"
"v_mfma_f32_16x16x32_bf16 a[152:155], v[232:235], v[72:75], a[152:155]\n"
"v_exp_f32 v38, v38\n"
"v_mfma_f32_16x16x32_bf16 a[160:163], v[232:235], v[80:83], a[160:163]\n"
"v_exp_f32 v39, v39\n"
"v_mfma_f32_16x16x32_bf16 a[168:171], v[232:235], v[88:91], a[168:171]\n"
"v_cvt_pk_bf16_f32 v33, v34, v35\n"
"v_cvt_pk_bf16_f32 v34, v36, v37\n"
"v_mfma_f32_16x16x32_bf16 a[176:179], v[232:235], v[96:99], a[176:179]\n"
"v_exp_f32 v40, v40\n"
"v_mfma_f32_16x16x32_bf16 a[184:187], v[232:235], v[104:107], a[184:187]\n"
"v_exp_f32 v41, v41\n"
"v_mfma_f32_16x16x32_bf16 a[148:151], v[236:239], v[64:67], a[148:151]\n"
"v_cvt_pk_bf16_f32 v35, v38, v39\n"
"v_cvt_pk_bf16_f32 v40, v40, v41\n"
"v_mfma_f32_16x16x32_bf16 a[156:159], v[236:239], v[72:75], a[156:159]\n"
"v_exp_f32 v42, v42\n"
"v_mfma_f32_16x16x32_bf16 a[164:167], v[236:239], v[80:83], a[164:167]\n"
"v_exp_f32 v43, v43\n"
"v_mfma_f32_16x16x32_bf16 a[172:175], v[236:239], v[88:91], a[172:175]\n"
"v_exp_f32 v44, v44\n"
"v_mfma_f32_16x16x32_bf16 a[180:183], v[236:239], v[96:99], a[180:183]\n"
"v_exp_f32 v45, v45\n"
"v_mfma_f32_16x16x32_bf16 a[188:191], v[236:239], v[104:107], a[188:191]\n"
"v_exp_f32 v46, v46\n"
"v_mfma_f32_16x16x32_bf16 a[192:195], v[240:243], v[64:67], a[192:195]\n"
"v_exp_f32 v47, v47\n"
"v_mfma_f32_16x16x32_bf16 a[196:199], v[240:243], v[72:75], a[196:199]\n"
"v_cvt_pk_bf16_f32 v41, v42, v43\n"
"v_cvt_pk_bf16_f32 v42, v44, v45\n"
"v_mfma_f32_16x16x32_bf16 a[200:203], v[240:243], v[80:83], a[200:203]\n"
"s_add_u32 m0, s60, 114688\n"
"s_add_u32 s69, s56, 114688\n"
"v_mfma_f32_16x16x32_bf16 a[204:207], v[240:243], v[88:91], a[204:207]\n"
"buffer_load_dwordx4 v1, s[40:43], s69 offen offset:0 nt lds\n"
"v_mfma_f32_16x16x32_bf16 a[208:211], v[240:243], v[96:99], a[208:211]\n"
"v_exp_f32 v48, v48\n"
"v_mfma_f32_16x16x32_bf16 a[212:215], v[240:243], v[104:107], a[212:215]\n"
"v_cvt_pk_bf16_f32 v43, v46, v47\n"
"s_waitcnt lgkmcnt(0)\n"
"v_mfma_f32_16x16x32_bf16 v[64:67], v[192:195], v[136:139], v[112:115]\n"
"s_cmp_lg_u32 s63, 0\n"
"s_cbranch_scc1 LT23_%=\n"
"LR23_%=:\n"
"v_mfma_f32_16x16x32_bf16 v[64:67], v[196:199], v[160:163], v[64:67]\n"
"v_exp_f32 v49, v49\n"
"v_mfma_f32_16x16x32_bf16 v[64:67], v[200:203], v[184:187], v[64:67]\n"
"v_exp_f32 v50, v50\n"
"v_mfma_f32_16x16x32_bf16 v[64:67], v[204:207], v[188:191], v[64:67]\n"
"v_exp_f32 v51, v51\n"
"v_mfma_f32_16x16x32_bf16 v[72:75], v[192:195], v[140:143], v[116:119]\n"
"v_exp_f32 v52, v52\n"
"v_mfma_f32_16x16x32_bf16 v[72:75], v[196:199], v[164:167], v[72:75]\n"
"v_exp_f32 v53, v53\n"
"v_mfma_f32_16x16x32_bf16 v[72:75], v[200:203], a[216:219], v[72:75]\n"
"v_exp_f32 v54, v54\n"
"v_mfma_f32_16x16x32_bf16 v[72:75], v[204:207], a[236:239], v[72:75]\n"
"v_exp_f32 v55, v55\n"
"v_mfma_f32_16x16x32_bf16 v[80:83], v[192:195], v[144:147], v[120:123]\n"
"v_cvt_pk_bf16_f32 v48, v48, v49\n"
"v_cvt_pk_bf16_f32 v49, v50, v51\n"
"v_mfma_f32_16x16x32_bf16 v[80:83], v[196:199], v[168:171], v[80:83]\n"
"v_exp_f32 v56, v56\n"
"v_mfma_f32_16x16x32_bf16 v[80:83], v[200:203], a[220:223], v[80:83]\n"
"v_exp_f32 v57, v57\n"
"v_mfma_f32_16x16x32_bf16 v[80:83], v[204:207], a[240:243], v[80:83]\n"
"v_cvt_pk_bf16_f32 v50, v52, v53\n"
"v_cvt_pk_bf16_f32 v51, v54, v55\n"
"v_mfma_f32_16x16x32_bf16 v[88:91], v[192:195], v[148:151], v[124:127]\n"
"v_exp_f32 v58, v58\n"
"v_mfma_f32_16x16x32_bf16 v[88:91], v[196:199], v[172:175], v[88:91]\n"
"buffer_load_dwordx4 v2, s[40:43], s69 offen offset:1024 nt lds\n"
"v_mfma_f32_16x16x32_bf16 v[88:91], v[200:203], a[224:227], v[88:91]\n"
"s_waitcnt vmcnt(10)\n"
"s_barrier\n"
"v_mfma_f32_16x16x32_bf16 v[88:91], v[204:207], a[244:247], v[88:91]\n"
"ds_read_b64_tr_b16 v[224:225], v7 offset:32768\n"
"ds_read_b64_tr_b16 v[226:227], v7 offset:36864\n"
"v_mfma_f32_16x16x32_bf16 v[96:99], v[192:195], v[152:155], v[128:131]\n"
"ds_read_b64_tr_b16 v[228:229], v8 offset:32768\n"
"ds_read_b64_tr_b16 v[230:231], v8 offset:36864\n"
"v_mfma_f32_16x16x32_bf16 v[96:99], v[196:199], v[176:179], v[96:99]\n"
"ds_read_b64_tr_b16 v[232:233], v7 offset:33024\n"
"ds_read_b64_tr_b16 v[234:235], v7 offset:37120\n"
"v_mfma_f32_16x16x32_bf16 v[96:99], v[200:203], a[228:231], v[96:99]\n"
"v_exp_f32 v59, v59\n"
"v_mfma_f32_16x16x32_bf16 v[96:99], v[204:207], a[248:251], v[96:99]\n"
"v_exp_f32 v60, v60\n"
"v_mfma_f32_16x16x32_bf16 v[104:107], v[192:195], v[156:159], v[132:135]\n"
"v_exp_f32 v61, v61\n"
"v_mfma_f32_16x16x32_bf16 v[104:107], v[196:199], v[180:183], v[104:107]\n"
"v_exp_f32 v62, v62\n"
"v_mfma_f32_16x16x32_bf16 v[104:107], v[200:203], a[232:235], v[104:107]\n"
"v_exp_f32 v63, v63\n"
"v_mfma_f32_16x16x32_bf16 v[104:107], v[204:207], a[252:255], v[104:107]\n"
"v_cvt_pk_bf16_f32 v56, v56, v57\n"
"v_cvt_pk_bf16_f32 v57, v58, v59\n"
"v_mfma_f32_16x16x32_bf16 v[68:71], v[208:211], v[136:139], v[112:115]\n"
"v_cvt_pk_bf16_f32 v58, v60, v61\n"
"v_cvt_pk_bf16_f32 v59, v62, v63\n"
"v_mfma_f32_16x16x32_bf16 v[68:71], v[212:215], v[160:163], v[68:71]\n"
"ds_read_b64_tr_b16 v[236:237], v8 offset:33024\n"
"ds_read_b64_tr_b16 v[238:239], v8 offset:37120\n"
"v_mfma_f32_16x16x32_bf16 v[68:71], v[216:219], v[184:187], v[68:71]\n"
"ds_read_b64_tr_b16 v[192:193], v7 offset:33280\n"
"ds_read_b64_tr_b16 v[194:195], v7 offset:37376\n"
"v_mfma_f32_16x16x32_bf16 v[68:71], v[220:223], v[188:191], v[68:71]\n"
"ds_read_b64_tr_b16 v[196:197], v8 offset:33280\n"
"ds_read_b64_tr_b16 v[198:199], v8 offset:37376\n"
"v_mfma_f32_16x16x32_bf16 v[76:79], v[208:211], v[140:143], v[116:119]\n"
"ds_read_b64_tr_b16 v[200:201], v7 offset:33536\n"
"ds_read_b64_tr_b16 v[202:203], v7 offset:37632\n"
"v_mfma_f32_16x16x32_bf16 v[76:79], v[212:215], v[164:167], v[76:79]\n"
"ds_read_b64_tr_b16 v[204:205], v8 offset:33536\n"
"ds_read_b64_tr_b16 v[206:207], v8 offset:37632\n"
"v_mfma_f32_16x16x32_bf16 v[76:79], v[216:219], a[216:219], v[76:79]\n"
"buffer_load_dwordx4 v1, s[40:43], s69 offen offset:2048 nt lds\n"
"v_mfma_f32_16x16x32_bf16 v[76:79], v[220:223], a[236:239], v[76:79]\n"
"v_max3_f32 v12, v64, v65, v66\n"
"v_max3_f32 v13, v67, v72, v73\n"
"v_mfma_f32_16x16x32_bf16 v[84:87], v[208:211], v[144:147], v[120:123]\n"
"v_max3_f32 v14, v74, v75, v80\n"
"v_max3_f32 v12, v12, v81, v82\n"
"v_mfma_f32_16x16x32_bf16 v[84:87], v[212:215], v[168:171], v[84:87]\n"
"v_max3_f32 v13, v13, v83, v88\n"
"v_max3_f32 v14, v14, v89, v90\n"
"v_mfma_f32_16x16x32_bf16 v[84:87], v[216:219], a[220:223], v[84:87]\n"
"v_max3_f32 v12, v12, v91, v96\n"
"v_max3_f32 v13, v13, v97, v98\n"
"v_mfma_f32_16x16x32_bf16 v[84:87], v[220:223], a[240:243], v[84:87]\n"
"v_max3_f32 v14, v14, v99, v104\n"
"v_max3_f32 v12, v12, v105, v106\n"
"v_mfma_f32_16x16x32_bf16 v[92:95], v[208:211], v[148:151], v[124:127]\n"
"v_max3_f32 v13, v13, v107, v68\n"
"v_max3_f32 v14, v14, v69, v70\n"
"v_mfma_f32_16x16x32_bf16 v[92:95], v[212:215], v[172:175], v[92:95]\n"
"v_max3_f32 v12, v12, v71, v76\n"
"v_max3_f32 v13, v13, v77, v78\n"
"v_mfma_f32_16x16x32_bf16 v[92:95], v[216:219], a[224:227], v[92:95]\n"
"v_max3_f32 v14, v14, v79, v84\n"
"v_max3_f32 v12, v12, v85, v86\n"
"v_mfma_f32_16x16x32_bf16 v[92:95], v[220:223], a[244:247], v[92:95]\n"
"v_add_u32 v7, 0x10000, v7\n"
"v_add_u32 v8, 0x10000, v8\n"
"v_mfma_f32_16x16x32_bf16 v[100:103], v[208:211], v[152:155], v[128:131]\n"
"v_mfma_f32_16x16x32_bf16 v[100:103], v[212:215], v[176:179], v[100:103]\n"
"v_mfma_f32_16x16x32_bf16 v[100:103], v[216:219], a[228:231], v[100:103]\n"
"s_nop 2\n"
"v_max3_f32 v13, v13, v87, v92\n"
"v_max3_f32 v14, v14, v93, v94\n"
"v_mfma_f32_16x16x32_bf16 v[100:103], v[220:223], a[248:251], v[100:103]\n"
"v_mfma_f32_16x16x32_bf16 v[108:111], v[208:211], v[156:159], v[132:135]\n"
"ds_read_b128 v[208:211], v6 offset:32768\n"
"v_mfma_f32_16x16x32_bf16 v[108:111], v[212:215], v[180:183], v[108:111]\n"
"ds_read_b128 v[212:215], v6 offset:33024\n"
"v_mfma_f32_16x16x32_bf16 v[108:111], v[216:219], a[232:235], v[108:111]\n"
"ds_read_b128 v[216:219], v6 offset:33280\n"
"s_nop 1\n"
"v_max3_f32 v12, v12, v95, v100\n"
"v_mfma_f32_16x16x32_bf16 v[108:111], v[220:223], a[252:255], v[108:111]\n"
"ds_read_b128 v[220:223], v6 offset:33536\n"
"s_waitcnt lgkmcnt(4)\n"
"v_mfma_f32_16x16x32_bf16 a[0:3], v[224:227], v[16:19], a[0:3]\n"
"buffer_load_dwordx4 v2, s[40:43], s69 offen offset:3072 nt lds\n"
"v_mfma_f32_16x16x32_bf16 a[8:11], v[224:227], v[24:27], a[8:11]\n"
"v_max3_f32 v13, v13, v101, v102\n"
"s_lshl_b32 s69, s91, 8\n"
"v_mfma_f32_16x16x32_bf16 a[16:19], v[224:227], v[32:35], a[16:19]\n"
"v_max3_f32 v14, v14, v103, v108\n"
"v_max3_f32 v12, v12, v109, v110\n"
"v_mfma_f32_16x16x32_bf16 a[24:27], v[224:227], v[40:43], a[24:27]\n"
"v_max3_f32 v12, v12, v13, v14\n"
"v_max_f32 v12, v12, v111\n"
"v_mfma_f32_16x16x32_bf16 a[32:35], v[224:227], v[48:51], a[32:35]\n"
"v_cmp_lt_f32 vcc, s78, v12\n"
"s_cbranch_vccnz LT24_%=\n"
"LR24_%=:\n"
"v_mfma_f32_16x16x32_bf16 a[40:43], v[224:227], v[56:59], a[40:43]\n"
"ds_read_b128 v[224:227], v6 offset:36864\n"
"s_add_u32 s58, s58, s69\n"
"v_mfma_f32_16x16x32_bf16 a[4:7], v[228:231], v[16:19], a[4:7]\n"
"v_exp_f32 v64, v64\n"
"v_mfma_f32_16x16x32_bf16 a[12:15], v[228:231], v[24:27], a[12:15]\n"
"v_exp_f32 v65, v65\n"
"v_mfma_f32_16x16x32_bf16 a[20:23], v[228:231], v[32:35], a[20:23]\n"
"v_exp_f32 v66, v66\n"
"v_mfma_f32_16x16x32_bf16 a[28:31], v[228:231], v[40:43], a[28:31]\n"
"v_exp_f32 v67, v67\n"
"v_mfma_f32_16x16x32_bf16 a[36:39], v[228:231], v[48:51], a[36:39]\n"
"v_exp_f32 v68, v68\n"
"v_mfma_f32_16x16x32_bf16 a[44:47], v[228:231], v[56:59], a[44:47]\n"
"ds_read_b128 v[228:231], v6 offset:37120\n"
"v_cvt_pk_bf16_f32 v64, v64, v65\n"
"v_mfma_f32_16x16x32_bf16 a[48:51], v[232:235], v[16:19], a[48:51]\n"
"v_exp_f32 v69, v69\n"
"v_mfma_f32_16x16x32_bf16 a[56:59], v[232:235], v[24:27], a[56:59]\n"
"v_exp_f32 v70, v70\n"
"v_mfma_f32_16x16x32_bf16 a[64:67], v[232:235], v[32:35], a[64:67]\n"
"v_exp_f32 v71, v71\n"
"v_mfma_f32_16x16x32_bf16 a[72:75], v[232:235], v[40:43], a[72:75]\n"
"v_cvt_pk_bf16_f32 v65, v66, v67\n"
"v_cvt_pk_bf16_f32 v66, v68, v69\n"
"v_mfma_f32_16x16x32_bf16 a[80:83], v[232:235], v[48:51], a[80:83]\n"
"s_add_u32 m0, s60, 147456\n"
"s_add_u32 s70, s56, 122880\n"
"v_mfma_f32_16x16x32_bf16 a[88:91], v[232:235], v[56:59], a[88:91]\n"
"buffer_load_dwordx4 v1, s[40:43], s70 offen offset:0 nt lds\n"
"v_mfma_f32_16x16x32_bf16 a[52:55], v[236:239], v[16:19], a[52:55]\n"
"ds_read_b128 v[232:235], v6 offset:37376\n"
"v_cvt_pk_bf16_f32 v67, v70, v71\n"
"v_mfma_f32_16x16x32_bf16 a[60:63], v[236:239], v[24:27], a[60:63]\n"
"v_exp_f32 v72, v72\n"
"v_mfma_f32_16x16x32_bf16 a[68:71], v[236:239], v[32:35], a[68:71]\n"
"buffer_load_dwordx4 v2, s[40:43], s70 offen offset:1024 nt lds\n"
"v_mfma_f32_16x16x32_bf16 a[76:79], v[236:239], v[40:43], a[76:79]\n"
"buffer_load_dwordx4 v1, s[40:43], s70 offen offset:2048 nt lds\n"
"v_mfma_f32_16x16x32_bf16 a[84:87], v[236:239], v[48:51], a[84:87]\n"
"buffer_load_dwordx4 v2, s[40:43], s70 offen offset:3072 nt lds\n"
"v_mfma_f32_16x16x32_bf16 a[92:95], v[236:239], v[56:59], a[92:95]\n"
"s_add_u32 s74, s57, s82\n"
"s_add_u32 m0, s61, 98304\n"
"v_mfma_f32_16x16x32_bf16 a[96:99], v[192:195], v[16:19], a[96:99]\n"
"s_add_u32 s71, s74, 0\n"
"ds_read_b128 v[236:239], v6 offset:37632\n"
"v_mfma_f32_16x16x32_bf16 a[104:107], v[192:195], v[24:27], a[104:107]\n"
"buffer_load_dwordx4 v4, s[44:47], s71 offen offset:0 lds\n"
"v_mfma_f32_16x16x32_bf16 a[112:115], v[192:195], v[32:35], a[112:115]\n"
"v_exp_f32 v73, v73\n"
"v_mfma_f32_16x16x32_bf16 a[120:123], v[192:195], v[40:43], a[120:123]\n"
"v_exp_f32 v74, v74\n"
"v_mfma_f32_16x16x32_bf16 a[128:131], v[192:195], v[48:51], a[128:131]\n"
"v_exp_f32 v75, v75\n"
"v_mfma_f32_16x16x32_bf16 a[136:139], v[192:195], v[56:59], a[136:139]\n"
"v_exp_f32 v76, v76\n"
"v_mfma_f32_16x16x32_bf16 a[100:103], v[196:199], v[16:19], a[100:103]\n"
"v_exp_f32 v77, v77\n"
"v_mfma_f32_16x16x32_bf16 a[108:111], v[196:199], v[24:27], a[108:111]\n"
"v_exp_f32 v78, v78\n"
"v_mfma_f32_16x16x32_bf16 a[116:119], v[196:199], v[32:35], a[116:119]\n"
"s_add_u32 s64, s74, 8192\n"
"v_cvt_pk_bf16_f32 v72, v72, v73\n"
"v_mfma_f32_16x16x32_bf16 a[124:127], v[196:199], v[40:43], a[124:127]\n"
"buffer_load_dwordx4 v5, s[44:47], s64 offen offset:1024 lds\n"
"v_mfma_f32_16x16x32_bf16 a[132:135], v[196:199], v[48:51], a[132:135]\n"
"v_exp_f32 v79, v79\n"
"v_mfma_f32_16x16x32_bf16 a[140:143], v[196:199], v[56:59], a[140:143]\n"
"v_cvt_pk_bf16_f32 v73, v74, v75\n"
"v_cvt_pk_bf16_f32 v74, v76, v77\n"
"v_mfma_f32_16x16x32_bf16 a[144:147], v[200:203], v[16:19], a[144:147]\n"
"v_exp_f32 v80, v80\n"
"v_mfma_f32_16x16x32_bf16 a[152:155], v[200:203], v[24:27], a[152:155]\n"
"v_exp_f32 v81, v81\n"
"v_mfma_f32_16x16x32_bf16 a[160:163], v[200:203], v[32:35], a[160:163]\n"
"v_cvt_pk_bf16_f32 v75, v78, v79\n"
"v_cvt_pk_bf16_f32 v80, v80, v81\n"
"v_mfma_f32_16x16x32_bf16 a[168:171], v[200:203], v[40:43], a[168:171]\n"
"v_exp_f32 v82, v82\n"
"v_mfma_f32_16x16x32_bf16 a[176:179], v[200:203], v[48:51], a[176:179]\n"
"v_exp_f32 v83, v83\n"
"v_mfma_f32_16x16x32_bf16 a[184:187], v[200:203], v[56:59], a[184:187]\n"
"v_exp_f32 v84, v84\n"
"v_mfma_f32_16x16x32_bf16 a[148:151], v[204:207], v[16:19], a[148:151]\n"
"v_exp_f32 v85, v85\n"
"v_mfma_f32_16x16x32_bf16 a[156:159], v[204:207], v[24:27], a[156:159]\n"
"v_exp_f32 v86, v86\n"
"v_mfma_f32_16x16x32_bf16 a[164:167], v[204:207], v[32:35], a[164:167]\n"
"s_add_u32 s65, s74, 16384\n"
"v_cvt_pk_bf16_f32 v81, v82, v83\n"
"v_mfma_f32_16x16x32_bf16 a[172:175], v[204:207], v[40:43], a[172:175]\n"
"buffer_load_dwordx4 v4, s[44:47], s65 offen offset:2048 lds\n"
"v_mfma_f32_16x16x32_bf16 a[180:183], v[204:207], v[48:51], a[180:183]\n"
"v_exp_f32 v87, v87\n"
"v_mfma_f32_16x16x32_bf16 a[188:191], v[204:207], v[56:59], a[188:191]\n"
"v_exp_f32 v88, v88\n"
"v_mfma_f32_16x16x32_bf16 a[192:195], v[240:243], v[16:19], a[192:195]\n"
"v_exp_f32 v89, v89\n"
"v_mfma_f32_16x16x32_bf16 a[196:199], v[240:243], v[24:27], a[196:199]\n"
"v_cvt_pk_bf16_f32 v82, v84, v85\n"
"v_cvt_pk_bf16_f32 v83, v86, v87\n"
"v_mfma_f32_16x16x32_bf16 a[200:203], v[240:243], v[32:35], a[200:203]\n"
"v_exp_f32 v90, v90\n"
"v_mfma_f32_16x16x32_bf16 a[204:207], v[240:243], v[40:43], a[204:207]\n"
"v_exp_f32 v91, v91\n"
"v_mfma_f32_16x16x32_bf16 a[208:211], v[240:243], v[48:51], a[208:211]\n"
"v_exp_f32 v92, v92\n"
"v_mfma_f32_16x16x32_bf16 a[212:215], v[240:243], v[56:59], a[212:215]\n"
"v_cvt_pk_bf16_f32 v88, v88, v89\n"
"s_waitcnt lgkmcnt(0)\n"
"v_mfma_f32_16x16x32_bf16 v[16:19], v[208:211], v[136:139], v[112:115]\n"
"s_cmp_lg_u32 s63, 0\n"
"s_cbranch_scc1 LT25_%=\n"
"LR25_%=:\n"
"v_mfma_f32_16x16x32_bf16 v[16:19], v[212:215], v[160:163], v[16:19]\n"
"v_exp_f32 v93, v93\n"
"v_mfma_f32_16x16x32_bf16 v[16:19], v[216:219], v[184:187], v[16:19]\n"
"v_exp_f32 v94, v94\n"
"v_mfma_f32_16x16x32_bf16 v[16:19], v[220:223], v[188:191], v[16:19]\n"
"v_exp_f32 v95, v95\n"
"v_mfma_f32_16x16x32_bf16 v[24:27], v[208:211], v[140:143], v[116:119]\n"
"v_cvt_pk_bf16_f32 v89, v90, v91\n"
"v_cvt_pk_bf16_f32 v90, v92, v93\n"
"v_mfma_f32_16x16x32_bf16 v[24:27], v[212:215], v[164:167], v[24:27]\n"
"v_exp_f32 v96, v96\n"
"v_mfma_f32_16x16x32_bf16 v[24:27], v[216:219], a[216:219], v[24:27]\n"
"v_exp_f32 v97, v97\n"
"v_mfma_f32_16x16x32_bf16 v[24:27], v[220:223], a[236:239], v[24:27]\n"
"v_cvt_pk_bf16_f32 v91, v94, v95\n"
"v_cvt_pk_bf16_f32 v96, v96, v97\n"
"v_mfma_f32_16x16x32_bf16 v[32:35], v[208:211], v[144:147], v[120:123]\n"
"v_exp_f32 v98, v98\n"
"v_mfma_f32_16x16x32_bf16 v[32:35], v[212:215], v[168:171], v[32:35]\n"
"v_exp_f32 v99, v99\n"
"v_mfma_f32_16x16x32_bf16 v[32:35], v[216:219], a[220:223], v[32:35]\n"
"v_exp_f32 v100, v100\n"
"v_mfma_f32_16x16x32_bf16 v[32:35], v[220:223], a[240:243], v[32:35]\n"
"s_waitcnt vmcnt(11)\n"
"s_barrier\n"
"v_mfma_f32_16x16x32_bf16 v[40:43], v[208:211], v[148:151], v[124:127]\n"
"ds_read_b64_tr_b16 v[192:193], v7 offset:0\n"
"ds_read_b64_tr_b16 v[194:195], v7 offset:4096\n"
"v_mfma_f32_16x16x32_bf16 v[40:43], v[212:215], v[172:175], v[40:43]\n"
"ds_read_b64_tr_b16 v[196:197], v8 offset:0\n"
"ds_read_b64_tr_b16 v[198:199], v8 offset:4096\n"
"v_mfma_f32_16x16x32_bf16 v[40:43], v[216:219], a[224:227], v[40:43]\n"
"v_exp_f32 v101, v101\n"
"v_mfma_f32_16x16x32_bf16 v[40:43], v[220:223], a[244:247], v[40:43]\n"
"v_exp_f32 v102, v102\n"
"v_mfma_f32_16x16x32_bf16 v[48:51], v[208:211], v[152:155], v[128:131]\n"
"v_exp_f32 v103, v103\n"
"v_mfma_f32_16x16x32_bf16 v[48:51], v[212:215], v[176:179], v[48:51]\n"
"ds_read_b64_tr_b16 v[200:201], v7 offset:256\n"
"ds_read_b64_tr_b16 v[202:203], v7 offset:4352\n"
"v_mfma_f32_16x16x32_bf16 v[48:51], v[216:219], a[228:231], v[48:51]\n"
"v_cvt_pk_bf16_f32 v97, v98, v99\n"
"v_cvt_pk_bf16_f32 v98, v100, v101\n"
"v_mfma_f32_16x16x32_bf16 v[48:51], v[220:223], a[248:251], v[48:51]\n"
"v_exp_f32 v104, v104\n"
"v_mfma_f32_16x16x32_bf16 v[56:59], v[208:211], v[156:159], v[132:135]\n"
"v_exp_f32 v105, v105\n"
"v_mfma_f32_16x16x32_bf16 v[56:59], v[212:215], v[180:183], v[56:59]\n"
"v_cvt_pk_bf16_f32 v99, v102, v103\n"
"v_cvt_pk_bf16_f32 v104, v104, v105\n"
"v_mfma_f32_16x16x32_bf16 v[56:59], v[216:219], a[232:235], v[56:59]\n"
"v_exp_f32 v106, v106\n"
"v_mfma_f32_16x16x32_bf16 v[56:59], v[220:223], a[252:255], v[56:59]\n"
"v_exp_f32 v107, v107\n"
"v_mfma_f32_16x16x32_bf16 v[20:23], v[224:227], v[136:139], v[112:115]\n"
"v_exp_f32 v108, v108\n"
"v_mfma_f32_16x16x32_bf16 v[20:23], v[228:231], v[160:163], v[20:23]\n"
"v_exp_f32 v109, v109\n"
"v_mfma_f32_16x16x32_bf16 v[20:23], v[232:235], v[184:187], v[20:23]\n"
"v_exp_f32 v110, v110\n"
"v_mfma_f32_16x16x32_bf16 v[20:23], v[236:239], v[188:191], v[20:23]\n"
"v_exp_f32 v111, v111\n"
"v_mfma_f32_16x16x32_bf16 v[28:31], v[224:227], v[140:143], v[116:119]\n"
"v_cvt_pk_bf16_f32 v105, v106, v107\n"
"v_cvt_pk_bf16_f32 v106, v108, v109\n"
"v_mfma_f32_16x16x32_bf16 v[28:31], v[228:231], v[164:167], v[28:31]\n"
"v_cvt_pk_bf16_f32 v107, v110, v111\n"
"ds_read_b64_tr_b16 v[204:205], v8 offset:256\n"
"v_mfma_f32_16x16x32_bf16 v[28:31], v[232:235], a[216:219], v[28:31]\n"
"ds_read_b64_tr_b16 v[206:207], v8 offset:4352\n"
"ds_read_b64_tr_b16 v[208:209], v7 offset:512\n"
"v_mfma_f32_16x16x32_bf16 v[28:31], v[236:239], a[236:239], v[28:31]\n"
"ds_read_b64_tr_b16 v[210:211], v7 offset:4608\n"
"ds_read_b64_tr_b16 v[212:213], v8 offset:512\n"
"v_mfma_f32_16x16x32_bf16 v[36:39], v[224:227], v[144:147], v[120:123]\n"
"ds_read_b64_tr_b16 v[214:215], v8 offset:4608\n"
"ds_read_b64_tr_b16 v[216:217], v7 offset:768\n"
"v_mfma_f32_16x16x32_bf16 v[36:39], v[228:231], v[168:171], v[36:39]\n"
"ds_read_b64_tr_b16 v[218:219], v7 offset:4864\n"
"ds_read_b64_tr_b16 v[220:221], v8 offset:768\n"
"v_mfma_f32_16x16x32_bf16 v[36:39], v[232:235], a[220:223], v[36:39]\n"
"ds_read_b64_tr_b16 v[222:223], v8 offset:4864\n"
"v_add_u32 v6, 0x10000, v6\n"
"v_mfma_f32_16x16x32_bf16 v[36:39], v[236:239], a[240:243], v[36:39]\n"
"v_max3_f32 v12, v16, v17, v18\n"
"v_max3_f32 v13, v19, v24, v25\n"
"v_mfma_f32_16x16x32_bf16 v[44:47], v[224:227], v[148:151], v[124:127]\n"
"v_max3_f32 v14, v26, v27, v32\n"
"v_max3_f32 v12, v12, v33, v34\n"
"v_mfma_f32_16x16x32_bf16 v[44:47], v[228:231], v[172:175], v[44:47]\n"
"v_max3_f32 v13, v13, v35, v40\n"
"v_max3_f32 v14, v14, v41, v42\n"
"v_mfma_f32_16x16x32_bf16 v[44:47], v[232:235], a[224:227], v[44:47]\n"
"v_max3_f32 v12, v12, v43, v48\n"
"v_max3_f32 v13, v13, v49, v50\n"
"v_mfma_f32_16x16x32_bf16 v[44:47], v[236:239], a[244:247], v[44:47]\n"
"v_max3_f32 v14, v14, v51, v56\n"
"v_max3_f32 v12, v12, v57, v58\n"
"v_mfma_f32_16x16x32_bf16 v[52:55], v[224:227], v[152:155], v[128:131]\n"
"v_max3_f32 v13, v13, v59, v20\n"
"v_max3_f32 v14, v14, v21, v22\n"
"v_mfma_f32_16x16x32_bf16 v[52:55], v[228:231], v[176:179], v[52:55]\n"
"v_max3_f32 v12, v12, v23, v28\n"
"v_max3_f32 v13, v13, v29, v30\n"
"v_mfma_f32_16x16x32_bf16 v[52:55], v[232:235], a[228:231], v[52:55]\n"
"v_max3_f32 v14, v14, v31, v36\n"
"v_max3_f32 v12, v12, v37, v38\n"
"v_mfma_f32_16x16x32_bf16 v[52:55], v[236:239], a[248:251], v[52:55]\n"
"v_max3_f32 v13, v13, v39, v44\n"
"v_max3_f32 v14, v14, v45, v46\n"
"v_mfma_f32_16x16x32_bf16 v[60:63], v[224:227], v[156:159], v[132:135]\n"
"ds_read_b128 v[224:227], v6 offset:0\n"
"v_mfma_f32_16x16x32_bf16 v[60:63], v[228:231], v[180:183], v[60:63]\n"
"ds_read_b128 v[228:231], v6 offset:256\n"
"v_mfma_f32_16x16x32_bf16 v[60:63], v[232:235], a[232:235], v[60:63]\n"
"ds_read_b128 v[232:235], v6 offset:512\n"
"v_max3_f32 v12, v12, v47, v52\n"
"v_mfma_f32_16x16x32_bf16 v[60:63], v[236:239], a[252:255], v[60:63]\n"
"ds_read_b128 v[236:239], v6 offset:768\n"
"s_waitcnt lgkmcnt(4)\n"
"v_mfma_f32_16x16x32_bf16 a[0:3], v[192:195], v[64:67], a[0:3]\n"
"v_max3_f32 v13, v13, v53, v54\n"
"v_mfma_f32_16x16x32_bf16 a[8:11], v[192:195], v[72:75], a[8:11]\n"
"v_mfma_f32_16x16x32_bf16 a[16:19], v[192:195], v[80:83], a[16:19]\n"
"s_nop 1\n"
"v_max3_f32 v14, v14, v55, v60\n"
"v_max3_f32 v12, v12, v61, v62\n"
"v_mfma_f32_16x16x32_bf16 a[24:27], v[192:195], v[88:91], a[24:27]\n"
"v_max3_f32 v12, v12, v13, v14\n"
"v_max_f32 v12, v12, v63\n"
"v_mfma_f32_16x16x32_bf16 a[32:35], v[192:195], v[96:99], a[32:35]\n"
"v_cmp_lt_f32 vcc, s78, v12\n"
"s_cbranch_vccnz LT26_%=\n"
"LR26_%=:\n"
"v_mfma_f32_16x16x32_bf16 a[40:43], v[192:195], v[104:107], a[40:43]\n"
"ds_read_b128 v[192:195], v6 offset:4096\n"
"v_mfma_f32_16x16x32_bf16 a[4:7], v[196:199], v[64:67], a[4:7]\n"
"v_exp_f32 v16, v16\n"
"v_mfma_f32_16x16x32_bf16 a[12:15], v[196:199], v[72:75], a[12:15]\n"
"v_exp_f32 v17, v17\n"
"v_mfma_f32_16x16x32_bf16 a[20:23], v[196:199], v[80:83], a[20:23]\n"
"v_exp_f32 v18, v18\n"
"v_mfma_f32_16x16x32_bf16 a[28:31], v[196:199], v[88:91], a[28:31]\n"
"v_exp_f32 v19, v19\n"
"v_mfma_f32_16x16x32_bf16 a[36:39], v[196:199], v[96:99], a[36:39]\n"
"v_exp_f32 v20, v20\n"
"v_mfma_f32_16x16x32_bf16 a[44:47], v[196:199], v[104:107], a[44:47]\n"
"ds_read_b128 v[196:199], v6 offset:4352\n"
"v_cvt_pk_bf16_f32 v16, v16, v17\n"
"v_mfma_f32_16x16x32_bf16 a[48:51], v[200:203], v[64:67], a[48:51]\n"
"v_exp_f32 v21, v21\n"
"v_mfma_f32_16x16x32_bf16 a[56:59], v[200:203], v[72:75], a[56:59]\n"
"v_exp_f32 v22, v22\n"
"v_mfma_f32_16x16x32_bf16 a[64:67], v[200:203], v[80:83], a[64:67]\n"
"v_exp_f32 v23, v23\n"
"v_mfma_f32_16x16x32_bf16 a[72:75], v[200:203], v[88:91], a[72:75]\n"
"v_cvt_pk_bf16_f32 v17, v18, v19\n"
"v_cvt_pk_bf16_f32 v18, v20, v21\n"
"v_mfma_f32_16x16x32_bf16 a[80:83], v[200:203], v[96:99], a[80:83]\n"
"v_exp_f32 v24, v24\n"
"v_mfma_f32_16x16x32_bf16 a[88:91], v[200:203], v[104:107], a[88:91]\n"
"ds_read_b128 v[200:203], v6 offset:4608\n"
"v_cvt_pk_bf16_f32 v19, v22, v23\n"
"v_mfma_f32_16x16x32_bf16 a[52:55], v[204:207], v[64:67], a[52:55]\n"
"v_exp_f32 v25, v25\n"
"v_mfma_f32_16x16x32_bf16 a[60:63], v[204:207], v[72:75], a[60:63]\n"
"v_exp_f32 v26, v26\n"
"v_mfma_f32_16x16x32_bf16 a[68:71], v[204:207], v[80:83], a[68:71]\n"
"s_add_u32 s74, s57, s82\n"
"s_add_u32 m0, s61, 131072\n"
"v_mfma_f32_16x16x32_bf16 a[76:79], v[204:207], v[88:91], a[76:79]\n"
"s_add_u32 s66, s74, 55296\n"
"v_cvt_pk_bf16_f32 v24, v24, v25\n"
"v_mfma_f32_16x16x32_bf16 a[84:87], v[204:207], v[96:99], a[84:87]\n"
"buffer_load_dwordx4 v4, s[44:47], s66 offen offset:0 lds\n"
"v_mfma_f32_16x16x32_bf16 a[92:95], v[204:207], v[104:107], a[92:95]\n"
"ds_read_b128 v[204:207], v6 offset:4864\n"
"v_add_u32 v6, 0xffff0000, v6\n"
"v_mfma_f32_16x16x32_bf16 a[96:99], v[208:211], v[64:67], a[96:99]\n"
"v_exp_f32 v27, v27\n"
"v_mfma_f32_16x16x32_bf16 a[104:107], v[208:211], v[72:75], a[104:107]\n"
"v_exp_f32 v28, v28\n"
"v_mfma_f32_16x16x32_bf16 a[112:115], v[208:211], v[80:83], a[112:115]\n"
"v_exp_f32 v29, v29\n"
"v_mfma_f32_16x16x32_bf16 a[120:123], v[208:211], v[88:91], a[120:123]\n"
"v_exp_f32 v30, v30\n"
"v_mfma_f32_16x16x32_bf16 a[128:131], v[208:211], v[96:99], a[128:131]\n"
"v_exp_f32 v31, v31\n"
"v_mfma_f32_16x16x32_bf16 a[136:139], v[208:211], v[104:107], a[136:139]\n"
"v_cvt_pk_bf16_f32 v25, v26, v27\n"
"v_cvt_pk_bf16_f32 v26, v28, v29\n"
"v_mfma_f32_16x16x32_bf16 a[100:103], v[212:215], v[64:67], a[100:103]\n"
"v_exp_f32 v32, v32\n"
"v_mfma_f32_16x16x32_bf16 a[108:111], v[212:215], v[72:75], a[108:111]\n"
"v_exp_f32 v33, v33\n"
"v_mfma_f32_16x16x32_bf16 a[116:119], v[212:215], v[80:83], a[116:119]\n"
"s_add_u32 s67, s74, 63488\n"
"v_cvt_pk_bf16_f32 v27, v30, v31\n"
"v_mfma_f32_16x16x32_bf16 a[124:127], v[212:215], v[88:91], a[124:127]\n"
"buffer_load_dwordx4 v5, s[44:47], s67 offen offset:1024 lds\n"
"v_mfma_f32_16x16x32_bf16 a[132:135], v[212:215], v[96:99], a[132:135]\n"
"v_exp_f32 v34, v34\n"
"v_mfma_f32_16x16x32_bf16 a[140:143], v[212:215], v[104:107], a[140:143]\n"
"v_exp_f32 v35, v35\n"
"v_mfma_f32_16x16x32_bf16 a[144:147], v[216:219], v[64:67], a[144:147]\n"
"v_exp_f32 v36, v36\n"
"v_mfma_f32_16x16x32_bf16 a[152:155], v[216:219], v[72:75], a[152:155]\n"
"v_exp_f32 v37, v37\n"
"v_mfma_f32_16x16x32_bf16 a[160:163], v[216:219], v[80:83], a[160:163]\n"
"v_exp_f32 v38, v38\n"
"v_mfma_f32_16x16x32_bf16 a[168:171], v[216:219], v[88:91], a[168:171]\n"
"v_exp_f32 v39, v39\n"
"v_mfma_f32_16x16x32_bf16 a[176:179], v[216:219], v[96:99], a[176:179]\n"
"v_cvt_pk_bf16_f32 v32, v32, v33\n"
"v_cvt_pk_bf16_f32 v33, v34, v35\n"
"v_mfma_f32_16x16x32_bf16 a[184:187], v[216:219], v[104:107], a[184:187]\n"
"v_exp_f32 v40, v40\n"
"v_mfma_f32_16x16x32_bf16 a[148:151], v[220:223], v[64:67], a[148:151]\n"
"v_exp_f32 v41, v41\n"
"v_mfma_f32_16x16x32_bf16 a[156:159], v[220:223], v[72:75], a[156:159]\n"
"v_cvt_pk_bf16_f32 v34, v36, v37\n"
"v_cvt_pk_bf16_f32 v35, v38, v39\n"
"v_mfma_f32_16x16x32_bf16 a[164:167], v[220:223], v[80:83], a[164:167]\n"
"s_add_u32 s68, s74, 71680\n"
"v_cvt_pk_bf16_f32 v40, v40, v41\n"
"v_mfma_f32_16x16x32_bf16 a[172:175], v[220:223], v[88:91], a[172:175]\n"
"buffer_load_dwordx4 v4, s[44:47], s68 offen offset:2048 lds\n"
"v_mfma_f32_16x16x32_bf16 a[180:183], v[220:223], v[96:99], a[180:183]\n"
"v_exp_f32 v42, v42\n"
"v_mfma_f32_16x16x32_bf16 a[188:191], v[220:223], v[104:107], a[188:191]\n"
"v_exp_f32 v43, v43\n"
"v_mfma_f32_16x16x32_bf16 a[192:195], v[240:243], v[64:67], a[192:195]\n"
"v_exp_f32 v44, v44\n"
"v_mfma_f32_16x16x32_bf16 a[196:199], v[240:243], v[72:75], a[196:199]\n"
"v_exp_f32 v45, v45\n"
"v_mfma_f32_16x16x32_bf16 a[200:203], v[240:243], v[80:83], a[200:203]\n"
"v_exp_f32 v46, v46\n"
"v_mfma_f32_16x16x32_bf16 a[204:207], v[240:243], v[88:91], a[204:207]\n"
"v_exp_f32 v47, v47\n"
"v_mfma_f32_16x16x32_bf16 a[208:211], v[240:243], v[96:99], a[208:211]\n"
"s_add_u32 s82, s82, 128\n"
"s_cmp_eq_u32 s82, 1152\n"
"s_cselect_b32 s82, 0, s82\n"
"v_mfma_f32_16x16x32_bf16 a[212:215], v[240:243], v[104:107], a[212:215]\n"
"v_cvt_pk_bf16_f32 v41, v42, v43\n"
"s_waitcnt lgkmcnt(0)\n"
"v_mfma_f32_16x16x32_bf16 v[64:67], v[224:227], v[136:139], v[112:115]\n"
"s_cmp_lg_u32 s63, 0\n"
"s_cbranch_scc1 LT27_%=\n"
"LR27_%=:\n"
"v_mfma_f32_16x16x32_bf16 v[64:67], v[228:231], v[160:163], v[64:67]\n"
"v_exp_f32 v48, v48\n"
"v_mfma_f32_16x16x32_bf16 v[64:67], v[232:235], v[184:187], v[64:67]\n"
"v_exp_f32 v49, v49\n"
"v_mfma_f32_16x16x32_bf16 v[64:67], v[236:239], v[188:191], v[64:67]\n"
"v_cvt_pk_bf16_f32 v42, v44, v45\n"
"v_cvt_pk_bf16_f32 v43, v46, v47\n"
"v_mfma_f32_16x16x32_bf16 v[72:75], v[224:227], v[140:143], v[116:119]\n"
"v_exp_f32 v50, v50\n"
"v_mfma_f32_16x16x32_bf16 v[72:75], v[228:231], v[164:167], v[72:75]\n"
"v_exp_f32 v51, v51\n"
"v_mfma_f32_16x16x32_bf16 v[72:75], v[232:235], a[216:219], v[72:75]\n"
"v_exp_f32 v52, v52\n"
"v_mfma_f32_16x16x32_bf16 v[72:75], v[236:239], a[236:239], v[72:75]\n"
"v_exp_f32 v53, v53\n"
"v_mfma_f32_16x16x32_bf16 v[80:83], v[224:227], v[144:147], v[120:123]\n"
"v_exp_f32 v54, v54\n"
"v_mfma_f32_16x16x32_bf16 v[80:83], v[228:231], v[168:171], v[80:83]\n"
"v_exp_f32 v55, v55\n"
"v_mfma_f32_16x16x32_bf16 v[80:83], v[232:235], a[220:223], v[80:83]\n"
"v_cvt_pk_bf16_f32 v48, v48, v49\n"
"v_cvt_pk_bf16_f32 v49, v50, v51\n"
"v_mfma_f32_16x16x32_bf16 v[80:83], v[236:239], a[240:243], v[80:83]\n"
"s_waitcnt vmcnt(3)\n"
"s_barrier\n"
"v_mfma_f32_16x16x32_bf16 v[88:91], v[224:227], v[148:151], v[124:127]\n"
"v_exp_f32 v56, v56\n"
"v_mfma_f32_16x16x32_bf16 v[88:91], v[228:231], v[172:175], v[88:91]\n"
"v_exp_f32 v57, v57\n"
"v_mfma_f32_16x16x32_bf16 v[88:91], v[232:235], a[224:227], v[88:91]\n"
"v_cvt_pk_bf16_f32 v50, v52, v53\n"
"v_cvt_pk_bf16_f32 v51, v54, v55\n"
"v_mfma_f32_16x16x32_bf16 v[88:91], v[236:239], a[244:247], v[88:91]\n"
"v_exp_f32 v58, v58\n"
"v_mfma_f32_16x16x32_bf16 v[96:99], v[224:227], v[152:155], v[128:131]\n"
"v_exp_f32 v59, v59\n"
"v_mfma_f32_16x16x32_bf16 v[96:99], v[228:231], v[176:179], v[96:99]\n"
"v_exp_f32 v60, v60\n"
"v_mfma_f32_16x16x32_bf16 v[96:99], v[232:235], a[228:231], v[96:99]\n"
"v_exp_f32 v61, v61\n"
"v_mfma_f32_16x16x32_bf16 v[96:99], v[236:239], a[248:251], v[96:99]\n"
"s_add_u32 s75, s58, s83\n"
"s_add_u32 m0, s60, 16384\n"
"v_mfma_f32_16x16x32_bf16 v[104:107], v[224:227], v[156:159], v[132:135]\n"
"s_add_u32 s70, s75, 0\n"
"v_cvt_pk_bf16_f32 v56, v56, v57\n"
"v_mfma_f32_16x16x32_bf16 v[104:107], v[228:231], v[180:183], v[104:107]\n"
"buffer_load_dwordx4 v3, s[48:51], s70 offen offset:0 lds\n"
"v_mfma_f32_16x16x32_bf16 v[104:107], v[232:235], a[232:235], v[104:107]\n"
"v_exp_f32 v62, v62\n"
"v_mfma_f32_16x16x32_bf16 v[104:107], v[236:239], a[252:255], v[104:107]\n"
"v_exp_f32 v63, v63\n"
"v_mfma_f32_16x16x32_bf16 v[68:71], v[192:195], v[136:139], v[112:115]\n"
"v_cvt_pk_bf16_f32 v57, v58, v59\n"
"v_cvt_pk_bf16_f32 v58, v60, v61\n"
"v_mfma_f32_16x16x32_bf16 v[68:71], v[196:199], v[160:163], v[68:71]\n"
"s_add_u32 s71, s75, 64512\n"
"v_cvt_pk_bf16_f32 v59, v62, v63\n"
"v_mfma_f32_16x16x32_bf16 v[68:71], v[200:203], v[184:187], v[68:71]\n"
"buffer_load_dwordx4 v3, s[48:51], s71 offen offset:1024 lds\n"
"v_mfma_f32_16x16x32_bf16 v[68:71], v[204:207], v[188:191], v[68:71]\n"
"ds_read_b64_tr_b16 v[208:209], v7 offset:33280\n"
"ds_read_b64_tr_b16 v[210:211], v7 offset:37376\n"
"v_mfma_f32_16x16x32_bf16 v[76:79], v[192:195], v[140:143], v[116:119]\n"
"ds_read_b64_tr_b16 v[212:213], v8 offset:33280\n"
"ds_read_b64_tr_b16 v[214:215], v8 offset:37376\n"
"v_mfma_f32_16x16x32_bf16 v[76:79], v[196:199], v[164:167], v[76:79]\n"
"ds_read_b64_tr_b16 v[216:217], v7 offset:33536\n"
"ds_read_b64_tr_b16 v[218:219], v7 offset:37632\n"
"v_mfma_f32_16x16x32_bf16 v[76:79], v[200:203], a[216:219], v[76:79]\n"
"ds_read_b128 a[216:219], v10 offset:32768\n"
"ds_read_b64_tr_b16 v[220:221], v8 offset:33536\n"
"v_mfma_f32_16x16x32_bf16 v[76:79], v[204:207], a[236:239], v[76:79]\n"
"s_add_u32 s64, s75, 129024\n"
"ds_read_b128 a[236:239], v10 offset:37376\n"
"v_mfma_f32_16x16x32_bf16 v[84:87], v[192:195], v[144:147], v[120:123]\n"
"buffer_load_dwordx4 v3, s[48:51], s64 offen offset:2048 lds\n"
"v_mfma_f32_16x16x32_bf16 v[84:87], v[196:199], v[168:171], v[84:87]\n"
"ds_read_b64_tr_b16 v[222:223], v8 offset:37632\n"
"v_max3_f32 v12, v64, v65, v66\n"
"v_mfma_f32_16x16x32_bf16 v[84:87], v[200:203], a[220:223], v[84:87]\n"
"ds_read_b128 a[220:223], v10 offset:33280\n"
"v_max3_f32 v13, v67, v72, v73\n"
"v_mfma_f32_16x16x32_bf16 v[84:87], v[204:207], a[240:243], v[84:87]\n"
"v_max3_f32 v14, v74, v75, v80\n"
"v_max3_f32 v12, v12, v81, v82\n"
"v_mfma_f32_16x16x32_bf16 v[92:95], v[192:195], v[148:151], v[124:127]\n"
"v_max3_f32 v13, v13, v83, v88\n"
"v_max3_f32 v14, v14, v89, v90\n"
"v_mfma_f32_16x16x32_bf16 v[92:95], v[196:199], v[172:175], v[92:95]\n"
"s_add_u32 s65, s75, 193536\n"
"v_max3_f32 v12, v12, v91, v96\n"
"v_mfma_f32_16x16x32_bf16 v[92:95], v[200:203], a[224:227], v[92:95]\n"
"buffer_load_dwordx4 v3, s[48:51], s65 offen offset:3072 lds\n"
"v_mfma_f32_16x16x32_bf16 v[92:95], v[204:207], a[244:247], v[92:95]\n"
"ds_read_b128 a[224:227], v10 offset:34816\n"
"v_max3_f32 v13, v13, v97, v98\n"
"v_mfma_f32_16x16x32_bf16 v[100:103], v[192:195], v[152:155], v[128:131]\n"
"v_max3_f32 v14, v14, v99, v104\n"
"v_max3_f32 v12, v12, v105, v106\n"
"v_mfma_f32_16x16x32_bf16 v[100:103], v[196:199], v[176:179], v[100:103]\n"
"v_max3_f32 v13, v13, v107, v68\n"
"v_max3_f32 v14, v14, v69, v70\n"
"v_mfma_f32_16x16x32_bf16 v[100:103], v[200:203], a[228:231], v[100:103]\n"
"ds_read_b128 a[228:231], v10 offset:35328\n"
"v_max3_f32 v12, v12, v71, v76\n"
"v_mfma_f32_16x16x32_bf16 v[100:103], v[204:207], a[248:251], v[100:103]\n"
"v_max3_f32 v13, v13, v77, v78\n"
"v_max3_f32 v14, v14, v79, v84\n"
"v_mfma_f32_16x16x32_bf16 v[108:111], v[192:195], v[156:159], v[132:135]\n"
"ds_read_b64_tr_b16 v[192:193], v7 offset:32768\n"
"ds_read_b64_tr_b16 v[194:195], v7 offset:36864\n"
"v_mfma_f32_16x16x32_bf16 v[108:111], v[196:199], v[180:183], v[108:111]\n"
"ds_read_b64_tr_b16 v[196:197], v8 offset:32768\n"
"ds_read_b64_tr_b16 v[198:199], v8 offset:36864\n"
"v_mfma_f32_16x16x32_bf16 v[108:111], v[200:203], a[232:235], v[108:111]\n"
"s_add_u32 s74, s57, s82\n"
"s_add_u32 m0, s61, 0\n"
"v_mfma_f32_16x16x32_bf16 v[108:111], v[204:207], a[252:255], v[108:111]\n"
"s_add_u32 s66, s74, 0\n"
"ds_read_b64_tr_b16 v[200:201], v7 offset:33024\n"
"ds_read_b64_tr_b16 v[202:203], v7 offset:37120\n"
"buffer_load_dwordx4 v4, s[44:47], s66 offen offset:0 lds\n"
"ds_read_b64_tr_b16 v[204:205], v8 offset:33024\n"
"ds_read_b64_tr_b16 v[206:207], v8 offset:37120\n"
"ds_read_b128 a[232:235], v10 offset:36864\n"
"v_max3_f32 v12, v12, v85, v86\n"
"v_add_u32 v7, 0x10000, v7\n"
"v_max3_f32 v13, v13, v87, v92\n"
"v_max3_f32 v14, v14, v93, v94\n"
"v_max3_f32 v12, v12, v95, v100\n"
"v_max3_f32 v13, v13, v101, v102\n"
"v_max3_f32 v14, v14, v103, v108\n"
"v_max3_f32 v12, v12, v109, v110\n"
"v_max3_f32 v12, v12, v13, v14\n"
"v_max_f32 v12, v12, v111\n"
"v_cmp_lt_f32 vcc, s78, v12\n"
"s_cbranch_vccnz LT28_%=\n"
"LR28_%=:\n"
"v_exp_f32 v64, v64\n"
"v_exp_f32 v65, v65\n"
"v_add_u32 v8, 0x10000, v8\n"
"s_waitcnt lgkmcnt(7)\n"
"v_mfma_f32_16x16x32_bf16 a[0:3], v[192:195], v[16:19], a[0:3]\n"
"v_exp_f32 v66, v66\n"
"v_mfma_f32_16x16x32_bf16 a[8:11], v[192:195], v[24:27], a[8:11]\n"
"v_exp_f32 v67, v67\n"
"v_mfma_f32_16x16x32_bf16 a[16:19], v[192:195], v[32:35], a[16:19]\n"
"s_add_u32 s67, s74, 8192\n"
"v_cvt_pk_bf16_f32 v64, v64, v65\n"
"v_mfma_f32_16x16x32_bf16 a[24:27], v[192:195], v[40:43], a[24:27]\n"
"buffer_load_dwordx4 v5, s[44:47], s67 offen offset:1024 lds\n"
"v_mfma_f32_16x16x32_bf16 a[32:35], v[192:195], v[48:51], a[32:35]\n"
"v_exp_f32 v68, v68\n"
"v_mfma_f32_16x16x32_bf16 a[40:43], v[192:195], v[56:59], a[40:43]\n"
"v_cvt_pk_bf16_f32 v65, v66, v67\n"
"s_waitcnt lgkmcnt(0)\n"
"v_mfma_f32_16x16x32_bf16 a[4:7], v[196:199], v[16:19], a[4:7]\n"
"v_exp_f32 v69, v69\n"
"v_mfma_f32_16x16x32_bf16 a[12:15], v[196:199], v[24:27], a[12:15]\n"
"v_exp_f32 v70, v70\n"
"v_mfma_f32_16x16x32_bf16 a[20:23], v[196:199], v[32:35], a[20:23]\n"
"s_add_u32 s68, s74, 16384\n"
"v_cvt_pk_bf16_f32 v66, v68, v69\n"
"v_mfma_f32_16x16x32_bf16 a[28:31], v[196:199], v[40:43], a[28:31]\n"
"buffer_load_dwordx4 v4, s[44:47], s68 offen offset:2048 lds\n"
"v_mfma_f32_16x16x32_bf16 a[36:39], v[196:199], v[48:51], a[36:39]\n"
"v_exp_f32 v71, v71\n"
"v_mfma_f32_16x16x32_bf16 a[44:47], v[196:199], v[56:59], a[44:47]\n"
"v_exp_f32 v72, v72\n"
"v_mfma_f32_16x16x32_bf16 a[48:51], v[200:203], v[16:19], a[48:51]\n"
"v_exp_f32 v73, v73\n"
"v_mfma_f32_16x16x32_bf16 a[56:59], v[200:203], v[24:27], a[56:59]\n"
"v_cvt_pk_bf16_f32 v67, v70, v71\n"
"v_cvt_pk_bf16_f32 v72, v72, v73\n"
"v_mfma_f32_16x16x32_bf16 a[64:67], v[200:203], v[32:35], a[64:67]\n"
"v_exp_f32 v74, v74\n"
"v_mfma_f32_16x16x32_bf16 a[72:75], v[200:203], v[40:43], a[72:75]\n"
"v_exp_f32 v75, v75\n"
"v_mfma_f32_16x16x32_bf16 a[80:83], v[200:203], v[48:51], a[80:83]\n"
"v_exp_f32 v76, v76\n"
"v_mfma_f32_16x16x32_bf16 a[88:91], v[200:203], v[56:59], a[88:91]\n"
"s_waitcnt vmcnt(7)\n"
"s_barrier\n"
"v_mfma_f32_16x16x32_bf16 a[52:55], v[204:207], v[16:19], a[52:55]\n"
"ds_read_b64_tr_b16 v[192:193], v7 offset:0\n"
"ds_read_b64_tr_b16 v[194:195], v7 offset:4096\n"
"v_mfma_f32_16x16x32_bf16 a[60:63], v[204:207], v[24:27], a[60:63]\n"
"v_exp_f32 v77, v77\n"
"v_mfma_f32_16x16x32_bf16 a[68:71], v[204:207], v[32:35], a[68:71]\n"
"v_exp_f32 v78, v78\n"
"v_mfma_f32_16x16x32_bf16 a[76:79], v[204:207], v[40:43], a[76:79]\n"
"v_exp_f32 v79, v79\n"
"v_mfma_f32_16x16x32_bf16 a[84:87], v[204:207], v[48:51], a[84:87]\n"
"v_cvt_pk_bf16_f32 v73, v74, v75\n"
"v_cvt_pk_bf16_f32 v74, v76, v77\n"
"v_mfma_f32_16x16x32_bf16 a[92:95], v[204:207], v[56:59], a[92:95]\n"
"v_exp_f32 v80, v80\n"
"v_mfma_f32_16x16x32_bf16 a[96:99], v[208:211], v[16:19], a[96:99]\n"
"v_exp_f32 v81, v81\n"
"v_mfma_f32_16x16x32_bf16 a[104:107], v[208:211], v[24:27], a[104:107]\n"
"s_add_u32 s75, s58, s83\n"
"s_add_u32 m0, s60, 49152\n"
"v_mfma_f32_16x16x32_bf16 a[112:115], v[208:211], v[32:35], a[112:115]\n"
"s_add_u32 s69, s75, 128\n"
"v_cvt_pk_bf16_f32 v75, v78, v79\n"
"v_mfma_f32_16x16x32_bf16 a[120:123], v[208:211], v[40:43], a[120:123]\n"
"buffer_load_dwordx4 v3, s[48:51], s69 offen offset:0 lds\n"
"v_mfma_f32_16x16x32_bf16 a[128:131], v[208:211], v[48:51], a[128:131]\n"
"v_exp_f32 v82, v82\n"
"v_mfma_f32_16x16x32_bf16 a[136:139], v[208:211], v[56:59], a[136:139]\n"
"v_exp_f32 v83, v83\n"
"v_mfma_f32_16x16x32_bf16 a[100:103], v[212:215], v[16:19], a[100:103]\n"
"v_exp_f32 v84, v84\n"
"v_mfma_f32_16x16x32_bf16 a[108:111], v[212:215], v[24:27], a[108:111]\n"
"v_exp_f32 v85, v85\n"
"v_mfma_f32_16x16x32_bf16 a[116:119], v[212:215], v[32:35], a[116:119]\n"
"s_add_u32 s70, s75, 64640\n"
"v_cvt_pk_bf16_f32 v80, v80, v81\n"
"v_mfma_f32_16x16x32_bf16 a[124:127], v[212:215], v[40:43], a[124:127]\n"
"buffer_load_dwordx4 v3, s[48:51], s70 offen offset:1024 lds\n"
"v_mfma_f32_16x16x32_bf16 a[132:135], v[212:215], v[48:51], a[132:135]\n"
"v_exp_f32 v86, v86\n"
"v_mfma_f32_16x16x32_bf16 a[140:143], v[212:215], v[56:59], a[140:143]\n"
"v_exp_f32 v87, v87\n"
"v_mfma_f32_16x16x32_bf16 a[144:147], v[216:219], v[16:19], a[144:147]\n"
"v_cvt_pk_bf16_f32 v81, v82, v83\n"
"v_cvt_pk_bf16_f32 v82, v84, v85\n"
"v_mfma_f32_16x16x32_bf16 a[152:155], v[216:219], v[24:27], a[152:155]\n"
"v_exp_f32 v88, v88\n"
"v_mfma_f32_16x16x32_bf16 a[160:163], v[216:219], v[32:35], a[160:163]\n"
"v_exp_f32 v89, v89\n"
"v_mfma_f32_16x16x32_bf16 a[168:171], v[216:219], v[40:43], a[168:171]\n"
"v_cvt_pk_bf16_f32 v83, v86, v87\n"
"v_cvt_pk_bf16_f32 v88, v88, v89\n"
"v_mfma_f32_16x16x32_bf16 a[176:179], v[216:219], v[48:51], a[176:179]\n"
"s_add_u32 s71, s75, 129152\n"
"ds_read_b64_tr_b16 v[196:197], v8 offset:0\n"
"v_mfma_f32_16x16x32_bf16 a[184:187], v[216:219], v[56:59], a[184:187]\n"
"buffer_load_dwordx4 v3, s[48:51], s71 offen offset:2048 lds\n"
"v_mfma_f32_16x16x32_bf16 a[148:151], v[220:223], v[16:19], a[148:151]\n"
"v_exp_f32 v90, v90\n"
"v_mfma_f32_16x16x32_bf16 a[156:159], v[220:223], v[24:27], a[156:159]\n"
"v_exp_f32 v91, v91\n"
"v_mfma_f32_16x16x32_bf16 a[164:167], v[220:223], v[32:35], a[164:167]\n"
"v_exp_f32 v92, v92\n"
"v_mfma_f32_16x16x32_bf16 a[172:175], v[220:223], v[40:43], a[172:175]\n"
"v_exp_f32 v93, v93\n"
"v_mfma_f32_16x16x32_bf16 a[180:183], v[220:223], v[48:51], a[180:183]\n"
"v_exp_f32 v94, v94\n"
"v_mfma_f32_16x16x32_bf16 a[188:191], v[220:223], v[56:59], a[188:191]\n"
"s_add_u32 s64, s75, 193664\n"
"v_cvt_pk_bf16_f32 v89, v90, v91\n"
"v_mfma_f32_16x16x32_bf16 a[192:195], v[240:243], v[16:19], a[192:195]\n"
"buffer_load_dwordx4 v3, s[48:51], s64 offen offset:3072 lds\n"
"v_mfma_f32_16x16x32_bf16 a[196:199], v[240:243], v[24:27], a[196:199]\n"
"v_exp_f32 v95, v95\n"
"v_mfma_f32_16x16x32_bf16 a[200:203], v[240:243], v[32:35], a[200:203]\n"
"v_exp_f32 v96, v96\n"
"v_mfma_f32_16x16x32_bf16 a[204:207], v[240:243], v[40:43], a[204:207]\n"
"v_exp_f32 v97, v97\n"
"v_mfma_f32_16x16x32_bf16 a[208:211], v[240:243], v[48:51], a[208:211]\n"
"v_cvt_pk_bf16_f32 v90, v92, v93\n"
"v_cvt_pk_bf16_f32 v91, v94, v95\n"
"v_mfma_f32_16x16x32_bf16 a[212:215], v[240:243], v[56:59], a[212:215]\n"
"s_cmp_lg_u32 s63, 0\n"
"s_cbranch_scc1 LT29_%=\n"
"LR29_%=:\n"
"v_mfma_f32_16x16x32_bf16 a[192:195], v[240:243], v[64:67], a[192:195]\n"
"v_exp_f32 v98, v98\n"
"v_mfma_f32_16x16x32_bf16 a[196:199], v[240:243], v[72:75], a[196:199]\n"
"v_exp_f32 v99, v99\n"
"v_mfma_f32_16x16x32_bf16 a[200:203], v[240:243], v[80:83], a[200:203]\n"
"v_exp_f32 v100, v100\n"
"v_exp_f32 v101, v101\n"
"v_exp_f32 v102, v102\n"
"v_exp_f32 v103, v103\n"
"v_mfma_f32_16x16x32_bf16 a[204:207], v[240:243], v[88:91], a[204:207]\n"
"ds_read_b64_tr_b16 v[198:199], v8 offset:4096\n"
"v_cvt_pk_bf16_f32 v96, v96, v97\n"
"v_cvt_pk_bf16_f32 v97, v98, v99\n"
"v_exp_f32 v104, v104\n"
"v_exp_f32 v105, v105\n"
"v_cvt_pk_bf16_f32 v98, v100, v101\n"
"v_cvt_pk_bf16_f32 v99, v102, v103\n"
"v_exp_f32 v106, v106\n"
"v_exp_f32 v107, v107\n"
"v_exp_f32 v108, v108\n"
"v_exp_f32 v109, v109\n"
"v_exp_f32 v110, v110\n"
"v_exp_f32 v111, v111\n"
"v_mfma_f32_16x16x32_bf16 a[208:211], v[240:243], v[96:99], a[208:211]\n"
"v_cvt_pk_bf16_f32 v104, v104, v105\n"
"v_cvt_pk_bf16_f32 v105, v106, v107\n"
"v_cvt_pk_bf16_f32 v106, v108, v109\n"
"v_cvt_pk_bf16_f32 v107, v110, v111\n"
"s_add_u32 s74, s57, s82\n"
"s_add_u32 m0, s61, 32768\n"
"v_mfma_f32_16x16x32_bf16 a[212:215], v[240:243], v[104:107], a[212:215]\n"
"s_add_u32 s65, s74, 55296\n"
"s_waitcnt lgkmcnt(1)\n"
"v_mfma_f32_16x16x32_bf16 a[0:3], v[192:195], v[64:67], a[0:3]\n"
"buffer_load_dwordx4 v4, s[44:47], s65 offen offset:0 lds\n"
"v_mfma_f32_16x16x32_bf16 a[8:11], v[192:195], v[72:75], a[8:11]\n"
"ds_read_b64_tr_b16 v[200:201], v7 offset:256\n"
"ds_read_b64_tr_b16 v[202:203], v7 offset:4352\n"
"v_mfma_f32_16x16x32_bf16 a[16:19], v[192:195], v[80:83], a[16:19]\n"
"s_add_u32 s66, s74, 63488\n"
"ds_read_b64_tr_b16 v[204:205], v8 offset:256\n"
"v_mfma_f32_16x16x32_bf16 a[24:27], v[192:195], v[88:91], a[24:27]\n"
"ds_read_b64_tr_b16 v[206:207], v8 offset:4352\n"
"ds_read_b64_tr_b16 v[224:225], v7 offset:512\n"
"v_mfma_f32_16x16x32_bf16 a[32:35], v[192:195], v[96:99], a[32:35]\n"
"buffer_load_dwordx4 v5, s[44:47], s66 offen offset:1024 lds\n"
"v_mfma_f32_16x16x32_bf16 a[40:43], v[192:195], v[104:107], a[40:43]\n"
"ds_read_b64_tr_b16 v[226:227], v7 offset:4608\n"
"s_waitcnt lgkmcnt(6)\n"
"v_mfma_f32_16x16x32_bf16 a[4:7], v[196:199], v[64:67], a[4:7]\n"
"ds_read_b64_tr_b16 v[228:229], v8 offset:512\n"
"ds_read_b64_tr_b16 v[230:231], v8 offset:4608\n"
"v_mfma_f32_16x16x32_bf16 a[12:15], v[196:199], v[72:75], a[12:15]\n"
"ds_read_b64_tr_b16 v[192:193], v7 offset:768\n"
"ds_read_b64_tr_b16 v[194:195], v7 offset:4864\n"
"v_mfma_f32_16x16x32_bf16 a[20:23], v[196:199], v[80:83], a[20:23]\n"
"v_add_u32 v10, 0x10000, v10\n"
"v_accvgpr_read_b32 v244, a192\n"
"v_mfma_f32_16x16x32_bf16 a[28:31], v[196:199], v[88:91], a[28:31]\n"
"ds_read_b128 a[240:243], v10 offset:0\n"
"ds_read_b128 a[244:247], v10 offset:512\n"
"v_mfma_f32_16x16x32_bf16 a[36:39], v[196:199], v[96:99], a[36:39]\n"
"s_add_u32 s67, s74, 71680\n"
"ds_read_b128 a[248:251], v10 offset:2048\n"
"v_mfma_f32_16x16x32_bf16 a[44:47], v[196:199], v[104:107], a[44:47]\n"
"ds_read_b64_tr_b16 v[196:197], v8 offset:768\n"
"s_waitcnt lgkmcnt(12)\n"
"v_mfma_f32_16x16x32_bf16 a[48:51], v[200:203], v[64:67], a[48:51]\n"
"buffer_load_dwordx4 v4, s[44:47], s67 offen offset:2048 lds\n"
"v_mfma_f32_16x16x32_bf16 a[56:59], v[200:203], v[72:75], a[56:59]\n"
"ds_read_b64_tr_b16 v[198:199], v8 offset:4864\n"
"ds_read_b128 a[252:255], v10 offset:2560\n"
"v_mfma_f32_16x16x32_bf16 a[64:67], v[200:203], v[80:83], a[64:67]\n"
"ds_read_b128 v[232:235], v10 offset:4096\n"
"ds_read_b128 v[236:239], v10 offset:4608\n"
"v_mfma_f32_16x16x32_bf16 a[72:75], v[200:203], v[88:91], a[72:75]\n"
"v_rcp_f32 v244, v244\n"
"v_mfma_f32_16x16x32_bf16 a[80:83], v[200:203], v[96:99], a[80:83]\n"
"v_accvgpr_read_b32 v240, a0\n"
"v_accvgpr_read_b32 v241, a1\n"
"v_mfma_f32_16x16x32_bf16 a[88:91], v[200:203], v[104:107], a[88:91]\n"
"v_accvgpr_read_b32 v242, a2\n"
"s_waitcnt lgkmcnt(8)\n"
"v_mfma_f32_16x16x32_bf16 a[52:55], v[204:207], v[64:67], a[52:55]\n"
"v_accvgpr_read_b32 v243, a3\n"
"v_mul_f32 v240, v244, v240\n"
"v_mfma_f32_16x16x32_bf16 a[60:63], v[204:207], v[72:75], a[60:63]\n"
"v_mul_f32 v241, v244, v241\n"
"v_accvgpr_read_b32 v250, a4\n"
"v_mfma_f32_16x16x32_bf16 a[68:71], v[204:207], v[80:83], a[68:71]\n"
"v_accvgpr_read_b32 v251, a5\n"
"v_accvgpr_read_b32 v252, a6\n"
"v_mfma_f32_16x16x32_bf16 a[76:79], v[204:207], v[88:91], a[76:79]\n"
"v_accvgpr_read_b32 v253, a7\n"
"v_mul_f32 v242, v244, v242\n"
"v_mfma_f32_16x16x32_bf16 a[84:87], v[204:207], v[96:99], a[84:87]\n"
"v_mul_f32 v243, v244, v243\n"
"v_cvt_pk_bf16_f32 v240, v240, v241\n"
"v_mfma_f32_16x16x32_bf16 a[92:95], v[204:207], v[104:107], a[92:95]\n"
"v_mul_f32 v250, v244, v250\n"
"v_mul_f32 v251, v244, v251\n"
"v_mfma_f32_16x16x32_bf16 a[96:99], v[224:227], v[64:67], a[96:99]\n"
"v_mul_f32 v252, v244, v252\n"
"v_mul_f32 v253, v244, v253\n"
"v_mfma_f32_16x16x32_bf16 a[104:107], v[224:227], v[72:75], a[104:107]\n"
"v_cvt_pk_bf16_f32 v241, v242, v243\n"
"v_cvt_pk_bf16_f32 v242, v250, v251\n"
"v_mfma_f32_16x16x32_bf16 a[112:115], v[224:227], v[80:83], a[112:115]\n"
"v_cvt_pk_bf16_f32 v243, v252, v253\n"
"v_accvgpr_read_b32 v245, a196\n"
"v_mfma_f32_16x16x32_bf16 a[120:123], v[224:227], v[88:91], a[120:123]\n"
"buffer_store_dwordx4 v[240:243], v11, s[52:55], s72 offen offset:0 sc1\n"
"v_mfma_f32_16x16x32_bf16 a[128:131], v[224:227], v[96:99], a[128:131]\n"
"v_rcp_f32 v245, v245\n"
"v_mfma_f32_16x16x32_bf16 a[136:139], v[224:227], v[104:107], a[136:139]\n"
"v_accvgpr_read_b32 v240, a8\n"
"v_accvgpr_read_b32 v241, a9\n"
"v_mfma_f32_16x16x32_bf16 a[100:103], v[228:231], v[64:67], a[100:103]\n"
"v_accvgpr_read_b32 v242, a10\n"
"v_accvgpr_read_b32 v243, a11\n"
"v_mfma_f32_16x16x32_bf16 a[108:111], v[228:231], v[72:75], a[108:111]\n"
"v_mul_f32 v240, v245, v240\n"
"v_mul_f32 v241, v245, v241\n"
"v_mfma_f32_16x16x32_bf16 a[116:119], v[228:231], v[80:83], a[116:119]\n"
"v_accvgpr_read_b32 v250, a12\n"
"v_accvgpr_read_b32 v251, a13\n"
"v_mfma_f32_16x16x32_bf16 a[124:127], v[228:231], v[88:91], a[124:127]\n"
"v_accvgpr_read_b32 v252, a14\n"
"v_accvgpr_read_b32 v253, a15\n"
"v_mfma_f32_16x16x32_bf16 a[132:135], v[228:231], v[96:99], a[132:135]\n"
"v_mul_f32 v242, v245, v242\n"
"v_mul_f32 v243, v245, v243\n"
"v_mfma_f32_16x16x32_bf16 a[140:143], v[228:231], v[104:107], a[140:143]\n"
"v_cvt_pk_bf16_f32 v240, v240, v241\n"
"v_mul_f32 v250, v245, v250\n"
"v_mfma_f32_16x16x32_bf16 a[144:147], v[192:195], v[64:67], a[144:147]\n"
"v_mul_f32 v251, v245, v251\n"
"v_mul_f32 v252, v245, v252\n"
"v_mfma_f32_16x16x32_bf16 a[152:155], v[192:195], v[72:75], a[152:155]\n"
"v_mul_f32 v253, v245, v253\n"
"v_cvt_pk_bf16_f32 v241, v242, v243\n"
"v_mfma_f32_16x16x32_bf16 a[160:163], v[192:195], v[80:83], a[160:163]\n"
"v_cvt_pk_bf16_f32 v242, v250, v251\n"
"v_cvt_pk_bf16_f32 v243, v252, v253\n"
"v_mfma_f32_16x16x32_bf16 a[168:171], v[192:195], v[88:91], a[168:171]\n"
"v_accvgpr_read_b32 v246, a200\n"
"v_accvgpr_read_b32 v250, a20\n"
"v_mfma_f32_16x16x32_bf16 a[176:179], v[192:195], v[96:99], a[176:179]\n"
"buffer_store_dwordx4 v[240:243], v11, s[52:55], s73 offen offset:1024 sc1\n"
"v_mfma_f32_16x16x32_bf16 a[184:187], v[192:195], v[104:107], a[184:187]\n"
"s_nop 0\n"
"v_accvgpr_read_b32 v240, a16\n"
"s_waitcnt lgkmcnt(0)\n"
"v_mfma_f32_16x16x32_bf16 a[148:151], v[196:199], v[64:67], a[148:151]\n"
"v_rcp_f32 v246, v246\n"
"v_mfma_f32_16x16x32_bf16 a[156:159], v[196:199], v[72:75], a[156:159]\n"
"v_accvgpr_read_b32 v241, a17\n"
"v_accvgpr_read_b32 v242, a18\n"
"v_mfma_f32_16x16x32_bf16 a[164:167], v[196:199], v[80:83], a[164:167]\n"
"v_accvgpr_read_b32 v243, a19\n"
"v_mul_f32 v240, v246, v240\n"
"v_mfma_f32_16x16x32_bf16 a[172:175], v[196:199], v[88:91], a[172:175]\n"
"v_mul_f32 v241, v246, v241\n"
"v_accvgpr_read_b32 v251, a21\n"
"v_mfma_f32_16x16x32_bf16 a[180:183], v[196:199], v[96:99], a[180:183]\n"
"v_accvgpr_read_b32 v252, a22\n"
"v_accvgpr_read_b32 v253, a23\n"
"v_mfma_f32_16x16x32_bf16 a[188:191], v[196:199], v[104:107], a[188:191]\n"
"v_mul_f32 v242, v246, v242\n"
"v_mul_f32 v243, v246, v243\n"
"v_cvt_pk_bf16_f32 v240, v240, v241\n"
"v_mul_f32 v250, v246, v250\n"
"v_mul_f32 v251, v246, v251\n"
"v_mul_f32 v252, v246, v252\n"
"v_mul_f32 v253, v246, v253\n"
"v_cvt_pk_bf16_f32 v241, v242, v243\n"
"v_cvt_pk_bf16_f32 v242, v250, v251\n"
"v_cvt_pk_bf16_f32 v243, v252, v253\n"
"s_add_u32 s72, s59, 0\n"
"v_accvgpr_read_b32 v247, a204\n"
"buffer_store_dwordx4 v[240:243], v11, s[52:55], s72 offen offset:2048 sc1\n"
"v_rcp_f32 v247, v247\n"
"s_nop 0\n"
"v_accvgpr_read_b32 v240, a24\n"
"v_accvgpr_read_b32 v241, a25\n"
"v_accvgpr_read_b32 v242, a26\n"
"v_accvgpr_read_b32 v243, a27\n"
"v_mul_f32 v240, v247, v240\n"
"v_mul_f32 v241, v247, v241\n"
"v_accvgpr_read_b32 v250, a28\n"
"v_accvgpr_read_b32 v251, a29\n"
"v_accvgpr_read_b32 v252, a30\n"
"v_accvgpr_read_b32 v253, a31\n"
"v_mul_f32 v242, v247, v242\n"
"v_mul_f32 v243, v247, v243\n"
"v_cvt_pk_bf16_f32 v240, v240, v241\n"
"v_mul_f32 v250, v247, v250\n"
"v_mul_f32 v251, v247, v251\n"
"v_mul_f32 v252, v247, v252\n"
"v_mul_f32 v253, v247, v253\n"
"v_cvt_pk_bf16_f32 v241, v242, v243\n"
"v_cvt_pk_bf16_f32 v242, v250, v251\n"
"v_cvt_pk_bf16_f32 v243, v252, v253\n"
"s_add_u32 s73, s59, 393216\n"
"v_accvgpr_read_b32 v248, a208\n"
"buffer_store_dwordx4 v[240:243], v11, s[52:55], s73 offen offset:0 sc1\n"
"v_rcp_f32 v248, v248\n"
"s_nop 0\n"
"v_accvgpr_read_b32 v240, a32\n"
"v_accvgpr_read_b32 v241, a33\n"
"v_accvgpr_read_b32 v242, a34\n"
"v_accvgpr_read_b32 v243, a35\n"
"v_mul_f32 v240, v248, v240\n"
"v_mul_f32 v241, v248, v241\n"
"v_accvgpr_read_b32 v250, a36\n"
"v_accvgpr_read_b32 v251, a37\n"
"v_accvgpr_read_b32 v252, a38\n"
"v_accvgpr_read_b32 v253, a39\n"
"v_mul_f32 v242, v248, v242\n"
"v_mul_f32 v243, v248, v243\n"
"v_cvt_pk_bf16_f32 v240, v240, v241\n"
"v_mul_f32 v250, v248, v250\n"
"v_mul_f32 v251, v248, v251\n"
"v_mul_f32 v252, v248, v252\n"
"v_mul_f32 v253, v248, v253\n"
"v_cvt_pk_bf16_f32 v241, v242, v243\n"
"s_waitcnt vmcnt(11)\n"
"s_barrier\n"
"v_accvgpr_read_b32 v249, a212\n"
"v_cvt_pk_bf16_f32 v242, v250, v251\n"
"v_cvt_pk_bf16_f32 v243, v252, v253\n"
"s_add_u32 s72, s59, 393216\n"
"s_lshl_b32 s68, s91, 17\n"
"s_add_u32 s83, s83, 524288\n"
"s_cmp_eq_u32 s83, 4718592\n"
"s_cselect_b32 s83, 0, s83\n"
"s_add_u32 s82, s82, 128\n"
"s_cmp_eq_u32 s82, 1152\n"
"s_cselect_b32 s82, 0, s82\n"
"v_rcp_f32 v249, v249\n"
"buffer_store_dwordx4 v[240:243], v11, s[52:55], s72 offen offset:1024 sc1\n"
"s_add_u32 s56, s56, s68\n"
"ds_read_b64_tr_b16 v[208:209], v9 offset:0\n"
"ds_read_b64_tr_b16 v[210:211], v9 offset:512\n"
"ds_read_b64_tr_b16 v[212:213], v9 offset:4096\n"
"ds_read_b64_tr_b16 v[214:215], v9 offset:4608\n"
"ds_read_b64_tr_b16 v[216:217], v9 offset:8\n"
"ds_read_b64_tr_b16 v[218:219], v9 offset:520\n"
"ds_read_b64_tr_b16 v[220:221], v9 offset:4104\n"
"ds_read_b64_tr_b16 v[222:223], v9 offset:4616\n"
"v_add_u32 v7, 0xfffe0000, v7\n"
"v_add_u32 v8, 0xfffe0000, v8\n"
"s_waitcnt lgkmcnt(0)\n"
"s_sub_u32 s62, s62, 1\n"
"s_cmp_lg_u32 s62, 0\n"
"s_nop 7\n"
"s_cbranch_scc1 LOOP_%=\n"
"v_accvgpr_read_b32 v240, a40\n"
"v_accvgpr_read_b32 v241, a41\n"
"v_accvgpr_read_b32 v242, a42\n"
"v_accvgpr_read_b32 v243, a43\n"
"v_mul_f32 v240, v249, v240\n"
"v_mul_f32 v241, v249, v241\n"
"v_accvgpr_read_b32 v250, a44\n"
"v_accvgpr_read_b32 v251, a45\n"
"v_accvgpr_read_b32 v252, a46\n"
"v_accvgpr_read_b32 v253, a47\n"
"v_mul_f32 v242, v249, v242\n"
"v_mul_f32 v243, v249, v243\n"
"v_cvt_pk_bf16_f32 v240, v240, v241\n"
"v_mul_f32 v250, v249, v250\n"
"v_mul_f32 v251, v249, v251\n"
"v_mul_f32 v252, v249, v252\n"
"v_mul_f32 v253, v249, v253\n"
"v_cvt_pk_bf16_f32 v241, v242, v243\n"
"v_cvt_pk_bf16_f32 v242, v250, v251\n"
"v_cvt_pk_bf16_f32 v243, v252, v253\n"
"s_add_u32 s73, s59, 393216\n"
"buffer_store_dwordx4 v[240:243], v11, s[52:55], s73 offen offset:2048 sc1\n"
"v_accvgpr_read_b32 v250, a52\n"
"s_nop 0\n"
"v_accvgpr_read_b32 v240, a48\n"
"v_accvgpr_read_b32 v241, a49\n"
"v_accvgpr_read_b32 v242, a50\n"
"v_accvgpr_read_b32 v243, a51\n"
"v_mul_f32 v240, v244, v240\n"
"v_mul_f32 v241, v244, v241\n"
"v_accvgpr_read_b32 v251, a53\n"
"v_accvgpr_read_b32 v252, a54\n"
"v_accvgpr_read_b32 v253, a55\n"
"v_mul_f32 v242, v244, v242\n"
"v_mul_f32 v243, v244, v243\n"
"v_cvt_pk_bf16_f32 v240, v240, v241\n"
"v_mul_f32 v250, v244, v250\n"
"v_mul_f32 v251, v244, v251\n"
"v_mul_f32 v252, v244, v252\n"
"v_mul_f32 v253, v244, v253\n"
"v_cvt_pk_bf16_f32 v241, v242, v243\n"
"v_cvt_pk_bf16_f32 v242, v250, v251\n"
"v_cvt_pk_bf16_f32 v243, v252, v253\n"
"s_add_u32 s72, s59, 3072\n"
"buffer_store_dwordx4 v[240:243], v11, s[52:55], s72 offen offset:0 sc1\n"
"v_accvgpr_read_b32 v250, a60\n"
"s_nop 0\n"
"v_accvgpr_read_b32 v240, a56\n"
"v_accvgpr_read_b32 v241, a57\n"
"v_accvgpr_read_b32 v242, a58\n"
"v_accvgpr_read_b32 v243, a59\n"
"v_mul_f32 v240, v245, v240\n"
"v_mul_f32 v241, v245, v241\n"
"v_accvgpr_read_b32 v251, a61\n"
"v_accvgpr_read_b32 v252, a62\n"
"v_accvgpr_read_b32 v253, a63\n"
"v_mul_f32 v242, v245, v242\n"
"v_mul_f32 v243, v245, v243\n"
"v_cvt_pk_bf16_f32 v240, v240, v241\n"
"v_mul_f32 v250, v245, v250\n"
"v_mul_f32 v251, v245, v251\n"
"v_mul_f32 v252, v245, v252\n"
"v_mul_f32 v253, v245, v253\n"
"v_cvt_pk_bf16_f32 v241, v242, v243\n"
"v_cvt_pk_bf16_f32 v242, v250, v251\n"
"v_cvt_pk_bf16_f32 v243, v252, v253\n"
"s_add_u32 s73, s59, 3072\n"
"buffer_store_dwordx4 v[240:243], v11, s[52:55], s73 offen offset:1024 sc1\n"
"v_accvgpr_read_b32 v250, a68\n"
"s_nop 0\n"
"v_accvgpr_read_b32 v240, a64\n"
"v_accvgpr_read_b32 v241, a65\n"
"v_accvgpr_read_b32 v242, a66\n"
"v_accvgpr_read_b32 v243, a67\n"
"v_mul_f32 v240, v246, v240\n"
"v_mul_f32 v241, v246, v241\n"
"v_accvgpr_read_b32 v251, a69\n"
"v_accvgpr_read_b32 v252, a70\n"
"v_accvgpr_read_b32 v253, a71\n"
"v_mul_f32 v242, v246, v242\n"
"v_mul_f32 v243, v246, v243\n"
"v_cvt_pk_bf16_f32 v240, v240, v241\n"
"v_mul_f32 v250, v246, v250\n"
"v_mul_f32 v251, v246, v251\n"
"v_mul_f32 v252, v246, v252\n"
"v_mul_f32 v253, v246, v253\n"
"v_cvt_pk_bf16_f32 v241, v242, v243\n"
"v_cvt_pk_bf16_f32 v242, v250, v251\n"
"v_cvt_pk_bf16_f32 v243, v252, v253\n"
"s_add_u32 s72, s59, 3072\n"
"buffer_store_dwordx4 v[240:243], v11, s[52:55], s72 offen offset:2048 sc1\n"
"v_accvgpr_read_b32 v250, a76\n"
"s_nop 0\n"
"v_accvgpr_read_b32 v240, a72\n"
"v_accvgpr_read_b32 v241, a73\n"
"v_accvgpr_read_b32 v242, a74\n"
"v_accvgpr_read_b32 v243, a75\n"
"v_mul_f32 v240, v247, v240\n"
"v_mul_f32 v241, v247, v241\n"
"v_accvgpr_read_b32 v251, a77\n"
"v_accvgpr_read_b32 v252, a78\n"
"v_accvgpr_read_b32 v253, a79\n"
"v_mul_f32 v242, v247, v242\n"
"v_mul_f32 v243, v247, v243\n"
"v_cvt_pk_bf16_f32 v240, v240, v241\n"
"v_mul_f32 v250, v247, v250\n"
"v_mul_f32 v251, v247, v251\n"
"v_mul_f32 v252, v247, v252\n"
"v_mul_f32 v253, v247, v253\n"
"v_cvt_pk_bf16_f32 v241, v242, v243\n"
"v_cvt_pk_bf16_f32 v242, v250, v251\n"
"v_cvt_pk_bf16_f32 v243, v252, v253\n"
"s_add_u32 s73, s59, 396288\n"
"buffer_store_dwordx4 v[240:243], v11, s[52:55], s73 offen offset:0 sc1\n"
"v_accvgpr_read_b32 v250, a84\n"
"s_nop 0\n"
"v_accvgpr_read_b32 v240, a80\n"
"v_accvgpr_read_b32 v241, a81\n"
"v_accvgpr_read_b32 v242, a82\n"
"v_accvgpr_read_b32 v243, a83\n"
"v_mul_f32 v240, v248, v240\n"
"v_mul_f32 v241, v248, v241\n"
"v_accvgpr_read_b32 v251, a85\n"
"v_accvgpr_read_b32 v252, a86\n"
"v_accvgpr_read_b32 v253, a87\n"
"v_mul_f32 v242, v248, v242\n"
"v_mul_f32 v243, v248, v243\n"
"v_cvt_pk_bf16_f32 v240, v240, v241\n"
"v_mul_f32 v250, v248, v250\n"
"v_mul_f32 v251, v248, v251\n"
"v_mul_f32 v252, v248, v252\n"
"v_mul_f32 v253, v248, v253\n"
"v_cvt_pk_bf16_f32 v241, v242, v243\n"
"v_cvt_pk_bf16_f32 v242, v250, v251\n"
"v_cvt_pk_bf16_f32 v243, v252, v253\n"
"s_add_u32 s72, s59, 396288\n"
"buffer_store_dwordx4 v[240:243], v11, s[52:55], s72 offen offset:1024 sc1\n"
"v_accvgpr_read_b32 v250, a92\n"
"s_nop 0\n"
"v_accvgpr_read_b32 v240, a88\n"
"v_accvgpr_read_b32 v241, a89\n"
"v_accvgpr_read_b32 v242, a90\n"
"v_accvgpr_read_b32 v243, a91\n"
"v_mul_f32 v240, v249, v240\n"
"v_mul_f32 v241, v249, v241\n"
"v_accvgpr_read_b32 v251, a93\n"
"v_accvgpr_read_b32 v252, a94\n"
"v_accvgpr_read_b32 v253, a95\n"
"v_mul_f32 v242, v249, v242\n"
"v_mul_f32 v243, v249, v243\n"
"v_cvt_pk_bf16_f32 v240, v240, v241\n"
"v_mul_f32 v250, v249, v250\n"
"v_mul_f32 v251, v249, v251\n"
"v_mul_f32 v252, v249, v252\n"
"v_mul_f32 v253, v249, v253\n"
"v_cvt_pk_bf16_f32 v241, v242, v243\n"
"v_cvt_pk_bf16_f32 v242, v250, v251\n"
"v_cvt_pk_bf16_f32 v243, v252, v253\n"
"s_add_u32 s73, s59, 396288\n"
"buffer_store_dwordx4 v[240:243], v11, s[52:55], s73 offen offset:2048 sc1\n"
"v_accvgpr_read_b32 v250, a100\n"
"s_nop 0\n"
"v_accvgpr_read_b32 v240, a96\n"
"v_accvgpr_read_b32 v241, a97\n"
"v_accvgpr_read_b32 v242, a98\n"
"v_accvgpr_read_b32 v243, a99\n"
"v_mul_f32 v240, v244, v240\n"
"v_mul_f32 v241, v244, v241\n"
"v_accvgpr_read_b32 v251, a101\n"
"v_accvgpr_read_b32 v252, a102\n"
"v_accvgpr_read_b32 v253, a103\n"
"v_mul_f32 v242, v244, v242\n"
"v_mul_f32 v243, v244, v243\n"
"v_cvt_pk_bf16_f32 v240, v240, v241\n"
"v_mul_f32 v250, v244, v250\n"
"v_mul_f32 v251, v244, v251\n"
"v_mul_f32 v252, v244, v252\n"
"v_mul_f32 v253, v244, v253\n"
"v_cvt_pk_bf16_f32 v241, v242, v243\n"
"v_cvt_pk_bf16_f32 v242, v250, v251\n"
"v_cvt_pk_bf16_f32 v243, v252, v253\n"
"s_add_u32 s72, s59, 6144\n"
"buffer_store_dwordx4 v[240:243], v11, s[52:55], s72 offen offset:0 sc1\n"
"v_accvgpr_read_b32 v250, a108\n"
"s_nop 0\n"
"v_accvgpr_read_b32 v240, a104\n"
"v_accvgpr_read_b32 v241, a105\n"
"v_accvgpr_read_b32 v242, a106\n"
"v_accvgpr_read_b32 v243, a107\n"
"v_mul_f32 v240, v245, v240\n"
"v_mul_f32 v241, v245, v241\n"
"v_accvgpr_read_b32 v251, a109\n"
"v_accvgpr_read_b32 v252, a110\n"
"v_accvgpr_read_b32 v253, a111\n"
"v_mul_f32 v242, v245, v242\n"
"v_mul_f32 v243, v245, v243\n"
"v_cvt_pk_bf16_f32 v240, v240, v241\n"
"v_mul_f32 v250, v245, v250\n"
"v_mul_f32 v251, v245, v251\n"
"v_mul_f32 v252, v245, v252\n"
"v_mul_f32 v253, v245, v253\n"
"v_cvt_pk_bf16_f32 v241, v242, v243\n"
"v_cvt_pk_bf16_f32 v242, v250, v251\n"
"v_cvt_pk_bf16_f32 v243, v252, v253\n"
"s_add_u32 s73, s59, 6144\n"
"buffer_store_dwordx4 v[240:243], v11, s[52:55], s73 offen offset:1024 sc1\n"
"v_accvgpr_read_b32 v250, a116\n"
"s_nop 0\n"
"v_accvgpr_read_b32 v240, a112\n"
"v_accvgpr_read_b32 v241, a113\n"
"v_accvgpr_read_b32 v242, a114\n"
"v_accvgpr_read_b32 v243, a115\n"
"v_mul_f32 v240, v246, v240\n"
"v_mul_f32 v241, v246, v241\n"
"v_accvgpr_read_b32 v251, a117\n"
"v_accvgpr_read_b32 v252, a118\n"
"v_accvgpr_read_b32 v253, a119\n"
"v_mul_f32 v242, v246, v242\n"
"v_mul_f32 v243, v246, v243\n"
"v_cvt_pk_bf16_f32 v240, v240, v241\n"
"v_mul_f32 v250, v246, v250\n"
"v_mul_f32 v251, v246, v251\n"
"v_mul_f32 v252, v246, v252\n"
"v_mul_f32 v253, v246, v253\n"
"v_cvt_pk_bf16_f32 v241, v242, v243\n"
"v_cvt_pk_bf16_f32 v242, v250, v251\n"
"v_cvt_pk_bf16_f32 v243, v252, v253\n"
"s_add_u32 s72, s59, 6144\n"
"buffer_store_dwordx4 v[240:243], v11, s[52:55], s72 offen offset:2048 sc1\n"
"v_accvgpr_read_b32 v250, a124\n"
"s_nop 0\n"
"v_accvgpr_read_b32 v240, a120\n"
"v_accvgpr_read_b32 v241, a121\n"
"v_accvgpr_read_b32 v242, a122\n"
"v_accvgpr_read_b32 v243, a123\n"
"v_mul_f32 v240, v247, v240\n"
"v_mul_f32 v241, v247, v241\n"
"v_accvgpr_read_b32 v251, a125\n"
"v_accvgpr_read_b32 v252, a126\n"
"v_accvgpr_read_b32 v253, a127\n"
"v_mul_f32 v242, v247, v242\n"
"v_mul_f32 v243, v247, v243\n"
"v_cvt_pk_bf16_f32 v240, v240, v241\n"
"v_mul_f32 v250, v247, v250\n"
"v_mul_f32 v251, v247, v251\n"
"v_mul_f32 v252, v247, v252\n"
"v_mul_f32 v253, v247, v253\n"
"v_cvt_pk_bf16_f32 v241, v242, v243\n"
"v_cvt_pk_bf16_f32 v242, v250, v251\n"
"v_cvt_pk_bf16_f32 v243, v252, v253\n"
"s_add_u32 s73, s59, 399360\n"
"buffer_store_dwordx4 v[240:243], v11, s[52:55], s73 offen offset:0 sc1\n"
"v_accvgpr_read_b32 v250, a132\n"
"s_nop 0\n"
"v_accvgpr_read_b32 v240, a128\n"
"v_accvgpr_read_b32 v241, a129\n"
"v_accvgpr_read_b32 v242, a130\n"
"v_accvgpr_read_b32 v243, a131\n"
"v_mul_f32 v240, v248, v240\n"
"v_mul_f32 v241, v248, v241\n"
"v_accvgpr_read_b32 v251, a133\n"
"v_accvgpr_read_b32 v252, a134\n"
"v_accvgpr_read_b32 v253, a135\n"
"v_mul_f32 v242, v248, v242\n"
"v_mul_f32 v243, v248, v243\n"
"v_cvt_pk_bf16_f32 v240, v240, v241\n"
"v_mul_f32 v250, v248, v250\n"
"v_mul_f32 v251, v248, v251\n"
"v_mul_f32 v252, v248, v252\n"
"v_mul_f32 v253, v248, v253\n"
"v_cvt_pk_bf16_f32 v241, v242, v243\n"
"v_cvt_pk_bf16_f32 v242, v250, v251\n"
"v_cvt_pk_bf16_f32 v243, v252, v253\n"
"s_add_u32 s72, s59, 399360\n"
"buffer_store_dwordx4 v[240:243], v11, s[52:55], s72 offen offset:1024 sc1\n"
"v_accvgpr_read_b32 v250, a140\n"
"s_nop 0\n"
"v_accvgpr_read_b32 v240, a136\n"
"v_accvgpr_read_b32 v241, a137\n"
"v_accvgpr_read_b32 v242, a138\n"
"v_accvgpr_read_b32 v243, a139\n"
"v_mul_f32 v240, v249, v240\n"
"v_mul_f32 v241, v249, v241\n"
"v_accvgpr_read_b32 v251, a141\n"
"v_accvgpr_read_b32 v252, a142\n"
"v_accvgpr_read_b32 v253, a143\n"
"v_mul_f32 v242, v249, v242\n"
"v_mul_f32 v243, v249, v243\n"
"v_cvt_pk_bf16_f32 v240, v240, v241\n"
"v_mul_f32 v250, v249, v250\n"
"v_mul_f32 v251, v249, v251\n"
"v_mul_f32 v252, v249, v252\n"
"v_mul_f32 v253, v249, v253\n"
"v_cvt_pk_bf16_f32 v241, v242, v243\n"
"v_cvt_pk_bf16_f32 v242, v250, v251\n"
"v_cvt_pk_bf16_f32 v243, v252, v253\n"
"s_add_u32 s73, s59, 399360\n"
"buffer_store_dwordx4 v[240:243], v11, s[52:55], s73 offen offset:2048 sc1\n"
"v_accvgpr_read_b32 v250, a148\n"
"s_nop 0\n"
"v_accvgpr_read_b32 v240, a144\n"
"v_accvgpr_read_b32 v241, a145\n"
"v_accvgpr_read_b32 v242, a146\n"
"v_accvgpr_read_b32 v243, a147\n"
"v_mul_f32 v240, v244, v240\n"
"v_mul_f32 v241, v244, v241\n"
"v_accvgpr_read_b32 v251, a149\n"
"v_accvgpr_read_b32 v252, a150\n"
"v_accvgpr_read_b32 v253, a151\n"
"v_mul_f32 v242, v244, v242\n"
"v_mul_f32 v243, v244, v243\n"
"v_cvt_pk_bf16_f32 v240, v240, v241\n"
"v_mul_f32 v250, v244, v250\n"
"v_mul_f32 v251, v244, v251\n"
"v_mul_f32 v252, v244, v252\n"
"v_mul_f32 v253, v244, v253\n"
"v_cvt_pk_bf16_f32 v241, v242, v243\n"
"v_cvt_pk_bf16_f32 v242, v250, v251\n"
"v_cvt_pk_bf16_f32 v243, v252, v253\n"
"s_add_u32 s72, s59, 9216\n"
"buffer_store_dwordx4 v[240:243], v11, s[52:55], s72 offen offset:0 sc1\n"
"v_accvgpr_read_b32 v250, a156\n"
"s_nop 0\n"
"v_accvgpr_read_b32 v240, a152\n"
"v_accvgpr_read_b32 v241, a153\n"
"v_accvgpr_read_b32 v242, a154\n"
"v_accvgpr_read_b32 v243, a155\n"
"v_mul_f32 v240, v245, v240\n"
"v_mul_f32 v241, v245, v241\n"
"v_accvgpr_read_b32 v251, a157\n"
"v_accvgpr_read_b32 v252, a158\n"
"v_accvgpr_read_b32 v253, a159\n"
"v_mul_f32 v242, v245, v242\n"
"v_mul_f32 v243, v245, v243\n"
"v_cvt_pk_bf16_f32 v240, v240, v241\n"
"v_mul_f32 v250, v245, v250\n"
"v_mul_f32 v251, v245, v251\n"
"v_mul_f32 v252, v245, v252\n"
"v_mul_f32 v253, v245, v253\n"
"v_cvt_pk_bf16_f32 v241, v242, v243\n"
"v_cvt_pk_bf16_f32 v242, v250, v251\n"
"v_cvt_pk_bf16_f32 v243, v252, v253\n"
"s_add_u32 s73, s59, 9216\n"
"buffer_store_dwordx4 v[240:243], v11, s[52:55], s73 offen offset:1024 sc1\n"
"v_accvgpr_read_b32 v250, a164\n"
"s_nop 0\n"
"v_accvgpr_read_b32 v240, a160\n"
"v_accvgpr_read_b32 v241, a161\n"
"v_accvgpr_read_b32 v242, a162\n"
"v_accvgpr_read_b32 v243, a163\n"
"v_mul_f32 v240, v246, v240\n"
"v_mul_f32 v241, v246, v241\n"
"v_accvgpr_read_b32 v251, a165\n"
"v_accvgpr_read_b32 v252, a166\n"
"v_accvgpr_read_b32 v253, a167\n"
"v_mul_f32 v242, v246, v242\n"
"v_mul_f32 v243, v246, v243\n"
"v_cvt_pk_bf16_f32 v240, v240, v241\n"
"v_mul_f32 v250, v246, v250\n"
"v_mul_f32 v251, v246, v251\n"
"v_mul_f32 v252, v246, v252\n"
"v_mul_f32 v253, v246, v253\n"
"v_cvt_pk_bf16_f32 v241, v242, v243\n"
"v_cvt_pk_bf16_f32 v242, v250, v251\n"
"v_cvt_pk_bf16_f32 v243, v252, v253\n"
"s_add_u32 s72, s59, 9216\n"
"buffer_store_dwordx4 v[240:243], v11, s[52:55], s72 offen offset:2048 sc1\n"
"v_accvgpr_read_b32 v250, a172\n"
"s_nop 0\n"
"v_accvgpr_read_b32 v240, a168\n"
"v_accvgpr_read_b32 v241, a169\n"
"v_accvgpr_read_b32 v242, a170\n"
"v_accvgpr_read_b32 v243, a171\n"
"v_mul_f32 v240, v247, v240\n"
"v_mul_f32 v241, v247, v241\n"
"v_accvgpr_read_b32 v251, a173\n"
"v_accvgpr_read_b32 v252, a174\n"
"v_accvgpr_read_b32 v253, a175\n"
"v_mul_f32 v242, v247, v242\n"
"v_mul_f32 v243, v247, v243\n"
"v_cvt_pk_bf16_f32 v240, v240, v241\n"
"v_mul_f32 v250, v247, v250\n"
"v_mul_f32 v251, v247, v251\n"
"v_mul_f32 v252, v247, v252\n"
"v_mul_f32 v253, v247, v253\n"
"v_cvt_pk_bf16_f32 v241, v242, v243\n"
"v_cvt_pk_bf16_f32 v242, v250, v251\n"
"v_cvt_pk_bf16_f32 v243, v252, v253\n"
"s_add_u32 s73, s59, 402432\n"
"buffer_store_dwordx4 v[240:243], v11, s[52:55], s73 offen offset:0 sc1\n"
"v_accvgpr_read_b32 v250, a180\n"
"s_nop 0\n"
"v_accvgpr_read_b32 v240, a176\n"
"v_accvgpr_read_b32 v241, a177\n"
"v_accvgpr_read_b32 v242, a178\n"
"v_accvgpr_read_b32 v243, a179\n"
"v_mul_f32 v240, v248, v240\n"
"v_mul_f32 v241, v248, v241\n"
"v_accvgpr_read_b32 v251, a181\n"
"v_accvgpr_read_b32 v252, a182\n"
"v_accvgpr_read_b32 v253, a183\n"
"v_mul_f32 v242, v248, v242\n"
"v_mul_f32 v243, v248, v243\n"
"v_cvt_pk_bf16_f32 v240, v240, v241\n"
"v_mul_f32 v250, v248, v250\n"
"v_mul_f32 v251, v248, v251\n"
"v_mul_f32 v252, v248, v252\n"
"v_mul_f32 v253, v248, v253\n"
"v_cvt_pk_bf16_f32 v241, v242, v243\n"
"v_cvt_pk_bf16_f32 v242, v250, v251\n"
"v_cvt_pk_bf16_f32 v243, v252, v253\n"
"s_add_u32 s72, s59, 402432\n"
"buffer_store_dwordx4 v[240:243], v11, s[52:55], s72 offen offset:1024 sc1\n"
"v_accvgpr_read_b32 v250, a188\n"
"s_nop 0\n"
"v_accvgpr_read_b32 v240, a184\n"
"v_accvgpr_read_b32 v241, a185\n"
"v_accvgpr_read_b32 v242, a186\n"
"v_accvgpr_read_b32 v243, a187\n"
"v_mul_f32 v240, v249, v240\n"
"v_mul_f32 v241, v249, v241\n"
"v_accvgpr_read_b32 v251, a189\n"
"v_accvgpr_read_b32 v252, a190\n"
"v_accvgpr_read_b32 v253, a191\n"
"v_mul_f32 v242, v249, v242\n"
"v_mul_f32 v243, v249, v243\n"
"v_cvt_pk_bf16_f32 v240, v240, v241\n"
"v_mul_f32 v250, v249, v250\n"
"v_mul_f32 v251, v249, v251\n"
"v_mul_f32 v252, v249, v252\n"
"v_mul_f32 v253, v249, v253\n"
"v_cvt_pk_bf16_f32 v241, v242, v243\n"
"v_cvt_pk_bf16_f32 v242, v250, v251\n"
"v_cvt_pk_bf16_f32 v243, v252, v253\n"
"s_add_u32 s73, s59, 402432\n"
"buffer_store_dwordx4 v[240:243], v11, s[52:55], s73 offen offset:2048 sc1\n"
"s_waitcnt vmcnt(0)\n"
"s_branch END_%=\n"
"LT0_%=:\n"
"s_call_b64 s[76:77], RA1_%=\n"
"s_branch LR0_%=\n"
"LT1_%=:\n"
"s_call_b64 s[76:77], RB_%=\n"
"s_branch LR1_%=\n"
"LT2_%=:\n"
"s_call_b64 s[76:77], RA0_%=\n"
"s_branch LR2_%=\n"
"LT3_%=:\n"
"s_call_b64 s[76:77], RB_%=\n"
"s_branch LR3_%=\n"
"LT4_%=:\n"
"s_call_b64 s[76:77], RA1_%=\n"
"s_branch LR4_%=\n"
"LT5_%=:\n"
"s_call_b64 s[76:77], RB_%=\n"
"s_branch LR5_%=\n"
"LT6_%=:\n"
"s_call_b64 s[76:77], RA0_%=\n"
"s_branch LR6_%=\n"
"LT7_%=:\n"
"s_call_b64 s[76:77], RB_%=\n"
"s_branch LR7_%=\n"
"LT8_%=:\n"
"s_call_b64 s[76:77], RA1_%=\n"
"s_branch LR8_%=\n"
"LT9_%=:\n"
"s_call_b64 s[76:77], RB_%=\n"
"s_branch LR9_%=\n"
"LT10_%=:\n"
"s_call_b64 s[76:77], RA0_%=\n"
"s_branch LR10_%=\n"
"LT11_%=:\n"
"s_call_b64 s[76:77], RB_%=\n"
"s_branch LR11_%=\n"
"LT12_%=:\n"
"s_call_b64 s[76:77], RA1_%=\n"
"s_branch LR12_%=\n"
"LT13_%=:\n"
"s_call_b64 s[76:77], RB_%=\n"
"s_branch LR13_%=\n"
"LT14_%=:\n"
"s_call_b64 s[76:77], RA0_%=\n"
"s_branch LR14_%=\n"
"LT15_%=:\n"
"s_call_b64 s[76:77], RB_%=\n"
"s_branch LR15_%=\n"
"LT16_%=:\n"
"s_call_b64 s[76:77], RA1_%=\n"
"s_branch LR16_%=\n"
"LT17_%=:\n"
"s_call_b64 s[76:77], RB_%=\n"
"s_branch LR17_%=\n"
"LT18_%=:\n"
"s_call_b64 s[76:77], RA0_%=\n"
"s_branch LR18_%=\n"
"LT19_%=:\n"
"s_call_b64 s[76:77], RB_%=\n"
"s_branch LR19_%=\n"
"LT20_%=:\n"
"s_call_b64 s[76:77], RA1_%=\n"
"s_branch LR20_%=\n"
"LT21_%=:\n"
"s_call_b64 s[76:77], RB_%=\n"
"s_branch LR21_%=\n"
"LT22_%=:\n"
"s_call_b64 s[76:77], RA0_%=\n"
"s_branch LR22_%=\n"
"LT23_%=:\n"
"s_call_b64 s[76:77], RB_%=\n"
"s_branch LR23_%=\n"
"LT24_%=:\n"
"s_call_b64 s[76:77], RA1_%=\n"
"s_branch LR24_%=\n"
"LT25_%=:\n"
"s_call_b64 s[76:77], RB_%=\n"
"s_branch LR25_%=\n"
"LT26_%=:\n"
"s_call_b64 s[76:77], RA0_%=\n"
"s_branch LR26_%=\n"
"LT27_%=:\n"
"s_call_b64 s[76:77], RB_%=\n"
"s_branch LR27_%=\n"
"LT28_%=:\n"
"s_call_b64 s[76:77], RA1_%=\n"
"s_branch LR28_%=\n"
"LT29_%=:\n"
"s_call_b64 s[76:77], RB_%=\n"
"s_branch LR29_%=\n"
"RA0_%=:\n"
"s_nop 7\n"
"v_max3_f32 v250, v16, v17, v18\n"
"v_max3_f32 v251, v19, v20, v21\n"
"v_max3_f32 v250, v250, v22, v23\n"
"v_max_f32 v250, v250, v251\n"
"v_mov_b32 v251, v250\n"
"s_nop 1\n"
"v_permlane16_swap_b32 v250, v251\n"
"v_max_f32 v250, v250, v251\n"
"v_mov_b32 v251, v250\n"
"s_nop 1\n"
"v_permlane32_swap_b32 v250, v251\n"
"v_max_f32 v250, v250, v251\n"
"v_max_f32 v250, 0, v250\n"
"v_sub_f32 v16, v16, v250\n"
"v_sub_f32 v17, v17, v250\n"
"v_sub_f32 v18, v18, v250\n"
"v_sub_f32 v19, v19, v250\n"
"v_sub_f32 v20, v20, v250\n"
"v_sub_f32 v21, v21, v250\n"
"v_sub_f32 v22, v22, v250\n"
"v_sub_f32 v23, v23, v250\n"
"v_sub_f32 v112, v112, v250\n"
"v_sub_f32 v113, v113, v250\n"
"v_sub_f32 v114, v114, v250\n"
"v_sub_f32 v115, v115, v250\n"
"v_exp_f32 v244, -v250\n"
"v_max3_f32 v250, v24, v25, v26\n"
"v_max3_f32 v251, v27, v28, v29\n"
"v_max3_f32 v250, v250, v30, v31\n"
"v_max_f32 v250, v250, v251\n"
"v_mov_b32 v251, v250\n"
"s_nop 1\n"
"v_permlane16_swap_b32 v250, v251\n"
"v_max_f32 v250, v250, v251\n"
"v_mov_b32 v251, v250\n"
"s_nop 1\n"
"v_permlane32_swap_b32 v250, v251\n"
"v_max_f32 v250, v250, v251\n"
"v_max_f32 v250, 0, v250\n"
"v_sub_f32 v24, v24, v250\n"
"v_sub_f32 v25, v25, v250\n"
"v_sub_f32 v26, v26, v250\n"
"v_sub_f32 v27, v27, v250\n"
"v_sub_f32 v28, v28, v250\n"
"v_sub_f32 v29, v29, v250\n"
"v_sub_f32 v30, v30, v250\n"
"v_sub_f32 v31, v31, v250\n"
"v_sub_f32 v116, v116, v250\n"
"v_sub_f32 v117, v117, v250\n"
"v_sub_f32 v118, v118, v250\n"
"v_sub_f32 v119, v119, v250\n"
"v_exp_f32 v245, -v250\n"
"v_max3_f32 v250, v32, v33, v34\n"
"v_max3_f32 v251, v35, v36, v37\n"
"v_max3_f32 v250, v250, v38, v39\n"
"v_max_f32 v250, v250, v251\n"
"v_mov_b32 v251, v250\n"
"s_nop 1\n"
"v_permlane16_swap_b32 v250, v251\n"
"v_max_f32 v250, v250, v251\n"
"v_mov_b32 v251, v250\n"
"s_nop 1\n"
"v_permlane32_swap_b32 v250, v251\n"
"v_max_f32 v250, v250, v251\n"
"v_max_f32 v250, 0, v250\n"
"v_sub_f32 v32, v32, v250\n"
"v_sub_f32 v33, v33, v250\n"
"v_sub_f32 v34, v34, v250\n"
"v_sub_f32 v35, v35, v250\n"
"v_sub_f32 v36, v36, v250\n"
"v_sub_f32 v37, v37, v250\n"
"v_sub_f32 v38, v38, v250\n"
"v_sub_f32 v39, v39, v250\n"
"v_sub_f32 v120, v120, v250\n"
"v_sub_f32 v121, v121, v250\n"
"v_sub_f32 v122, v122, v250\n"
"v_sub_f32 v123, v123, v250\n"
"v_exp_f32 v246, -v250\n"
"v_max3_f32 v250, v40, v41, v42\n"
"v_max3_f32 v251, v43, v44, v45\n"
"v_max3_f32 v250, v250, v46, v47\n"
"v_max_f32 v250, v250, v251\n"
"v_mov_b32 v251, v250\n"
"s_nop 1\n"
"v_permlane16_swap_b32 v250, v251\n"
"v_max_f32 v250, v250, v251\n"
"v_mov_b32 v251, v250\n"
"s_nop 1\n"
"v_permlane32_swap_b32 v250, v251\n"
"v_max_f32 v250, v250, v251\n"
"v_max_f32 v250, 0, v250\n"
"v_sub_f32 v40, v40, v250\n"
"v_sub_f32 v41, v41, v250\n"
"v_sub_f32 v42, v42, v250\n"
"v_sub_f32 v43, v43, v250\n"
"v_sub_f32 v44, v44, v250\n"
"v_sub_f32 v45, v45, v250\n"
"v_sub_f32 v46, v46, v250\n"
"v_sub_f32 v47, v47, v250\n"
"v_sub_f32 v124, v124, v250\n"
"v_sub_f32 v125, v125, v250\n"
"v_sub_f32 v126, v126, v250\n"
"v_sub_f32 v127, v127, v250\n"
"v_exp_f32 v247, -v250\n"
"v_max3_f32 v250, v48, v49, v50\n"
"v_max3_f32 v251, v51, v52, v53\n"
"v_max3_f32 v250, v250, v54, v55\n"
"v_max_f32 v250, v250, v251\n"
"v_mov_b32 v251, v250\n"
"s_nop 1\n"
"v_permlane16_swap_b32 v250, v251\n"
"v_max_f32 v250, v250, v251\n"
"v_mov_b32 v251, v250\n"
"s_nop 1\n"
"v_permlane32_swap_b32 v250, v251\n"
"v_max_f32 v250, v250, v251\n"
"v_max_f32 v250, 0, v250\n"
"v_sub_f32 v48, v48, v250\n"
"v_sub_f32 v49, v49, v250\n"
"v_sub_f32 v50, v50, v250\n"
"v_sub_f32 v51, v51, v250\n"
"v_sub_f32 v52, v52, v250\n"
"v_sub_f32 v53, v53, v250\n"
"v_sub_f32 v54, v54, v250\n"
"v_sub_f32 v55, v55, v250\n"
"v_sub_f32 v128, v128, v250\n"
"v_sub_f32 v129, v129, v250\n"
"v_sub_f32 v130, v130, v250\n"
"v_sub_f32 v131, v131, v250\n"
"v_exp_f32 v248, -v250\n"
"v_max3_f32 v250, v56, v57, v58\n"
"v_max3_f32 v251, v59, v60, v61\n"
"v_max3_f32 v250, v250, v62, v63\n"
"v_max_f32 v250, v250, v251\n"
"v_mov_b32 v251, v250\n"
"s_nop 1\n"
"v_permlane16_swap_b32 v250, v251\n"
"v_max_f32 v250, v250, v251\n"
"v_mov_b32 v251, v250\n"
"s_nop 1\n"
"v_permlane32_swap_b32 v250, v251\n"
"v_max_f32 v250, v250, v251\n"
"v_max_f32 v250, 0, v250\n"
"v_sub_f32 v56, v56, v250\n"
"v_sub_f32 v57, v57, v250\n"
"v_sub_f32 v58, v58, v250\n"
"v_sub_f32 v59, v59, v250\n"
"v_sub_f32 v60, v60, v250\n"
"v_sub_f32 v61, v61, v250\n"
"v_sub_f32 v62, v62, v250\n"
"v_sub_f32 v63, v63, v250\n"
"v_sub_f32 v132, v132, v250\n"
"v_sub_f32 v133, v133, v250\n"
"v_sub_f32 v134, v134, v250\n"
"v_sub_f32 v135, v135, v250\n"
"v_exp_f32 v249, -v250\n"
"s_mov_b32 s63, 1\n"
"s_nop 3\n"
"s_setpc_b64 s[76:77]\n"
"RA1_%=:\n"
"s_nop 7\n"
"v_max3_f32 v250, v64, v65, v66\n"
"v_max3_f32 v251, v67, v68, v69\n"
"v_max3_f32 v250, v250, v70, v71\n"
"v_max_f32 v250, v250, v251\n"
"v_mov_b32 v251, v250\n"
"s_nop 1\n"
"v_permlane16_swap_b32 v250, v251\n"
"v_max_f32 v250, v250, v251\n"
"v_mov_b32 v251, v250\n"
"s_nop 1\n"
"v_permlane32_swap_b32 v250, v251\n"
"v_max_f32 v250, v250, v251\n"
"v_max_f32 v250, 0, v250\n"
"v_sub_f32 v64, v64, v250\n"
"v_sub_f32 v65, v65, v250\n"
"v_sub_f32 v66, v66, v250\n"
"v_sub_f32 v67, v67, v250\n"
"v_sub_f32 v68, v68, v250\n"
"v_sub_f32 v69, v69, v250\n"
"v_sub_f32 v70, v70, v250\n"
"v_sub_f32 v71, v71, v250\n"
"v_sub_f32 v112, v112, v250\n"
"v_sub_f32 v113, v113, v250\n"
"v_sub_f32 v114, v114, v250\n"
"v_sub_f32 v115, v115, v250\n"
"v_exp_f32 v244, -v250\n"
"v_max3_f32 v250, v72, v73, v74\n"
"v_max3_f32 v251, v75, v76, v77\n"
"v_max3_f32 v250, v250, v78, v79\n"
"v_max_f32 v250, v250, v251\n"
"v_mov_b32 v251, v250\n"
"s_nop 1\n"
"v_permlane16_swap_b32 v250, v251\n"
"v_max_f32 v250, v250, v251\n"
"v_mov_b32 v251, v250\n"
"s_nop 1\n"
"v_permlane32_swap_b32 v250, v251\n"
"v_max_f32 v250, v250, v251\n"
"v_max_f32 v250, 0, v250\n"
"v_sub_f32 v72, v72, v250\n"
"v_sub_f32 v73, v73, v250\n"
"v_sub_f32 v74, v74, v250\n"
"v_sub_f32 v75, v75, v250\n"
"v_sub_f32 v76, v76, v250\n"
"v_sub_f32 v77, v77, v250\n"
"v_sub_f32 v78, v78, v250\n"
"v_sub_f32 v79, v79, v250\n"
"v_sub_f32 v116, v116, v250\n"
"v_sub_f32 v117, v117, v250\n"
"v_sub_f32 v118, v118, v250\n"
"v_sub_f32 v119, v119, v250\n"
"v_exp_f32 v245, -v250\n"
"v_max3_f32 v250, v80, v81, v82\n"
"v_max3_f32 v251, v83, v84, v85\n"
"v_max3_f32 v250, v250, v86, v87\n"
"v_max_f32 v250, v250, v251\n"
"v_mov_b32 v251, v250\n"
"s_nop 1\n"
"v_permlane16_swap_b32 v250, v251\n"
"v_max_f32 v250, v250, v251\n"
"v_mov_b32 v251, v250\n"
"s_nop 1\n"
"v_permlane32_swap_b32 v250, v251\n"
"v_max_f32 v250, v250, v251\n"
"v_max_f32 v250, 0, v250\n"
"v_sub_f32 v80, v80, v250\n"
"v_sub_f32 v81, v81, v250\n"
"v_sub_f32 v82, v82, v250\n"
"v_sub_f32 v83, v83, v250\n"
"v_sub_f32 v84, v84, v250\n"
"v_sub_f32 v85, v85, v250\n"
"v_sub_f32 v86, v86, v250\n"
"v_sub_f32 v87, v87, v250\n"
"v_sub_f32 v120, v120, v250\n"
"v_sub_f32 v121, v121, v250\n"
"v_sub_f32 v122, v122, v250\n"
"v_sub_f32 v123, v123, v250\n"
"v_exp_f32 v246, -v250\n"
"v_max3_f32 v250, v88, v89, v90\n"
"v_max3_f32 v251, v91, v92, v93\n"
"v_max3_f32 v250, v250, v94, v95\n"
"v_max_f32 v250, v250, v251\n"
"v_mov_b32 v251, v250\n"
"s_nop 1\n"
"v_permlane16_swap_b32 v250, v251\n"
"v_max_f32 v250, v250, v251\n"
"v_mov_b32 v251, v250\n"
"s_nop 1\n"
"v_permlane32_swap_b32 v250, v251\n"
"v_max_f32 v250, v250, v251\n"
"v_max_f32 v250, 0, v250\n"
"v_sub_f32 v88, v88, v250\n"
"v_sub_f32 v89, v89, v250\n"
"v_sub_f32 v90, v90, v250\n"
"v_sub_f32 v91, v91, v250\n"
"v_sub_f32 v92, v92, v250\n"
"v_sub_f32 v93, v93, v250\n"
"v_sub_f32 v94, v94, v250\n"
"v_sub_f32 v95, v95, v250\n"
"v_sub_f32 v124, v124, v250\n"
"v_sub_f32 v125, v125, v250\n"
"v_sub_f32 v126, v126, v250\n"
"v_sub_f32 v127, v127, v250\n"
"v_exp_f32 v247, -v250\n"
"v_max3_f32 v250, v96, v97, v98\n"
"v_max3_f32 v251, v99, v100, v101\n"
"v_max3_f32 v250, v250, v102, v103\n"
"v_max_f32 v250, v250, v251\n"
"v_mov_b32 v251, v250\n"
"s_nop 1\n"
"v_permlane16_swap_b32 v250, v251\n"
"v_max_f32 v250, v250, v251\n"
"v_mov_b32 v251, v250\n"
"s_nop 1\n"
"v_permlane32_swap_b32 v250, v251\n"
"v_max_f32 v250, v250, v251\n"
"v_max_f32 v250, 0, v250\n"
"v_sub_f32 v96, v96, v250\n"
"v_sub_f32 v97, v97, v250\n"
"v_sub_f32 v98, v98, v250\n"
"v_sub_f32 v99, v99, v250\n"
"v_sub_f32 v100, v100, v250\n"
"v_sub_f32 v101, v101, v250\n"
"v_sub_f32 v102, v102, v250\n"
"v_sub_f32 v103, v103, v250\n"
"v_sub_f32 v128, v128, v250\n"
"v_sub_f32 v129, v129, v250\n"
"v_sub_f32 v130, v130, v250\n"
"v_sub_f32 v131, v131, v250\n"
"v_exp_f32 v248, -v250\n"
"v_max3_f32 v250, v104, v105, v106\n"
"v_max3_f32 v251, v107, v108, v109\n"
"v_max3_f32 v250, v250, v110, v111\n"
"v_max_f32 v250, v250, v251\n"
"v_mov_b32 v251, v250\n"
"s_nop 1\n"
"v_permlane16_swap_b32 v250, v251\n"
"v_max_f32 v250, v250, v251\n"
"v_mov_b32 v251, v250\n"
"s_nop 1\n"
"v_permlane32_swap_b32 v250, v251\n"
"v_max_f32 v250, v250, v251\n"
"v_max_f32 v250, 0, v250\n"
"v_sub_f32 v104, v104, v250\n"
"v_sub_f32 v105, v105, v250\n"
"v_sub_f32 v106, v106, v250\n"
"v_sub_f32 v107, v107, v250\n"
"v_sub_f32 v108, v108, v250\n"
"v_sub_f32 v109, v109, v250\n"
"v_sub_f32 v110, v110, v250\n"
"v_sub_f32 v111, v111, v250\n"
"v_sub_f32 v132, v132, v250\n"
"v_sub_f32 v133, v133, v250\n"
"v_sub_f32 v134, v134, v250\n"
"v_sub_f32 v135, v135, v250\n"
"v_exp_f32 v249, -v250\n"
"s_mov_b32 s63, 1\n"
"s_nop 3\n"
"s_setpc_b64 s[76:77]\n"
"RB_%=:\n"
"s_nop 15\n"
"v_accvgpr_read_b32 v250, a0\n"
"v_accvgpr_read_b32 v251, a1\n"
"v_accvgpr_read_b32 v252, a2\n"
"v_accvgpr_read_b32 v253, a3\n"
"v_mul_f32 v250, v244, v250\n"
"v_mul_f32 v251, v244, v251\n"
"v_mul_f32 v252, v244, v252\n"
"v_mul_f32 v253, v244, v253\n"
"v_accvgpr_write_b32 a0, v250\n"
"v_accvgpr_write_b32 a1, v251\n"
"v_accvgpr_write_b32 a2, v252\n"
"v_accvgpr_write_b32 a3, v253\n"
"v_accvgpr_read_b32 v250, a4\n"
"v_accvgpr_read_b32 v251, a5\n"
"v_accvgpr_read_b32 v252, a6\n"
"v_accvgpr_read_b32 v253, a7\n"
"v_mul_f32 v250, v244, v250\n"
"v_mul_f32 v251, v244, v251\n"
"v_mul_f32 v252, v244, v252\n"
"v_mul_f32 v253, v244, v253\n"
"v_accvgpr_write_b32 a4, v250\n"
"v_accvgpr_write_b32 a5, v251\n"
"v_accvgpr_write_b32 a6, v252\n"
"v_accvgpr_write_b32 a7, v253\n"
"v_accvgpr_read_b32 v250, a48\n"
"v_accvgpr_read_b32 v251, a49\n"
"v_accvgpr_read_b32 v252, a50\n"
"v_accvgpr_read_b32 v253, a51\n"
"v_mul_f32 v250, v244, v250\n"
"v_mul_f32 v251, v244, v251\n"
"v_mul_f32 v252, v244, v252\n"
"v_mul_f32 v253, v244, v253\n"
"v_accvgpr_write_b32 a48, v250\n"
"v_accvgpr_write_b32 a49, v251\n"
"v_accvgpr_write_b32 a50, v252\n"
"v_accvgpr_write_b32 a51, v253\n"
"v_accvgpr_read_b32 v250, a52\n"
"v_accvgpr_read_b32 v251, a53\n"
"v_accvgpr_read_b32 v252, a54\n"
"v_accvgpr_read_b32 v253, a55\n"
"v_mul_f32 v250, v244, v250\n"
"v_mul_f32 v251, v244, v251\n"
"v_mul_f32 v252, v244, v252\n"
"v_mul_f32 v253, v244, v253\n"
"v_accvgpr_write_b32 a52, v250\n"
"v_accvgpr_write_b32 a53, v251\n"
"v_accvgpr_write_b32 a54, v252\n"
"v_accvgpr_write_b32 a55, v253\n"
"v_accvgpr_read_b32 v250, a96\n"
"v_accvgpr_read_b32 v251, a97\n"
"v_accvgpr_read_b32 v252, a98\n"
"v_accvgpr_read_b32 v253, a99\n"
"v_mul_f32 v250, v244, v250\n"
"v_mul_f32 v251, v244, v251\n"
"v_mul_f32 v252, v244, v252\n"
"v_mul_f32 v253, v244, v253\n"
"v_accvgpr_write_b32 a96, v250\n"
"v_accvgpr_write_b32 a97, v251\n"
"v_accvgpr_write_b32 a98, v252\n"
"v_accvgpr_write_b32 a99, v253\n"
"v_accvgpr_read_b32 v250, a100\n"
"v_accvgpr_read_b32 v251, a101\n"
"v_accvgpr_read_b32 v252, a102\n"
"v_accvgpr_read_b32 v253, a103\n"
"v_mul_f32 v250, v244, v250\n"
"v_mul_f32 v251, v244, v251\n"
"v_mul_f32 v252, v244, v252\n"
"v_mul_f32 v253, v244, v253\n"
"v_accvgpr_write_b32 a100, v250\n"
"v_accvgpr_write_b32 a101, v251\n"
"v_accvgpr_write_b32 a102, v252\n"
"v_accvgpr_write_b32 a103, v253\n"
"v_accvgpr_read_b32 v250, a144\n"
"v_accvgpr_read_b32 v251, a145\n"
"v_accvgpr_read_b32 v252, a146\n"
"v_accvgpr_read_b32 v253, a147\n"
"v_mul_f32 v250, v244, v250\n"
"v_mul_f32 v251, v244, v251\n"
"v_mul_f32 v252, v244, v252\n"
"v_mul_f32 v253, v244, v253\n"
"v_accvgpr_write_b32 a144, v250\n"
"v_accvgpr_write_b32 a145, v251\n"
"v_accvgpr_write_b32 a146, v252\n"
"v_accvgpr_write_b32 a147, v253\n"
"v_accvgpr_read_b32 v250, a148\n"
"v_accvgpr_read_b32 v251, a149\n"
"v_accvgpr_read_b32 v252, a150\n"
"v_accvgpr_read_b32 v253, a151\n"
"v_mul_f32 v250, v244, v250\n"
"v_mul_f32 v251, v244, v251\n"
"v_mul_f32 v252, v244, v252\n"
"v_mul_f32 v253, v244, v253\n"
"v_accvgpr_write_b32 a148, v250\n"
"v_accvgpr_write_b32 a149, v251\n"
"v_accvgpr_write_b32 a150, v252\n"
"v_accvgpr_write_b32 a151, v253\n"
"v_accvgpr_read_b32 v250, a192\n"
"v_accvgpr_read_b32 v251, a193\n"
"v_accvgpr_read_b32 v252, a194\n"
"v_accvgpr_read_b32 v253, a195\n"
"v_mul_f32 v250, v244, v250\n"
"v_mul_f32 v251, v244, v251\n"
"v_mul_f32 v252, v244, v252\n"
"v_mul_f32 v253, v244, v253\n"
"v_accvgpr_write_b32 a192, v250\n"
"v_accvgpr_write_b32 a193, v251\n"
"v_accvgpr_write_b32 a194, v252\n"
"v_accvgpr_write_b32 a195, v253\n"
"v_accvgpr_read_b32 v250, a8\n"
"v_accvgpr_read_b32 v251, a9\n"
"v_accvgpr_read_b32 v252, a10\n"
"v_accvgpr_read_b32 v253, a11\n"
"v_mul_f32 v250, v245, v250\n"
"v_mul_f32 v251, v245, v251\n"
"v_mul_f32 v252, v245, v252\n"
"v_mul_f32 v253, v245, v253\n"
"v_accvgpr_write_b32 a8, v250\n"
"v_accvgpr_write_b32 a9, v251\n"
"v_accvgpr_write_b32 a10, v252\n"
"v_accvgpr_write_b32 a11, v253\n"
"v_accvgpr_read_b32 v250, a12\n"
"v_accvgpr_read_b32 v251, a13\n"
"v_accvgpr_read_b32 v252, a14\n"
"v_accvgpr_read_b32 v253, a15\n"
"v_mul_f32 v250, v245, v250\n"
"v_mul_f32 v251, v245, v251\n"
"v_mul_f32 v252, v245, v252\n"
"v_mul_f32 v253, v245, v253\n"
"v_accvgpr_write_b32 a12, v250\n"
"v_accvgpr_write_b32 a13, v251\n"
"v_accvgpr_write_b32 a14, v252\n"
"v_accvgpr_write_b32 a15, v253\n"
"v_accvgpr_read_b32 v250, a56\n"
"v_accvgpr_read_b32 v251, a57\n"
"v_accvgpr_read_b32 v252, a58\n"
"v_accvgpr_read_b32 v253, a59\n"
"v_mul_f32 v250, v245, v250\n"
"v_mul_f32 v251, v245, v251\n"
"v_mul_f32 v252, v245, v252\n"
"v_mul_f32 v253, v245, v253\n"
"v_accvgpr_write_b32 a56, v250\n"
"v_accvgpr_write_b32 a57, v251\n"
"v_accvgpr_write_b32 a58, v252\n"
"v_accvgpr_write_b32 a59, v253\n"
"v_accvgpr_read_b32 v250, a60\n"
"v_accvgpr_read_b32 v251, a61\n"
"v_accvgpr_read_b32 v252, a62\n"
"v_accvgpr_read_b32 v253, a63\n"
"v_mul_f32 v250, v245, v250\n"
"v_mul_f32 v251, v245, v251\n"
"v_mul_f32 v252, v245, v252\n"
"v_mul_f32 v253, v245, v253\n"
"v_accvgpr_write_b32 a60, v250\n"
"v_accvgpr_write_b32 a61, v251\n"
"v_accvgpr_write_b32 a62, v252\n"
"v_accvgpr_write_b32 a63, v253\n"
"v_accvgpr_read_b32 v250, a104\n"
"v_accvgpr_read_b32 v251, a105\n"
"v_accvgpr_read_b32 v252, a106\n"
"v_accvgpr_read_b32 v253, a107\n"
"v_mul_f32 v250, v245, v250\n"
"v_mul_f32 v251, v245, v251\n"
"v_mul_f32 v252, v245, v252\n"
"v_mul_f32 v253, v245, v253\n"
"v_accvgpr_write_b32 a104, v250\n"
"v_accvgpr_write_b32 a105, v251\n"
"v_accvgpr_write_b32 a106, v252\n"
"v_accvgpr_write_b32 a107, v253\n"
"v_accvgpr_read_b32 v250, a108\n"
"v_accvgpr_read_b32 v251, a109\n"
"v_accvgpr_read_b32 v252, a110\n"
"v_accvgpr_read_b32 v253, a111\n"
"v_mul_f32 v250, v245, v250\n"
"v_mul_f32 v251, v245, v251\n"
"v_mul_f32 v252, v245, v252\n"
"v_mul_f32 v253, v245, v253\n"
"v_accvgpr_write_b32 a108, v250\n"
"v_accvgpr_write_b32 a109, v251\n"
"v_accvgpr_write_b32 a110, v252\n"
"v_accvgpr_write_b32 a111, v253\n"
"v_accvgpr_read_b32 v250, a152\n"
"v_accvgpr_read_b32 v251, a153\n"
"v_accvgpr_read_b32 v252, a154\n"
"v_accvgpr_read_b32 v253, a155\n"
"v_mul_f32 v250, v245, v250\n"
"v_mul_f32 v251, v245, v251\n"
"v_mul_f32 v252, v245, v252\n"
"v_mul_f32 v253, v245, v253\n"
"v_accvgpr_write_b32 a152, v250\n"
"v_accvgpr_write_b32 a153, v251\n"
"v_accvgpr_write_b32 a154, v252\n"
"v_accvgpr_write_b32 a155, v253\n"
"v_accvgpr_read_b32 v250, a156\n"
"v_accvgpr_read_b32 v251, a157\n"
"v_accvgpr_read_b32 v252, a158\n"
"v_accvgpr_read_b32 v253, a159\n"
"v_mul_f32 v250, v245, v250\n"
"v_mul_f32 v251, v245, v251\n"
"v_mul_f32 v252, v245, v252\n"
"v_mul_f32 v253, v245, v253\n"
"v_accvgpr_write_b32 a156, v250\n"
"v_accvgpr_write_b32 a157, v251\n"
"v_accvgpr_write_b32 a158, v252\n"
"v_accvgpr_write_b32 a159, v253\n"
"v_accvgpr_read_b32 v250, a196\n"
"v_accvgpr_read_b32 v251, a197\n"
"v_accvgpr_read_b32 v252, a198\n"
"v_accvgpr_read_b32 v253, a199\n"
"v_mul_f32 v250, v245, v250\n"
"v_mul_f32 v251, v245, v251\n"
"v_mul_f32 v252, v245, v252\n"
"v_mul_f32 v253, v245, v253\n"
"v_accvgpr_write_b32 a196, v250\n"
"v_accvgpr_write_b32 a197, v251\n"
"v_accvgpr_write_b32 a198, v252\n"
"v_accvgpr_write_b32 a199, v253\n"
"v_accvgpr_read_b32 v250, a16\n"
"v_accvgpr_read_b32 v251, a17\n"
"v_accvgpr_read_b32 v252, a18\n"
"v_accvgpr_read_b32 v253, a19\n"
"v_mul_f32 v250, v246, v250\n"
"v_mul_f32 v251, v246, v251\n"
"v_mul_f32 v252, v246, v252\n"
"v_mul_f32 v253, v246, v253\n"
"v_accvgpr_write_b32 a16, v250\n"
"v_accvgpr_write_b32 a17, v251\n"
"v_accvgpr_write_b32 a18, v252\n"
"v_accvgpr_write_b32 a19, v253\n"
"v_accvgpr_read_b32 v250, a20\n"
"v_accvgpr_read_b32 v251, a21\n"
"v_accvgpr_read_b32 v252, a22\n"
"v_accvgpr_read_b32 v253, a23\n"
"v_mul_f32 v250, v246, v250\n"
"v_mul_f32 v251, v246, v251\n"
"v_mul_f32 v252, v246, v252\n"
"v_mul_f32 v253, v246, v253\n"
"v_accvgpr_write_b32 a20, v250\n"
"v_accvgpr_write_b32 a21, v251\n"
"v_accvgpr_write_b32 a22, v252\n"
"v_accvgpr_write_b32 a23, v253\n"
"v_accvgpr_read_b32 v250, a64\n"
"v_accvgpr_read_b32 v251, a65\n"
"v_accvgpr_read_b32 v252, a66\n"
"v_accvgpr_read_b32 v253, a67\n"
"v_mul_f32 v250, v246, v250\n"
"v_mul_f32 v251, v246, v251\n"
"v_mul_f32 v252, v246, v252\n"
"v_mul_f32 v253, v246, v253\n"
"v_accvgpr_write_b32 a64, v250\n"
"v_accvgpr_write_b32 a65, v251\n"
"v_accvgpr_write_b32 a66, v252\n"
"v_accvgpr_write_b32 a67, v253\n"
"v_accvgpr_read_b32 v250, a68\n"
"v_accvgpr_read_b32 v251, a69\n"
"v_accvgpr_read_b32 v252, a70\n"
"v_accvgpr_read_b32 v253, a71\n"
"v_mul_f32 v250, v246, v250\n"
"v_mul_f32 v251, v246, v251\n"
"v_mul_f32 v252, v246, v252\n"
"v_mul_f32 v253, v246, v253\n"
"v_accvgpr_write_b32 a68, v250\n"
"v_accvgpr_write_b32 a69, v251\n"
"v_accvgpr_write_b32 a70, v252\n"
"v_accvgpr_write_b32 a71, v253\n"
"v_accvgpr_read_b32 v250, a112\n"
"v_accvgpr_read_b32 v251, a113\n"
"v_accvgpr_read_b32 v252, a114\n"
"v_accvgpr_read_b32 v253, a115\n"
"v_mul_f32 v250, v246, v250\n"
"v_mul_f32 v251, v246, v251\n"
"v_mul_f32 v252, v246, v252\n"
"v_mul_f32 v253, v246, v253\n"
"v_accvgpr_write_b32 a112, v250\n"
"v_accvgpr_write_b32 a113, v251\n"
"v_accvgpr_write_b32 a114, v252\n"
"v_accvgpr_write_b32 a115, v253\n"
"v_accvgpr_read_b32 v250, a116\n"
"v_accvgpr_read_b32 v251, a117\n"
"v_accvgpr_read_b32 v252, a118\n"
"v_accvgpr_read_b32 v253, a119\n"
"v_mul_f32 v250, v246, v250\n"
"v_mul_f32 v251, v246, v251\n"
"v_mul_f32 v252, v246, v252\n"
"v_mul_f32 v253, v246, v253\n"
"v_accvgpr_write_b32 a116, v250\n"
"v_accvgpr_write_b32 a117, v251\n"
"v_accvgpr_write_b32 a118, v252\n"
"v_accvgpr_write_b32 a119, v253\n"
"v_accvgpr_read_b32 v250, a160\n"
"v_accvgpr_read_b32 v251, a161\n"
"v_accvgpr_read_b32 v252, a162\n"
"v_accvgpr_read_b32 v253, a163\n"
"v_mul_f32 v250, v246, v250\n"
"v_mul_f32 v251, v246, v251\n"
"v_mul_f32 v252, v246, v252\n"
"v_mul_f32 v253, v246, v253\n"
"v_accvgpr_write_b32 a160, v250\n"
"v_accvgpr_write_b32 a161, v251\n"
"v_accvgpr_write_b32 a162, v252\n"
"v_accvgpr_write_b32 a163, v253\n"
"v_accvgpr_read_b32 v250, a164\n"
"v_accvgpr_read_b32 v251, a165\n"
"v_accvgpr_read_b32 v252, a166\n"
"v_accvgpr_read_b32 v253, a167\n"
"v_mul_f32 v250, v246, v250\n"
"v_mul_f32 v251, v246, v251\n"
"v_mul_f32 v252, v246, v252\n"
"v_mul_f32 v253, v246, v253\n"
"v_accvgpr_write_b32 a164, v250\n"
"v_accvgpr_write_b32 a165, v251\n"
"v_accvgpr_write_b32 a166, v252\n"
"v_accvgpr_write_b32 a167, v253\n"
"v_accvgpr_read_b32 v250, a200\n"
"v_accvgpr_read_b32 v251, a201\n"
"v_accvgpr_read_b32 v252, a202\n"
"v_accvgpr_read_b32 v253, a203\n"
"v_mul_f32 v250, v246, v250\n"
"v_mul_f32 v251, v246, v251\n"
"v_mul_f32 v252, v246, v252\n"
"v_mul_f32 v253, v246, v253\n"
"v_accvgpr_write_b32 a200, v250\n"
"v_accvgpr_write_b32 a201, v251\n"
"v_accvgpr_write_b32 a202, v252\n"
"v_accvgpr_write_b32 a203, v253\n"
"v_accvgpr_read_b32 v250, a24\n"
"v_accvgpr_read_b32 v251, a25\n"
"v_accvgpr_read_b32 v252, a26\n"
"v_accvgpr_read_b32 v253, a27\n"
"v_mul_f32 v250, v247, v250\n"
"v_mul_f32 v251, v247, v251\n"
"v_mul_f32 v252, v247, v252\n"
"v_mul_f32 v253, v247, v253\n"
"v_accvgpr_write_b32 a24, v250\n"
"v_accvgpr_write_b32 a25, v251\n"
"v_accvgpr_write_b32 a26, v252\n"
"v_accvgpr_write_b32 a27, v253\n"
"v_accvgpr_read_b32 v250, a28\n"
"v_accvgpr_read_b32 v251, a29\n"
"v_accvgpr_read_b32 v252, a30\n"
"v_accvgpr_read_b32 v253, a31\n"
"v_mul_f32 v250, v247, v250\n"
"v_mul_f32 v251, v247, v251\n"
"v_mul_f32 v252, v247, v252\n"
"v_mul_f32 v253, v247, v253\n"
"v_accvgpr_write_b32 a28, v250\n"
"v_accvgpr_write_b32 a29, v251\n"
"v_accvgpr_write_b32 a30, v252\n"
"v_accvgpr_write_b32 a31, v253\n"
"v_accvgpr_read_b32 v250, a72\n"
"v_accvgpr_read_b32 v251, a73\n"
"v_accvgpr_read_b32 v252, a74\n"
"v_accvgpr_read_b32 v253, a75\n"
"v_mul_f32 v250, v247, v250\n"
"v_mul_f32 v251, v247, v251\n"
"v_mul_f32 v252, v247, v252\n"
"v_mul_f32 v253, v247, v253\n"
"v_accvgpr_write_b32 a72, v250\n"
"v_accvgpr_write_b32 a73, v251\n"
"v_accvgpr_write_b32 a74, v252\n"
"v_accvgpr_write_b32 a75, v253\n"
"v_accvgpr_read_b32 v250, a76\n"
"v_accvgpr_read_b32 v251, a77\n"
"v_accvgpr_read_b32 v252, a78\n"
"v_accvgpr_read_b32 v253, a79\n"
"v_mul_f32 v250, v247, v250\n"
"v_mul_f32 v251, v247, v251\n"
"v_mul_f32 v252, v247, v252\n"
"v_mul_f32 v253, v247, v253\n"
"v_accvgpr_write_b32 a76, v250\n"
"v_accvgpr_write_b32 a77, v251\n"
"v_accvgpr_write_b32 a78, v252\n"
"v_accvgpr_write_b32 a79, v253\n"
"v_accvgpr_read_b32 v250, a120\n"
"v_accvgpr_read_b32 v251, a121\n"
"v_accvgpr_read_b32 v252, a122\n"
"v_accvgpr_read_b32 v253, a123\n"
"v_mul_f32 v250, v247, v250\n"
"v_mul_f32 v251, v247, v251\n"
"v_mul_f32 v252, v247, v252\n"
"v_mul_f32 v253, v247, v253\n"
"v_accvgpr_write_b32 a120, v250\n"
"v_accvgpr_write_b32 a121, v251\n"
"v_accvgpr_write_b32 a122, v252\n"
"v_accvgpr_write_b32 a123, v253\n"
"v_accvgpr_read_b32 v250, a124\n"
"v_accvgpr_read_b32 v251, a125\n"
"v_accvgpr_read_b32 v252, a126\n"
"v_accvgpr_read_b32 v253, a127\n"
"v_mul_f32 v250, v247, v250\n"
"v_mul_f32 v251, v247, v251\n"
"v_mul_f32 v252, v247, v252\n"
"v_mul_f32 v253, v247, v253\n"
"v_accvgpr_write_b32 a124, v250\n"
"v_accvgpr_write_b32 a125, v251\n"
"v_accvgpr_write_b32 a126, v252\n"
"v_accvgpr_write_b32 a127, v253\n"
"v_accvgpr_read_b32 v250, a168\n"
"v_accvgpr_read_b32 v251, a169\n"
"v_accvgpr_read_b32 v252, a170\n"
"v_accvgpr_read_b32 v253, a171\n"
"v_mul_f32 v250, v247, v250\n"
"v_mul_f32 v251, v247, v251\n"
"v_mul_f32 v252, v247, v252\n"
"v_mul_f32 v253, v247, v253\n"
"v_accvgpr_write_b32 a168, v250\n"
"v_accvgpr_write_b32 a169, v251\n"
"v_accvgpr_write_b32 a170, v252\n"
"v_accvgpr_write_b32 a171, v253\n"
"v_accvgpr_read_b32 v250, a172\n"
"v_accvgpr_read_b32 v251, a173\n"
"v_accvgpr_read_b32 v252, a174\n"
"v_accvgpr_read_b32 v253, a175\n"
"v_mul_f32 v250, v247, v250\n"
"v_mul_f32 v251, v247, v251\n"
"v_mul_f32 v252, v247, v252\n"
"v_mul_f32 v253, v247, v253\n"
"v_accvgpr_write_b32 a172, v250\n"
"v_accvgpr_write_b32 a173, v251\n"
"v_accvgpr_write_b32 a174, v252\n"
"v_accvgpr_write_b32 a175, v253\n"
"v_accvgpr_read_b32 v250, a204\n"
"v_accvgpr_read_b32 v251, a205\n"
"v_accvgpr_read_b32 v252, a206\n"
"v_accvgpr_read_b32 v253, a207\n"
"v_mul_f32 v250, v247, v250\n"
"v_mul_f32 v251, v247, v251\n"
"v_mul_f32 v252, v247, v252\n"
"v_mul_f32 v253, v247, v253\n"
"v_accvgpr_write_b32 a204, v250\n"
"v_accvgpr_write_b32 a205, v251\n"
"v_accvgpr_write_b32 a206, v252\n"
"v_accvgpr_write_b32 a207, v253\n"
"v_accvgpr_read_b32 v250, a32\n"
"v_accvgpr_read_b32 v251, a33\n"
"v_accvgpr_read_b32 v252, a34\n"
"v_accvgpr_read_b32 v253, a35\n"
"v_mul_f32 v250, v248, v250\n"
"v_mul_f32 v251, v248, v251\n"
"v_mul_f32 v252, v248, v252\n"
"v_mul_f32 v253, v248, v253\n"
"v_accvgpr_write_b32 a32, v250\n"
"v_accvgpr_write_b32 a33, v251\n"
"v_accvgpr_write_b32 a34, v252\n"
"v_accvgpr_write_b32 a35, v253\n"
"v_accvgpr_read_b32 v250, a36\n"
"v_accvgpr_read_b32 v251, a37\n"
"v_accvgpr_read_b32 v252, a38\n"
"v_accvgpr_read_b32 v253, a39\n"
"v_mul_f32 v250, v248, v250\n"
"v_mul_f32 v251, v248, v251\n"
"v_mul_f32 v252, v248, v252\n"
"v_mul_f32 v253, v248, v253\n"
"v_accvgpr_write_b32 a36, v250\n"
"v_accvgpr_write_b32 a37, v251\n"
"v_accvgpr_write_b32 a38, v252\n"
"v_accvgpr_write_b32 a39, v253\n"
"v_accvgpr_read_b32 v250, a80\n"
"v_accvgpr_read_b32 v251, a81\n"
"v_accvgpr_read_b32 v252, a82\n"
"v_accvgpr_read_b32 v253, a83\n"
"v_mul_f32 v250, v248, v250\n"
"v_mul_f32 v251, v248, v251\n"
"v_mul_f32 v252, v248, v252\n"
"v_mul_f32 v253, v248, v253\n"
"v_accvgpr_write_b32 a80, v250\n"
"v_accvgpr_write_b32 a81, v251\n"
"v_accvgpr_write_b32 a82, v252\n"
"v_accvgpr_write_b32 a83, v253\n"
"v_accvgpr_read_b32 v250, a84\n"
"v_accvgpr_read_b32 v251, a85\n"
"v_accvgpr_read_b32 v252, a86\n"
"v_accvgpr_read_b32 v253, a87\n"
"v_mul_f32 v250, v248, v250\n"
"v_mul_f32 v251, v248, v251\n"
"v_mul_f32 v252, v248, v252\n"
"v_mul_f32 v253, v248, v253\n"
"v_accvgpr_write_b32 a84, v250\n"
"v_accvgpr_write_b32 a85, v251\n"
"v_accvgpr_write_b32 a86, v252\n"
"v_accvgpr_write_b32 a87, v253\n"
"v_accvgpr_read_b32 v250, a128\n"
"v_accvgpr_read_b32 v251, a129\n"
"v_accvgpr_read_b32 v252, a130\n"
"v_accvgpr_read_b32 v253, a131\n"
"v_mul_f32 v250, v248, v250\n"
"v_mul_f32 v251, v248, v251\n"
"v_mul_f32 v252, v248, v252\n"
"v_mul_f32 v253, v248, v253\n"
"v_accvgpr_write_b32 a128, v250\n"
"v_accvgpr_write_b32 a129, v251\n"
"v_accvgpr_write_b32 a130, v252\n"
"v_accvgpr_write_b32 a131, v253\n"
"v_accvgpr_read_b32 v250, a132\n"
"v_accvgpr_read_b32 v251, a133\n"
"v_accvgpr_read_b32 v252, a134\n"
"v_accvgpr_read_b32 v253, a135\n"
"v_mul_f32 v250, v248, v250\n"
"v_mul_f32 v251, v248, v251\n"
"v_mul_f32 v252, v248, v252\n"
"v_mul_f32 v253, v248, v253\n"
"v_accvgpr_write_b32 a132, v250\n"
"v_accvgpr_write_b32 a133, v251\n"
"v_accvgpr_write_b32 a134, v252\n"
"v_accvgpr_write_b32 a135, v253\n"
"v_accvgpr_read_b32 v250, a176\n"
"v_accvgpr_read_b32 v251, a177\n"
"v_accvgpr_read_b32 v252, a178\n"
"v_accvgpr_read_b32 v253, a179\n"
"v_mul_f32 v250, v248, v250\n"
"v_mul_f32 v251, v248, v251\n"
"v_mul_f32 v252, v248, v252\n"
"v_mul_f32 v253, v248, v253\n"
"v_accvgpr_write_b32 a176, v250\n"
"v_accvgpr_write_b32 a177, v251\n"
"v_accvgpr_write_b32 a178, v252\n"
"v_accvgpr_write_b32 a179, v253\n"
"v_accvgpr_read_b32 v250, a180\n"
"v_accvgpr_read_b32 v251, a181\n"
"v_accvgpr_read_b32 v252, a182\n"
"v_accvgpr_read_b32 v253, a183\n"
"v_mul_f32 v250, v248, v250\n"
"v_mul_f32 v251, v248, v251\n"
"v_mul_f32 v252, v248, v252\n"
"v_mul_f32 v253, v248, v253\n"
"v_accvgpr_write_b32 a180, v250\n"
"v_accvgpr_write_b32 a181, v251\n"
"v_accvgpr_write_b32 a182, v252\n"
"v_accvgpr_write_b32 a183, v253\n"
"v_accvgpr_read_b32 v250, a208\n"
"v_accvgpr_read_b32 v251, a209\n"
"v_accvgpr_read_b32 v252, a210\n"
"v_accvgpr_read_b32 v253, a211\n"
"v_mul_f32 v250, v248, v250\n"
"v_mul_f32 v251, v248, v251\n"
"v_mul_f32 v252, v248, v252\n"
"v_mul_f32 v253, v248, v253\n"
"v_accvgpr_write_b32 a208, v250\n"
"v_accvgpr_write_b32 a209, v251\n"
"v_accvgpr_write_b32 a210, v252\n"
"v_accvgpr_write_b32 a211, v253\n"
"v_accvgpr_read_b32 v250, a40\n"
"v_accvgpr_read_b32 v251, a41\n"
"v_accvgpr_read_b32 v252, a42\n"
"v_accvgpr_read_b32 v253, a43\n"
"v_mul_f32 v250, v249, v250\n"
"v_mul_f32 v251, v249, v251\n"
"v_mul_f32 v252, v249, v252\n"
"v_mul_f32 v253, v249, v253\n"
"v_accvgpr_write_b32 a40, v250\n"
"v_accvgpr_write_b32 a41, v251\n"
"v_accvgpr_write_b32 a42, v252\n"
"v_accvgpr_write_b32 a43, v253\n"
"v_accvgpr_read_b32 v250, a44\n"
"v_accvgpr_read_b32 v251, a45\n"
"v_accvgpr_read_b32 v252, a46\n"
"v_accvgpr_read_b32 v253, a47\n"
"v_mul_f32 v250, v249, v250\n"
"v_mul_f32 v251, v249, v251\n"
"v_mul_f32 v252, v249, v252\n"
"v_mul_f32 v253, v249, v253\n"
"v_accvgpr_write_b32 a44, v250\n"
"v_accvgpr_write_b32 a45, v251\n"
"v_accvgpr_write_b32 a46, v252\n"
"v_accvgpr_write_b32 a47, v253\n"
"v_accvgpr_read_b32 v250, a88\n"
"v_accvgpr_read_b32 v251, a89\n"
"v_accvgpr_read_b32 v252, a90\n"
"v_accvgpr_read_b32 v253, a91\n"
"v_mul_f32 v250, v249, v250\n"
"v_mul_f32 v251, v249, v251\n"
"v_mul_f32 v252, v249, v252\n"
"v_mul_f32 v253, v249, v253\n"
"v_accvgpr_write_b32 a88, v250\n"
"v_accvgpr_write_b32 a89, v251\n"
"v_accvgpr_write_b32 a90, v252\n"
"v_accvgpr_write_b32 a91, v253\n"
"v_accvgpr_read_b32 v250, a92\n"
"v_accvgpr_read_b32 v251, a93\n"
"v_accvgpr_read_b32 v252, a94\n"
"v_accvgpr_read_b32 v253, a95\n"
"v_mul_f32 v250, v249, v250\n"
"v_mul_f32 v251, v249, v251\n"
"v_mul_f32 v252, v249, v252\n"
"v_mul_f32 v253, v249, v253\n"
"v_accvgpr_write_b32 a92, v250\n"
"v_accvgpr_write_b32 a93, v251\n"
"v_accvgpr_write_b32 a94, v252\n"
"v_accvgpr_write_b32 a95, v253\n"
"v_accvgpr_read_b32 v250, a136\n"
"v_accvgpr_read_b32 v251, a137\n"
"v_accvgpr_read_b32 v252, a138\n"
"v_accvgpr_read_b32 v253, a139\n"
"v_mul_f32 v250, v249, v250\n"
"v_mul_f32 v251, v249, v251\n"
"v_mul_f32 v252, v249, v252\n"
"v_mul_f32 v253, v249, v253\n"
"v_accvgpr_write_b32 a136, v250\n"
"v_accvgpr_write_b32 a137, v251\n"
"v_accvgpr_write_b32 a138, v252\n"
"v_accvgpr_write_b32 a139, v253\n"
"v_accvgpr_read_b32 v250, a140\n"
"v_accvgpr_read_b32 v251, a141\n"
"v_accvgpr_read_b32 v252, a142\n"
"v_accvgpr_read_b32 v253, a143\n"
"v_mul_f32 v250, v249, v250\n"
"v_mul_f32 v251, v249, v251\n"
"v_mul_f32 v252, v249, v252\n"
"v_mul_f32 v253, v249, v253\n"
"v_accvgpr_write_b32 a140, v250\n"
"v_accvgpr_write_b32 a141, v251\n"
"v_accvgpr_write_b32 a142, v252\n"
"v_accvgpr_write_b32 a143, v253\n"
"v_accvgpr_read_b32 v250, a184\n"
"v_accvgpr_read_b32 v251, a185\n"
"v_accvgpr_read_b32 v252, a186\n"
"v_accvgpr_read_b32 v253, a187\n"
"v_mul_f32 v250, v249, v250\n"
"v_mul_f32 v251, v249, v251\n"
"v_mul_f32 v252, v249, v252\n"
"v_mul_f32 v253, v249, v253\n"
"v_accvgpr_write_b32 a184, v250\n"
"v_accvgpr_write_b32 a185, v251\n"
"v_accvgpr_write_b32 a186, v252\n"
"v_accvgpr_write_b32 a187, v253\n"
"v_accvgpr_read_b32 v250, a188\n"
"v_accvgpr_read_b32 v251, a189\n"
"v_accvgpr_read_b32 v252, a190\n"
"v_accvgpr_read_b32 v253, a191\n"
"v_mul_f32 v250, v249, v250\n"
"v_mul_f32 v251, v249, v251\n"
"v_mul_f32 v252, v249, v252\n"
"v_mul_f32 v253, v249, v253\n"
"v_accvgpr_write_b32 a188, v250\n"
"v_accvgpr_write_b32 a189, v251\n"
"v_accvgpr_write_b32 a190, v252\n"
"v_accvgpr_write_b32 a191, v253\n"
"v_accvgpr_read_b32 v250, a212\n"
"v_accvgpr_read_b32 v251, a213\n"
"v_accvgpr_read_b32 v252, a214\n"
"v_accvgpr_read_b32 v253, a215\n"
"v_mul_f32 v250, v249, v250\n"
"v_mul_f32 v251, v249, v251\n"
"v_mul_f32 v252, v249, v252\n"
"v_mul_f32 v253, v249, v253\n"
"v_accvgpr_write_b32 a212, v250\n"
"v_accvgpr_write_b32 a213, v251\n"
"v_accvgpr_write_b32 a214, v252\n"
"v_accvgpr_write_b32 a215, v253\n"
"s_mov_b32 s63, 0\n"
"s_nop 3\n"
"s_setpc_b64 s[76:77]\n"
"END_%=:\n"
  :: [k] "s"(k), [v] "s"(v), [x] "s"(x), [w] "s"(w), [o] "s"(o), [wo] "s"(wo), [wt] "s"(wt), [hp] "s"(hp), [b] "s"(b), [lw] "s"(lw), [rot] "s"(rot), [dh] "s"(dh), [jr] "s"(jr), [lds] "s"(ldsb), [tid] "v"(tid)
  : "v1","v2","v3","v4","v5","v6","v7","v8","v9","v10","v11","v12","v13","v14","v15","v16","v17","v18","v19","v20","v21","v22","v23","v24","v25","v26","v27","v28","v29","v30","v31","v32","v33","v34","v35","v36","v37","v38","v39","v40","v41","v42","v43","v44","v45","v46","v47","v48","v49","v50","v51","v52","v53","v54","v55","v56","v57","v58","v59","v60","v61","v62","v63","v64","v65","v66","v67","v68","v69","v70","v71","v72","v73","v74","v75","v76","v77","v78","v79","v80","v81","v82","v83","v84","v85","v86","v87","v88","v89","v90","v91","v92","v93","v94","v95","v96","v97","v98","v99","v100","v101","v102","v103","v104","v105","v106","v107","v108","v109","v110","v111","v112","v113","v114","v115","v116","v117","v118","v119","v120","v121","v122","v123","v124","v125","v126","v127","v128","v129","v130","v131","v132","v133","v134","v135","v136","v137","v138","v139","v140","v141","v142","v143","v144","v145","v146","v147","v148","v149","v150","v151","v152","v153","v154","v155","v156","v157","v158","v159","v160","v161","v162","v163","v164","v165","v166","v167","v168","v169","v170","v171","v172","v173","v174","v175","v176","v177","v178","v179","v180","v181","v182","v183","v184","v185","v186","v187","v188","v189","v190","v191","v192","v193","v194","v195","v196","v197","v198","v199","v200","v201","v202","v203","v204","v205","v206","v207","v208","v209","v210","v211","v212","v213","v214","v215","v216","v217","v218","v219","v220","v221","v222","v223","v224","v225","v226","v227","v228","v229","v230","v231","v232","v233","v234","v235","v236","v237","v238","v239","v240","v241","v242","v243","v244","v245","v246","v247","v248","v249","v250","v251","v252","v253","v254","v255","a0","a1","a2","a3","a4","a5","a6","a7","a8","a9","a10","a11","a12","a13","a14","a15","a16","a17","a18","a19","a20","a21","a22","a23","a24","a25","a26","a27","a28","a29","a30","a31","a32","a33","a34","a35","a36","a37","a38","a39","a40","a41","a42","a43","a44","a45","a46","a47","a48","a49","a50","a51","a52","a53","a54","a55","a56","a57","a58","a59","a60","a61","a62","a63","a64","a65","a66","a67","a68","a69","a70","a71","a72","a73","a74","a75","a76","a77","a78","a79","a80","a81","a82","a83","a84","a85","a86","a87","a88","a89","a90","a91","a92","a93","a94","a95","a96","a97","a98","a99","a100","a101","a102","a103","a104","a105","a106","a107","a108","a109","a110","a111","a112","a113","a114","a115","a116","a117","a118","a119","a120","a121","a122","a123","a124","a125","a126","a127","a128","a129","a130","a131","a132","a133","a134","a135","a136","a137","a138","a139","a140","a141","a142","a143","a144","a145","a146","a147","a148","a149","a150","a151","a152","a153","a154","a155","a156","a157","a158","a159","a160","a161","a162","a163","a164","a165","a166","a167","a168","a169","a170","a171","a172","a173","a174","a175","a176","a177","a178","a179","a180","a181","a182","a183","a184","a185","a186","a187","a188","a189","a190","a191","a192","a193","a194","a195","a196","a197","a198","a199","a200","a201","a202","a203","a204","a205","a206","a207","a208","a209","a210","a211","a212","a213","a214","a215","a216","a217","a218","a219","a220","a221","a222","a223","a224","a225","a226","a227","a228","a229","a230","a231","a232","a233","a234","a235","a236","a237","a238","a239","a240","a241","a242","a243","a244","a245","a246","a247","a248","a249","a250","a251","a252","a253","a254","a255","s36","s37","s38","s39","s40","s41","s42","s43","s44","s45","s46","s47","s48","s49","s50","s51","s52","s53","s54","s55","s56","s57","s58","s59","s60","s61","s62","s63","s64","s65","s66","s67","s68","s69","s70","s71","s72","s73","s74","s75","s76","s77","s78","s79","s80","s81","s82","s83","s84","s85","s86","s87","s88","s89","s90","s91","s92","s93","s94","s95","s96","s97","s98","s99","vcc","scc","memory");
}

__global__ __launch_bounds__(256, 1) void k2_oproj(char* __restrict__ ws, u16* __restrict__ outp) {
  extern __shared__ __attribute__((aligned(16))) char lds[];
  const unsigned bid = blockIdx.x, xc = bid & 7u, i = bid >> 3, br = xc >> 2, kh = xc & 1u, b = ((xc >> 1) & 1u) * 8u + (i >> 2), nt = i & 3u;
  const unsigned pair = (br * 16u + b) * 4u + nt;
  const char* o = ws + WS_O + (size_t)br * 25165824u + (size_t)(b * 512u + kh * 64u) * 3072u;
  const char* wt = ws + WS_WT + (size_t)br * 4718592u + (size_t)(kh * 64u * 36u + nt * 9u) * 1024u;
  char* part = ws + WS_P + (size_t)pair * 131072u; char* flag = ws + WS_F + pair * 256u;
  char* out = (char*)outp + (size_t)b * 192u * 2304u + br * 1152u + nt * 288u;
  const unsigned ldsb = (unsigned)(size_t)lds; const unsigned tid = threadIdx.x;
  asm volatile(
"v_lshrrev_b32 v8, 6, %[tid]\n"
"v_and_b32 v9, 63, %[tid]\n"
"v_readfirstlane_b32 s66, v8\n"
"v_lshlrev_b32 v1, 4, v9\n"
"s_mov_b64 s[40:41], %[wt]\n"
"s_and_b32 s41, s41, 0xffff\n"
"s_mov_b32 s42, 2331648\n"
"s_mov_b32 s43, 0x20000\n"
"s_mov_b64 s[36:37], %[o]\n"
"s_mul_i32 s76, s66, 393216\n"
"s_add_u32 s36, s36, s76\n"
"s_addc_u32 s37, s37, 0\n"
"s_and_b32 s37, s37, 0xffff\n"
"s_mov_b32 s38, 196608\n"
"s_mov_b32 s39, 0x20000\n"
"s_mul_i32 s57, s66, 36864\n"
"s_mov_b32 s56, 0\n"
"s_mul_i32 s60, %[kh], 5120\n"
"s_sub_u32 s58, 5120, s60\n"
"s_mov_b32 s59, 4096\n"
"s_mul_i32 s64, s66, 9216\n"
"s_add_u32 s64, s64, %[lds]\n"
"s_add_u32 m0, s64, 0\n"
"s_add_u32 s68, s57, s58\n"
"buffer_load_dwordx4 v1, s[40:43], s68 offen offset:0 lds\n"
"buffer_load_dwordx4 v1, s[40:43], s68 offen offset:1024 lds\n"
"buffer_load_dwordx4 v1, s[40:43], s68 offen offset:2048 lds\n"
"buffer_load_dwordx4 v1, s[40:43], s68 offen offset:3072 lds\n"
"s_add_u32 m0, s64, 4096\n"
"s_add_u32 s69, s57, s59\n"
"buffer_load_dwordx4 v1, s[40:43], s69 offen offset:0 lds\n"
"s_add_u32 s70, s56, 0\n"
"buffer_load_dwordx4 v[64:67], v1, s[36:39], s70 offen offset:0\n"
"buffer_load_dwordx4 v[68:71], v1, s[36:39], s70 offen offset:1024\n"
"buffer_load_dwordx4 v[72:75], v1, s[36:39], s70 offen offset:2048\n"
"buffer_load_dwordx4 v[76:79], v1, s[36:39], s70 offen offset:3072\n"
"s_add_u32 m0, s64, 5120\n"
"s_add_u32 s71, s57, s60\n"
"buffer_load_dwordx4 v1, s[40:43], s71 offen offset:0 lds\n"
"buffer_load_dwordx4 v1, s[40:43], s71 offen offset:1024 lds\n"
"buffer_load_dwordx4 v1, s[40:43], s71 offen offset:2048 lds\n"
"buffer_load_dwordx4 v1, s[40:43], s71 offen offset:3072 lds\n"
"s_add_u32 s72, s56, 4096\n"
"buffer_load_dwordx4 v[80:83], v1, s[36:39], s72 offen offset:0\n"
"buffer_load_dwordx4 v[84:87], v1, s[36:39], s72 offen offset:1024\n"
"buffer_load_dwordx4 v[88:91], v1, s[36:39], s72 offen offset:2048\n"
"buffer_load_dwordx4 v[92:95], v1, s[36:39], s72 offen offset:3072\n"
"s_add_u32 s73, s56, 8192\n"
"buffer_load_dwordx4 v[96:99], v1, s[36:39], s73 offen offset:0\n"
"buffer_load_dwordx4 v[100:103], v1, s[36:39], s73 offen offset:1024\n"
"buffer_load_dwordx4 v[104:107], v1, s[36:39], s73 offen offset:2048\n"
"buffer_load_dwordx4 v[108:111], v1, s[36:39], s73 offen offset:3072\n"
"s_add_u32 s57, s57, 147456\n"
"s_add_u32 s56, s56, 12288\n"
"v_add_u32 v2, %[lds], v1\n"
"v_add_u32 v3, 0x10000, v2\n"
"v_add_u32 v4, 0x20000, v2\n"
"v_accvgpr_write_b32 a0, 0\n"
"v_accvgpr_write_b32 a1, 0\n"
"v_accvgpr_write_b32 a2, 0\n"
"v_accvgpr_write_b32 a3, 0\n"
"v_accvgpr_write_b32 a4, 0\n"
"v_accvgpr_write_b32 a5, 0\n"
"v_accvgpr_write_b32 a6, 0\n"
"v_accvgpr_write_b32 a7, 0\n"
"v_accvgpr_write_b32 a8, 0\n"
"v_accvgpr_write_b32 a9, 0\n"
"v_accvgpr_write_b32 a10, 0\n"
"v_accvgpr_write_b32 a11, 0\n"
"v_accvgpr_write_b32 a12, 0\n"
"v_accvgpr_write_b32 a13, 0\n"
"v_accvgpr_write_b32 a14, 0\n"
"v_accvgpr_write_b32 a15, 0\n"
"v_accvgpr_write_b32 a16, 0\n"
"v_accvgpr_write_b32 a17, 0\n"
"v_accvgpr_write_b32 a18, 0\n"
"v_accvgpr_write_b32 a19, 0\n"
"v_accvgpr_write_b32 a20, 0\n"
"v_accvgpr_write_b32 a21, 0\n"
"v_accvgpr_write_b32 a22, 0\n"
"v_accvgpr_write_b32 a23, 0\n"
"v_accvgpr_write_b32 a24, 0\n"
"v_accvgpr_write_b32 a25, 0\n"
"v_accvgpr_write_b32 a26, 0\n"
"v_accvgpr_write_b32 a27, 0\n"
"v_accvgpr_write_b32 a28, 0\n"
"v_accvgpr_write_b32 a29, 0\n"
"v_accvgpr_write_b32 a30, 0\n"
"v_accvgpr_write_b32 a31, 0\n"
"v_accvgpr_write_b32 a32, 0\n"
"v_accvgpr_write_b32 a33, 0\n"
"v_accvgpr_write_b32 a34, 0\n"
"v_accvgpr_write_b32 a35, 0\n"
"v_accvgpr_write_b32 a36, 0\n"
"v_accvgpr_write_b32 a37, 0\n"
"v_accvgpr_write_b32 a38, 0\n"
"v_accvgpr_write_b32 a39, 0\n"
"v_accvgpr_write_b32 a40, 0\n"
"v_accvgpr_write_b32 a41, 0\n"
"v_accvgpr_write_b32 a42, 0\n"
"v_accvgpr_write_b32 a43, 0\n"
"v_accvgpr_write_b32 a44, 0\n"
"v_accvgpr_write_b32 a45, 0\n"
"v_accvgpr_write_b32 a46, 0\n"
"v_accvgpr_write_b32 a47, 0\n"
"v_accvgpr_write_b32 a48, 0\n"
"v_accvgpr_write_b32 a49, 0\n"
"v_accvgpr_write_b32 a50, 0\n"
"v_accvgpr_write_b32 a51, 0\n"
"v_accvgpr_write_b32 a52, 0\n"
"v_accvgpr_write_b32 a53, 0\n"
"v_accvgpr_write_b32 a54, 0\n"
"v_accvgpr_write_b32 a55, 0\n"
"v_accvgpr_write_b32 a56, 0\n"
"v_accvgpr_write_b32 a57, 0\n"
"v_accvgpr_write_b32 a58, 0\n"
"v_accvgpr_write_b32 a59, 0\n"
"v_accvgpr_write_b32 a60, 0\n"
"v_accvgpr_write_b32 a61, 0\n"
"v_accvgpr_write_b32 a62, 0\n"
"v_accvgpr_write_b32 a63, 0\n"
"v_accvgpr_write_b32 a64, 0\n"
"v_accvgpr_write_b32 a65, 0\n"
"v_accvgpr_write_b32 a66, 0\n"
"v_accvgpr_write_b32 a67, 0\n"
"v_accvgpr_write_b32 a68, 0\n"
"v_accvgpr_write_b32 a69, 0\n"
"v_accvgpr_write_b32 a70, 0\n"
"v_accvgpr_write_b32 a71, 0\n"
"v_accvgpr_write_b32 a72, 0\n"
"v_accvgpr_write_b32 a73, 0\n"
"v_accvgpr_write_b32 a74, 0\n"
"v_accvgpr_write_b32 a75, 0\n"
"v_accvgpr_write_b32 a76, 0\n"
"v_accvgpr_write_b32 a77, 0\n"
"v_accvgpr_write_b32 a78, 0\n"
"v_accvgpr_write_b32 a79, 0\n"
"v_accvgpr_write_b32 a80, 0\n"
"v_accvgpr_write_b32 a81, 0\n"
"v_accvgpr_write_b32 a82, 0\n"
"v_accvgpr_write_b32 a83, 0\n"
"v_accvgpr_write_b32 a84, 0\n"
"v_accvgpr_write_b32 a85, 0\n"
"v_accvgpr_write_b32 a86, 0\n"
"v_accvgpr_write_b32 a87, 0\n"
"v_accvgpr_write_b32 a88, 0\n"
"v_accvgpr_write_b32 a89, 0\n"
"v_accvgpr_write_b32 a90, 0\n"
"v_accvgpr_write_b32 a91, 0\n"
"v_accvgpr_write_b32 a92, 0\n"
"v_accvgpr_write_b32 a93, 0\n"
"v_accvgpr_write_b32 a94, 0\n"
"v_accvgpr_write_b32 a95, 0\n"
"v_accvgpr_write_b32 a96, 0\n"
"v_accvgpr_write_b32 a97, 0\n"
"v_accvgpr_write_b32 a98, 0\n"
"v_accvgpr_write_b32 a99, 0\n"
"v_accvgpr_write_b32 a100, 0\n"
"v_accvgpr_write_b32 a101, 0\n"
"v_accvgpr_write_b32 a102, 0\n"
"v_accvgpr_write_b32 a103, 0\n"
"v_accvgpr_write_b32 a104, 0\n"
"v_accvgpr_write_b32 a105, 0\n"
"v_accvgpr_write_b32 a106, 0\n"
"v_accvgpr_write_b32 a107, 0\n"
"s_mov_b64 s[44:45], %[part]\n"
"s_and_b32 s45, s45, 0xffff\n"
"s_mov_b32 s46, 131072\n"
"s_mov_b32 s47, 0x20000\n"
"s_mov_b64 s[52:53], %[flag]\n"
"s_and_b32 s53, s53, 0xffff\n"
"s_mov_b32 s54, 256\n"
"s_mov_b32 s55, 0x20000\n"
"s_mov_b64 s[48:49], %[out]\n"
"s_and_b32 s49, s49, 0xffff\n"
"s_mov_b32 s50, 440352\n"
"s_mov_b32 s51, 0x20000\n"
"s_mov_b32 s65, 3\n"
"s_lshl_b32 s76, s66, 14\n"
"s_lshl_b32 s77, %[kh], 16\n"
"s_add_u32 s61, s76, s77\n"
"s_xor_b32 s62, s61, 65536\n"
"s_lshl_b32 s63, %[kh], 7\n"
"s_xor_b32 s67, s63, 128\n"
"s_mul_i32 s88, %[kh], 160\n"
"s_mul_i32 s89, %[kh], 0x1000000\n"
"s_add_u32 s89, s89, 128\n"
"v_and_b32 v8, 15, v9\n"
"v_mul_u32_u24 v5, 2304, v8\n"
"v_lshrrev_b32 v8, 4, v9\n"
"v_lshl_add_u32 v5, v8, 3, v5\n"
"s_mul_i32 s76, s66, 110592\n"
"v_add_u32 v5, s76, v5\n"
"s_waitcnt vmcnt(8)\n"
"s_barrier\n"
"ds_read_b128 a[112:115], v2 offset:0\n"
"ds_read_b128 a[116:119], v2 offset:9216\n"
"ds_read_b128 a[120:123], v2 offset:18432\n"
"ds_read_b128 a[124:127], v2 offset:27648\n"
"ds_read_b128 a[128:131], v2 offset:1024\n"
"ds_read_b128 a[132:135], v2 offset:10240\n"
"ds_read_b128 a[136:139], v2 offset:19456\n"
"ds_read_b128 a[140:143], v2 offset:28672\n"
"s_add_u32 m0, s64, 36864\n"
"s_add_u32 s74, s57, s58\n"
"buffer_load_dwordx4 v1, s[40:43], s74 offen offset:0 lds\n"
"buffer_load_dwordx4 v1, s[40:43], s74 offen offset:1024 lds\n"
"buffer_load_dwordx4 v1, s[40:43], s74 offen offset:2048 lds\n"
"buffer_load_dwordx4 v1, s[40:43], s74 offen offset:3072 lds\n"
"s_add_u32 m0, s64, 40960\n"
"s_add_u32 s75, s57, s59\n"
"buffer_load_dwordx4 v1, s[40:43], s75 offen offset:0 lds\n"
"s_add_u32 s68, s56, 0\n"
"buffer_load_dwordx4 v[112:115], v1, s[36:39], s68 offen offset:0\n"
"buffer_load_dwordx4 v[116:119], v1, s[36:39], s68 offen offset:1024\n"
"buffer_load_dwordx4 v[120:123], v1, s[36:39], s68 offen offset:2048\n"
"buffer_load_dwordx4 v[124:127], v1, s[36:39], s68 offen offset:3072\n"
"s_add_u32 m0, s64, 41984\n"
"s_add_u32 s69, s57, s60\n"
"buffer_load_dwordx4 v1, s[40:43], s69 offen offset:0 lds\n"
"buffer_load_dwordx4 v1, s[40:43], s69 offen offset:1024 lds\n"
"buffer_load_dwordx4 v1, s[40:43], s69 offen offset:2048 lds\n"
"buffer_load_dwordx4 v1, s[40:43], s69 offen offset:3072 lds\n"
"s_add_u32 s70, s56, 4096\n"
"buffer_load_dwordx4 v[128:131], v1, s[36:39], s70 offen offset:0\n"
"buffer_load_dwordx4 v[132:135], v1, s[36:39], s70 offen offset:1024\n"
"buffer_load_dwordx4 v[136:139], v1, s[36:39], s70 offen offset:2048\n"
"buffer_load_dwordx4 v[140:143], v1, s[36:39], s70 offen offset:3072\n"
"s_add_u32 s71, s56, 8192\n"
"buffer_load_dwordx4 v[144:147], v1, s[36:39], s71 offen offset:0\n"
"buffer_load_dwordx4 v[148:151], v1, s[36:39], s71 offen offset:1024\n"
"buffer_load_dwordx4 v[152:155], v1, s[36:39], s71 offen offset:2048\n"
"buffer_load_dwordx4 v[156:159], v1, s[36:39], s71 offen offset:3072\n"
"s_add_u32 s57, s57, 147456\n"
"s_add_u32 s56, s56, 12288\n"
"s_waitcnt lgkmcnt(0)\n"
"LOOP_%=:\n"
"v_mfma_f32_16x16x32_bf16 a[0:3], a[112:115], v[64:67], a[0:3]\n"
"s_add_u32 m0, s64, 73728\n"
"s_add_u32 s72, s57, s58\n"
"v_mfma_f32_16x16x32_bf16 a[0:3], a[116:119], v[76:79], a[0:3]\n"
"buffer_load_dwordx4 v1, s[40:43], s72 offen offset:0 lds\n"
"s_waitcnt vmcnt(27)\n"
"v_mfma_f32_16x16x32_bf16 a[0:3], a[120:123], v[88:91], a[0:3]\n"
"ds_read_b128 a[144:147], v2 offset:2048\n"
"ds_read_b128 a[148:151], v2 offset:11264\n"
"s_waitcnt vmcnt(24)\n"
"v_mfma_f32_16x16x32_bf16 a[0:3], a[124:127], v[100:103], a[0:3]\n"
"ds_read_b128 a[152:155], v2 offset:20480\n"
"ds_read_b128 a[156:159], v2 offset:29696\n"
"v_mfma_f32_16x16x32_bf16 a[4:7], a[112:115], v[68:71], a[4:7]\n"
"ds_read_b128 a[160:163], v2 offset:3072\n"
"ds_read_b128 a[164:167], v2 offset:12288\n"
"v_mfma_f32_16x16x32_bf16 a[4:7], a[116:119], v[80:83], a[4:7]\n"
"buffer_load_dwordx4 v1, s[40:43], s72 offen offset:1024 lds\n"
"v_mfma_f32_16x16x32_bf16 a[4:7], a[120:123], v[92:95], a[4:7]\n"
"ds_read_b128 a[168:171], v2 offset:21504\n"
"ds_read_b128 a[172:175], v2 offset:30720\n"
"s_waitcnt vmcnt(24)\n"
"v_mfma_f32_16x16x32_bf16 a[4:7], a[124:127], v[104:107], a[4:7]\n"
"ds_read_b128 a[176:179], v2 offset:4096\n"
"ds_read_b128 a[180:183], v2 offset:13312\n"
"v_mfma_f32_16x16x32_bf16 a[8:11], a[112:115], v[72:75], a[8:11]\n"
"ds_read_b128 a[184:187], v2 offset:22528\n"
"ds_read_b128 a[188:191], v2 offset:31744\n"
"v_mfma_f32_16x16x32_bf16 a[8:11], a[116:119], v[84:87], a[8:11]\n"
"ds_read_b128 a[192:195], v2 offset:5120\n"
"ds_read_b128 a[196:199], v2 offset:14336\n"
"v_mfma_f32_16x16x32_bf16 a[8:11], a[120:123], v[96:99], a[8:11]\n"
"buffer_load_dwordx4 v1, s[40:43], s72 offen offset:2048 lds\n"
"s_waitcnt vmcnt(24)\n"
"v_mfma_f32_16x16x32_bf16 a[8:11], a[124:127], v[108:111], a[8:11]\n"
"ds_read_b128 a[200:203], v2 offset:23552\n"
"ds_read_b128 a[204:207], v2 offset:32768\n"
"v_mfma_f32_16x16x32_bf16 a[12:15], a[128:131], v[64:67], a[12:15]\n"
"ds_read_b128 a[208:211], v2 offset:6144\n"
"ds_read_b128 a[212:215], v2 offset:15360\n"
"v_mfma_f32_16x16x32_bf16 a[12:15], a[132:135], v[76:79], a[12:15]\n"
"ds_read_b128 a[216:219], v2 offset:24576\n"
"ds_read_b128 a[220:223], v2 offset:33792\n"
"v_mfma_f32_16x16x32_bf16 a[12:15], a[136:139], v[88:91], a[12:15]\n"
"ds_read_b128 a[224:227], v2 offset:7168\n"
"ds_read_b128 a[228:231], v2 offset:16384\n"
"v_mfma_f32_16x16x32_bf16 a[12:15], a[140:143], v[100:103], a[12:15]\n"
"buffer_load_dwordx4 v1, s[40:43], s72 offen offset:3072 lds\n"
"v_mfma_f32_16x16x32_bf16 a[16:19], a[128:131], v[68:71], a[16:19]\n"
"ds_read_b128 a[232:235], v2 offset:25600\n"
"ds_read_b128 a[236:239], v2 offset:34816\n"
"v_mfma_f32_16x16x32_bf16 a[16:19], a[132:135], v[80:83], a[16:19]\n"
"ds_read_b128 a[240:243], v2 offset:8192\n"
"ds_read_b128 a[244:247], v2 offset:17408\n"
"v_mfma_f32_16x16x32_bf16 a[16:19], a[136:139], v[92:95], a[16:19]\n"
"ds_read_b128 a[248:251], v2 offset:26624\n"
"ds_read_b128 a[252:255], v2 offset:35840\n"
"v_mfma_f32_16x16x32_bf16 a[16:19], a[140:143], v[104:107], a[16:19]\n"
"v_mfma_f32_16x16x32_bf16 a[20:23], a[128:131], v[72:75], a[20:23]\n"
"s_add_u32 m0, s64, 77824\n"
"s_add_u32 s73, s57, s59\n"
"v_mfma_f32_16x16x32_bf16 a[20:23], a[132:135], v[84:87], a[20:23]\n"
"buffer_load_dwordx4 v1, s[40:43], s73 offen offset:0 lds\n"
"v_mfma_f32_16x16x32_bf16 a[20:23], a[136:139], v[96:99], a[20:23]\n"
"v_mfma_f32_16x16x32_bf16 a[20:23], a[140:143], v[108:111], a[20:23]\n"
"s_waitcnt lgkmcnt(7)\n"
"v_mfma_f32_16x16x32_bf16 a[24:27], a[144:147], v[64:67], a[24:27]\n"
"v_mfma_f32_16x16x32_bf16 a[24:27], a[148:151], v[76:79], a[24:27]\n"
"s_add_u32 s74, s56, 0\n"
"v_mfma_f32_16x16x32_bf16 a[24:27], a[152:155], v[88:91], a[24:27]\n"
"buffer_load_dwordx4 v[160:163], v1, s[36:39], s74 offen offset:0\n"
"v_mfma_f32_16x16x32_bf16 a[24:27], a[156:159], v[100:103], a[24:27]\n"
"v_mfma_f32_16x16x32_bf16 a[28:31], a[144:147], v[68:71], a[28:31]\n"
"v_mfma_f32_16x16x32_bf16 a[28:31], a[148:151], v[80:83], a[28:31]\n"
"v_mfma_f32_16x16x32_bf16 a[28:31], a[152:155], v[92:95], a[28:31]\n"
"buffer_load_dwordx4 v[164:167], v1, s[36:39], s74 offen offset:1024\n"
"v_mfma_f32_16x16x32_bf16 a[28:31], a[156:159], v[104:107], a[28:31]\n"
"v_mfma_f32_16x16x32_bf16 a[32:35], a[144:147], v[72:75], a[32:35]\n"
"v_mfma_f32_16x16x32_bf16 a[32:35], a[148:151], v[84:87], a[32:35]\n"
"v_mfma_f32_16x16x32_bf16 a[32:35], a[152:155], v[96:99], a[32:35]\n"
"v_mfma_f32_16x16x32_bf16 a[32:35], a[156:159], v[108:111], a[32:35]\n"
"buffer_load_dwordx4 v[168:171], v1, s[36:39], s74 offen offset:2048\n"
"v_mfma_f32_16x16x32_bf16 a[36:39], a[160:163], v[64:67], a[36:39]\n"
"v_mfma_f32_16x16x32_bf16 a[36:39], a[164:167], v[76:79], a[36:39]\n"
"v_mfma_f32_16x16x32_bf16 a[36:39], a[168:171], v[88:91], a[36:39]\n"
"v_mfma_f32_16x16x32_bf16 a[36:39], a[172:175], v[100:103], a[36:39]\n"
"v_mfma_f32_16x16x32_bf16 a[40:43], a[160:163], v[68:71], a[40:43]\n"
"buffer_load_dwordx4 v[172:175], v1, s[36:39], s74 offen offset:3072\n"
"v_mfma_f32_16x16x32_bf16 a[40:43], a[164:167], v[80:83], a[40:43]\n"
"v_mfma_f32_16x16x32_bf16 a[40:43], a[168:171], v[92:95], a[40:43]\n"
"v_mfma_f32_16x16x32_bf16 a[40:43], a[172:175], v[104:107], a[40:43]\n"
"v_mfma_f32_16x16x32_bf16 a[44:47], a[160:163], v[72:75], a[44:47]\n"
"v_mfma_f32_16x16x32_bf16 a[44:47], a[164:167], v[84:87], a[44:47]\n"
"s_add_u32 m0, s64, 78848\n"
"s_add_u32 s75, s57, s60\n"
"v_mfma_f32_16x16x32_bf16 a[44:47], a[168:171], v[96:99], a[44:47]\n"
"buffer_load_dwordx4 v1, s[40:43], s75 offen offset:0 lds\n"
"v_mfma_f32_16x16x32_bf16 a[44:47], a[172:175], v[108:111], a[44:47]\n"
"s_add_u32 s57, s57, 147456\n"
"v_mfma_f32_16x16x32_bf16 a[48:51], a[176:179], v[64:67], a[48:51]\n"
"v_mfma_f32_16x16x32_bf16 a[48:51], a[180:183], v[76:79], a[48:51]\n"
"v_mfma_f32_16x16x32_bf16 a[48:51], a[184:187], v[88:91], a[48:51]\n"
"buffer_load_dwordx4 v1, s[40:43], s75 offen offset:1024 lds\n"
"v_mfma_f32_16x16x32_bf16 a[48:51], a[188:191], v[100:103], a[48:51]\n"
"v_mfma_f32_16x16x32_bf16 a[52:55], a[176:179], v[68:71], a[52:55]\n"
"v_mfma_f32_16x16x32_bf16 a[52:55], a[180:183], v[80:83], a[52:55]\n"
"v_mfma_f32_16x16x32_bf16 a[52:55], a[184:187], v[92:95], a[52:55]\n"
"v_mfma_f32_16x16x32_bf16 a[52:55], a[188:191], v[104:107], a[52:55]\n"
"buffer_load_dwordx4 v1, s[40:43], s75 offen offset:2048 lds\n"
"v_mfma_f32_16x16x32_bf16 a[56:59], a[176:179], v[72:75], a[56:59]\n"
"v_mfma_f32_16x16x32_bf16 a[56:59], a[180:183], v[84:87], a[56:59]\n"
"v_mfma_f32_16x16x32_bf16 a[56:59], a[184:187], v[96:99], a[56:59]\n"
"v_mfma_f32_16x16x32_bf16 a[56:59], a[188:191], v[108:111], a[56:59]\n"
"v_mfma_f32_16x16x32_bf16 a[60:63], a[192:195], v[64:67], a[60:63]\n"
"buffer_load_dwordx4 v1, s[40:43], s75 offen offset:3072 lds\n"
"v_mfma_f32_16x16x32_bf16 a[60:63], a[196:199], v[76:79], a[60:63]\n"
"s_waitcnt vmcnt(21) lgkmcnt(0)\n"
"s_barrier\n"
"v_mfma_f32_16x16x32_bf16 a[60:63], a[200:203], v[88:91], a[60:63]\n"
"ds_read_b128 a[112:115], v2 offset:36864\n"
"ds_read_b128 a[116:119], v2 offset:46080\n"
"v_mfma_f32_16x16x32_bf16 a[60:63], a[204:207], v[100:103], a[60:63]\n"
"ds_read_b128 a[120:123], v2 offset:55296\n"
"ds_read_b128 a[124:127], v2 offset:64512\n"
"v_mfma_f32_16x16x32_bf16 a[64:67], a[192:195], v[68:71], a[64:67]\n"
"ds_read_b128 a[128:131], v2 offset:37888\n"
"ds_read_b128 a[132:135], v2 offset:47104\n"
"v_mfma_f32_16x16x32_bf16 a[64:67], a[196:199], v[80:83], a[64:67]\n"
"s_add_u32 s68, s56, 4096\n"
"ds_read_b128 a[136:139], v2 offset:56320\n"
"v_mfma_f32_16x16x32_bf16 a[64:67], a[200:203], v[92:95], a[64:67]\n"
"buffer_load_dwordx4 v[176:179], v1, s[36:39], s68 offen offset:0\n"
"v_mfma_f32_16x16x32_bf16 a[64:67], a[204:207], v[104:107], a[64:67]\n"
"ds_read_b128 a[140:143], v3 offset:0\n"
"ds_read_b128 a[144:147], v2 offset:38912\n"
"v_mfma_f32_16x16x32_bf16 a[68:71], a[192:195], v[72:75], a[68:71]\n"
"ds_read_b128 a[148:151], v2 offset:48128\n"
"ds_read_b128 a[152:155], v2 offset:57344\n"
"v_mfma_f32_16x16x32_bf16 a[68:71], a[196:199], v[84:87], a[68:71]\n"
"ds_read_b128 a[156:159], v3 offset:1024\n"
"ds_read_b128 a[160:163], v2 offset:39936\n"
"v_mfma_f32_16x16x32_bf16 a[68:71], a[200:203], v[96:99], a[68:71]\n"
"buffer_load_dwordx4 v[180:183], v1, s[36:39], s68 offen offset:1024\n"
"v_mfma_f32_16x16x32_bf16 a[68:71], a[204:207], v[108:111], a[68:71]\n"
"ds_read_b128 a[164:167], v2 offset:49152\n"
"ds_read_b128 a[168:171], v2 offset:58368\n"
"v_mfma_f32_16x16x32_bf16 a[72:75], a[208:211], v[64:67], a[72:75]\n"
"ds_read_b128 a[172:175], v3 offset:2048\n"
"ds_read_b128 a[176:179], v2 offset:40960\n"
"v_mfma_f32_16x16x32_bf16 a[72:75], a[212:215], v[76:79], a[72:75]\n"
"ds_read_b128 a[180:183], v2 offset:50176\n"
"ds_read_b128 a[184:187], v2 offset:59392\n"
"v_mfma_f32_16x16x32_bf16 a[72:75], a[216:219], v[88:91], a[72:75]\n"
"ds_read_b128 a[188:191], v3 offset:3072\n"
"ds_read_b128 a[192:195], v2 offset:41984\n"
"v_mfma_f32_16x16x32_bf16 a[72:75], a[220:223], v[100:103], a[72:75]\n"
"buffer_load_dwordx4 v[184:187], v1, s[36:39], s68 offen offset:2048\n"
"v_mfma_f32_16x16x32_bf16 a[76:79], a[208:211], v[68:71], a[76:79]\n"
"ds_read_b128 a[196:199], v2 offset:51200\n"
"ds_read_b128 a[200:203], v2 offset:60416\n"
"v_mfma_f32_16x16x32_bf16 a[76:79], a[212:215], v[80:83], a[76:79]\n"
"ds_read_b128 a[204:207], v3 offset:4096\n"
"v_mfma_f32_16x16x32_bf16 a[76:79], a[216:219], v[92:95], a[76:79]\n"
"v_mfma_f32_16x16x32_bf16 a[76:79], a[220:223], v[104:107], a[76:79]\n"
"v_mfma_f32_16x16x32_bf16 a[80:83], a[208:211], v[72:75], a[80:83]\n"
"buffer_load_dwordx4 v[188:191], v1, s[36:39], s68 offen offset:3072\n"
"v_mfma_f32_16x16x32_bf16 a[80:83], a[212:215], v[84:87], a[80:83]\n"
"ds_read_b128 a[208:211], v2 offset:43008\n"
"ds_read_b128 a[212:215], v2 offset:52224\n"
"v_mfma_f32_16x16x32_bf16 a[80:83], a[216:219], v[96:99], a[80:83]\n"
"ds_read_b128 a[216:219], v2 offset:61440\n"
"v_mfma_f32_16x16x32_bf16 a[80:83], a[220:223], v[108:111], a[80:83]\n"
"ds_read_b128 a[220:223], v3 offset:5120\n"
"v_mfma_f32_16x16x32_bf16 a[84:87], a[224:227], v[64:67], a[84:87]\n"
"v_mfma_f32_16x16x32_bf16 a[84:87], a[228:231], v[76:79], a[84:87]\n"
"s_add_u32 s69, s56, 8192\n"
"s_add_u32 s56, s56, 12288\n"
"v_mfma_f32_16x16x32_bf16 a[84:87], a[232:235], v[88:91], a[84:87]\n"
"buffer_load_dwordx4 v[192:195], v1, s[36:39], s69 offen offset:0\n"
"v_mfma_f32_16x16x32_bf16 a[84:87], a[236:239], v[100:103], a[84:87]\n"
"v_mfma_f32_16x16x32_bf16 a[88:91], a[224:227], v[68:71], a[88:91]\n"
"v_mfma_f32_16x16x32_bf16 a[88:91], a[228:231], v[80:83], a[88:91]\n"
"v_mfma_f32_16x16x32_bf16 a[88:91], a[232:235], v[92:95], a[88:91]\n"
"buffer_load_dwordx4 v[196:199], v1, s[36:39], s69 offen offset:1024\n"
"v_mfma_f32_16x16x32_bf16 a[88:91], a[236:239], v[104:107], a[88:91]\n"
"v_mfma_f32_16x16x32_bf16 a[92:95], a[224:227], v[72:75], a[92:95]\n"
"ds_read_b128 a[224:227], v2 offset:44032\n"
"v_mfma_f32_16x16x32_bf16 a[92:95], a[228:231], v[84:87], a[92:95]\n"
"ds_read_b128 a[228:231], v2 offset:53248\n"
"v_mfma_f32_16x16x32_bf16 a[92:95], a[232:235], v[96:99], a[92:95]\n"
"ds_read_b128 a[232:235], v2 offset:62464\n"
"v_mfma_f32_16x16x32_bf16 a[92:95], a[236:239], v[108:111], a[92:95]\n"
"buffer_load_dwordx4 v[200:203], v1, s[36:39], s69 offen offset:2048\n"
"v_mfma_f32_16x16x32_bf16 a[96:99], a[240:243], v[64:67], a[96:99]\n"
"ds_read_b128 a[236:239], v3 offset:6144\n"
"v_mfma_f32_16x16x32_bf16 a[96:99], a[244:247], v[76:79], a[96:99]\n"
"v_mfma_f32_16x16x32_bf16 a[96:99], a[248:251], v[88:91], a[96:99]\n"
"v_mfma_f32_16x16x32_bf16 a[96:99], a[252:255], v[100:103], a[96:99]\n"
"v_mfma_f32_16x16x32_bf16 a[100:103], a[240:243], v[68:71], a[100:103]\n"
"buffer_load_dwordx4 v[204:207], v1, s[36:39], s69 offen offset:3072\n"
"v_mfma_f32_16x16x32_bf16 a[100:103], a[244:247], v[80:83], a[100:103]\n"
"v_mfma_f32_16x16x32_bf16 a[100:103], a[248:251], v[92:95], a[100:103]\n"
"v_mfma_f32_16x16x32_bf16 a[100:103], a[252:255], v[104:107], a[100:103]\n"
"v_mfma_f32_16x16x32_bf16 a[104:107], a[240:243], v[72:75], a[104:107]\n"
"ds_read_b128 a[240:243], v2 offset:45056\n"
"v_mfma_f32_16x16x32_bf16 a[104:107], a[244:247], v[84:87], a[104:107]\n"
"ds_read_b128 a[244:247], v2 offset:54272\n"
"v_mfma_f32_16x16x32_bf16 a[104:107], a[248:251], v[96:99], a[104:107]\n"
"ds_read_b128 a[248:251], v2 offset:63488\n"
"v_mfma_f32_16x16x32_bf16 a[104:107], a[252:255], v[108:111], a[104:107]\n"
"ds_read_b128 a[252:255], v3 offset:7168\n"
"s_waitcnt lgkmcnt(4)\n"
"v_mfma_f32_16x16x32_bf16 a[0:3], a[112:115], v[112:115], a[0:3]\n"
"s_add_u32 m0, s64, 110592\n"
"s_add_u32 s70, s57, s58\n"
"v_mfma_f32_16x16x32_bf16 a[0:3], a[116:119], v[124:127], a[0:3]\n"
"buffer_load_dwordx4 v1, s[40:43], s70 offen offset:0 lds\n"
"s_waitcnt vmcnt(27)\n"
"v_mfma_f32_16x16x32_bf16 a[0:3], a[120:123], v[136:139], a[0:3]\n"
"s_waitcnt vmcnt(24)\n"
"v_mfma_f32_16x16x32_bf16 a[0:3], a[124:127], v[148:151], a[0:3]\n"
"v_mfma_f32_16x16x32_bf16 a[4:7], a[112:115], v[116:119], a[4:7]\n"
"v_mfma_f32_16x16x32_bf16 a[4:7], a[116:119], v[128:131], a[4:7]\n"
"buffer_load_dwordx4 v1, s[40:43], s70 offen offset:1024 lds\n"
"v_mfma_f32_16x16x32_bf16 a[4:7], a[120:123], v[140:143], a[4:7]\n"
"s_waitcnt vmcnt(24)\n"
"v_mfma_f32_16x16x32_bf16 a[4:7], a[124:127], v[152:155], a[4:7]\n"
"v_mfma_f32_16x16x32_bf16 a[8:11], a[112:115], v[120:123], a[8:11]\n"
"v_mfma_f32_16x16x32_bf16 a[8:11], a[116:119], v[132:135], a[8:11]\n"
"v_mfma_f32_16x16x32_bf16 a[8:11], a[120:123], v[144:147], a[8:11]\n"
"buffer_load_dwordx4 v1, s[40:43], s70 offen offset:2048 lds\n"
"s_waitcnt vmcnt(24)\n"
"v_mfma_f32_16x16x32_bf16 a[8:11], a[124:127], v[156:159], a[8:11]\n"
"v_mfma_f32_16x16x32_bf16 a[12:15], a[128:131], v[112:115], a[12:15]\n"
"v_mfma_f32_16x16x32_bf16 a[12:15], a[132:135], v[124:127], a[12:15]\n"
"v_mfma_f32_16x16x32_bf16 a[12:15], a[136:139], v[136:139], a[12:15]\n"
"v_mfma_f32_16x16x32_bf16 a[12:15], a[140:143], v[148:151], a[12:15]\n"
"buffer_load_dwordx4 v1, s[40:43], s70 offen offset:3072 lds\n"
"v_mfma_f32_16x16x32_bf16 a[16:19], a[128:131], v[116:119], a[16:19]\n"
"v_mfma_f32_16x16x32_bf16 a[16:19], a[132:135], v[128:131], a[16:19]\n"
"v_mfma_f32_16x16x32_bf16 a[16:19], a[136:139], v[140:143], a[16:19]\n"
"v_mfma_f32_16x16x32_bf16 a[16:19], a[140:143], v[152:155], a[16:19]\n"
"v_mfma_f32_16x16x32_bf16 a[20:23], a[128:131], v[120:123], a[20:23]\n"
"s_add_u32 m0, s64, 114688\n"
"s_add_u32 s71, s57, s59\n"
"v_mfma_f32_16x16x32_bf16 a[20:23], a[132:135], v[132:135], a[20:23]\n"
"buffer_load_dwordx4 v1, s[40:43], s71 offen offset:0 lds\n"
"v_mfma_f32_16x16x32_bf16 a[20:23], a[136:139], v[144:147], a[20:23]\n"
"v_mfma_f32_16x16x32_bf16 a[20:23], a[140:143], v[156:159], a[20:23]\n"
"v_mfma_f32_16x16x32_bf16 a[24:27], a[144:147], v[112:115], a[24:27]\n"
"v_mfma_f32_16x16x32_bf16 a[24:27], a[148:151], v[124:127], a[24:27]\n"
"s_add_u32 s72, s56, 0\n"
"v_mfma_f32_16x16x32_bf16 a[24:27], a[152:155], v[136:139], a[24:27]\n"
"buffer_load_dwordx4 v[208:211], v1, s[36:39], s72 offen offset:0\n"
"v_mfma_f32_16x16x32_bf16 a[24:27], a[156:159], v[148:151], a[24:27]\n"
"v_mfma_f32_16x16x32_bf16 a[28:31], a[144:147], v[116:119], a[28:31]\n"
"v_mfma_f32_16x16x32_bf16 a[28:31], a[148:151], v[128:131], a[28:31]\n"
"v_mfma_f32_16x16x32_bf16 a[28:31], a[152:155], v[140:143], a[28:31]\n"
"buffer_load_dwordx4 v[212:215], v1, s[36:39], s72 offen offset:1024\n"
"v_mfma_f32_16x16x32_bf16 a[28:31], a[156:159], v[152:155], a[28:31]\n"
"v_mfma_f32_16x16x32_bf16 a[32:35], a[144:147], v[120:123], a[32:35]\n"
"v_mfma_f32_16x16x32_bf16 a[32:35], a[148:151], v[132:135], a[32:35]\n"
"v_mfma_f32_16x16x32_bf16 a[32:35], a[152:155], v[144:147], a[32:35]\n"
"v_mfma_f32_16x16x32_bf16 a[32:35], a[156:159], v[156:159], a[32:35]\n"
"buffer_load_dwordx4 v[216:219], v1, s[36:39], s72 offen offset:2048\n"
"v_mfma_f32_16x16x32_bf16 a[36:39], a[160:163], v[112:115], a[36:39]\n"
"v_mfma_f32_16x16x32_bf16 a[36:39], a[164:167], v[124:127], a[36:39]\n"
"v_mfma_f32_16x16x32_bf16 a[36:39], a[168:171], v[136:139], a[36:39]\n"
"v_mfma_f32_16x16x32_bf16 a[36:39], a[172:175], v[148:151], a[36:39]\n"
"v_mfma_f32_16x16x32_bf16 a[40:43], a[160:163], v[116:119], a[40:43]\n"
"buffer_load_dwordx4 v[220:223], v1, s[36:39], s72 offen offset:3072\n"
"v_mfma_f32_16x16x32_bf16 a[40:43], a[164:167], v[128:131], a[40:43]\n"
"v_mfma_f32_16x16x32_bf16 a[40:43], a[168:171], v[140:143], a[40:43]\n"
"v_mfma_f32_16x16x32_bf16 a[40:43], a[172:175], v[152:155], a[40:43]\n"
"v_mfma_f32_16x16x32_bf16 a[44:47], a[160:163], v[120:123], a[44:47]\n"
"v_mfma_f32_16x16x32_bf16 a[44:47], a[164:167], v[132:135], a[44:47]\n"
"s_add_u32 m0, s64, 115712\n"
"s_add_u32 s73, s57, s60\n"
"v_mfma_f32_16x16x32_bf16 a[44:47], a[168:171], v[144:147], a[44:47]\n"
"buffer_load_dwordx4 v1, s[40:43], s73 offen offset:0 lds\n"
"v_mfma_f32_16x16x32_bf16 a[44:47], a[172:175], v[156:159], a[44:47]\n"
"s_add_u32 s57, s57, 147456\n"
"v_mfma_f32_16x16x32_bf16 a[48:51], a[176:179], v[112:115], a[48:51]\n"
"v_mfma_f32_16x16x32_bf16 a[48:51], a[180:183], v[124:127], a[48:51]\n"
"v_mfma_f32_16x16x32_bf16 a[48:51], a[184:187], v[136:139], a[48:51]\n"
"buffer_load_dwordx4 v1, s[40:43], s73 offen offset:1024 lds\n"
"v_mfma_f32_16x16x32_bf16 a[48:51], a[188:191], v[148:151], a[48:51]\n"
"v_mfma_f32_16x16x32_bf16 a[52:55], a[176:179], v[116:119], a[52:55]\n"
"v_mfma_f32_16x16x32_bf16 a[52:55], a[180:183], v[128:131], a[52:55]\n"
"v_mfma_f32_16x16x32_bf16 a[52:55], a[184:187], v[140:143], a[52:55]\n"
"v_mfma_f32_16x16x32_bf16 a[52:55], a[188:191], v[152:155], a[52:55]\n"
"buffer_load_dwordx4 v1, s[40:43], s73 offen offset:2048 lds\n"
"v_mfma_f32_16x16x32_bf16 a[56:59], a[176:179], v[120:123], a[56:59]\n"
"v_mfma_f32_16x16x32_bf16 a[56:59], a[180:183], v[132:135], a[56:59]\n"
"v_mfma_f32_16x16x32_bf16 a[56:59], a[184:187], v[144:147], a[56:59]\n"
"v_mfma_f32_16x16x32_bf16 a[56:59], a[188:191], v[156:159], a[56:59]\n"
"v_mfma_f32_16x16x32_bf16 a[60:63], a[192:195], v[112:115], a[60:63]\n"
"buffer_load_dwordx4 v1, s[40:43], s73 offen offset:3072 lds\n"
"v_mfma_f32_16x16x32_bf16 a[60:63], a[196:199], v[124:127], a[60:63]\n"
"s_waitcnt vmcnt(21) lgkmcnt(0)\n"
"s_barrier\n"
"v_mfma_f32_16x16x32_bf16 a[60:63], a[200:203], v[136:139], a[60:63]\n"
"ds_read_b128 a[112:115], v3 offset:8192\n"
"ds_read_b128 a[116:119], v3 offset:17408\n"
"v_mfma_f32_16x16x32_bf16 a[60:63], a[204:207], v[148:151], a[60:63]\n"
"ds_read_b128 a[120:123], v3 offset:26624\n"
"ds_read_b128 a[124:127], v3 offset:35840\n"
"v_mfma_f32_16x16x32_bf16 a[64:67], a[192:195], v[116:119], a[64:67]\n"
"ds_read_b128 a[128:131], v3 offset:9216\n"
"ds_read_b128 a[132:135], v3 offset:18432\n"
"v_mfma_f32_16x16x32_bf16 a[64:67], a[196:199], v[128:131], a[64:67]\n"
"s_add_u32 s74, s56, 4096\n"
"ds_read_b128 a[136:139], v3 offset:27648\n"
"v_mfma_f32_16x16x32_bf16 a[64:67], a[200:203], v[140:143], a[64:67]\n"
"buffer_load_dwordx4 v[224:227], v1, s[36:39], s74 offen offset:0\n"
"v_mfma_f32_16x16x32_bf16 a[64:67], a[204:207], v[152:155], a[64:67]\n"
"ds_read_b128 a[140:143], v3 offset:36864\n"
"ds_read_b128 a[144:147], v3 offset:10240\n"
"v_mfma_f32_16x16x32_bf16 a[68:71], a[192:195], v[120:123], a[68:71]\n"
"ds_read_b128 a[148:151], v3 offset:19456\n"
"ds_read_b128 a[152:155], v3 offset:28672\n"
"v_mfma_f32_16x16x32_bf16 a[68:71], a[196:199], v[132:135], a[68:71]\n"
"ds_read_b128 a[156:159], v3 offset:37888\n"
"ds_read_b128 a[160:163], v3 offset:11264\n"
"v_mfma_f32_16x16x32_bf16 a[68:71], a[200:203], v[144:147], a[68:71]\n"
"buffer_load_dwordx4 v[228:231], v1, s[36:39], s74 offen offset:1024\n"
"v_mfma_f32_16x16x32_bf16 a[68:71], a[204:207], v[156:159], a[68:71]\n"
"ds_read_b128 a[164:167], v3 offset:20480\n"
"ds_read_b128 a[168:171], v3 offset:29696\n"
"v_mfma_f32_16x16x32_bf16 a[72:75], a[208:211], v[112:115], a[72:75]\n"
"ds_read_b128 a[172:175], v3 offset:38912\n"
"ds_read_b128 a[176:179], v3 offset:12288\n"
"v_mfma_f32_16x16x32_bf16 a[72:75], a[212:215], v[124:127], a[72:75]\n"
"ds_read_b128 a[180:183], v3 offset:21504\n"
"ds_read_b128 a[184:187], v3 offset:30720\n"
"v_mfma_f32_16x16x32_bf16 a[72:75], a[216:219], v[136:139], a[72:75]\n"
"ds_read_b128 a[188:191], v3 offset:39936\n"
"ds_read_b128 a[192:195], v3 offset:13312\n"
"v_mfma_f32_16x16x32_bf16 a[72:75], a[220:223], v[148:151], a[72:75]\n"
"buffer_load_dwordx4 v[232:235], v1, s[36:39], s74 offen offset:2048\n"
"v_mfma_f32_16x16x32_bf16 a[76:79], a[208:211], v[116:119], a[76:79]\n"
"ds_read_b128 a[196:199], v3 offset:22528\n"
"ds_read_b128 a[200:203], v3 offset:31744\n"
"v_mfma_f32_16x16x32_bf16 a[76:79], a[212:215], v[128:131], a[76:79]\n"
"ds_read_b128 a[204:207], v3 offset:40960\n"
"v_mfma_f32_16x16x32_bf16 a[76:79], a[216:219], v[140:143], a[76:79]\n"
"v_mfma_f32_16x16x32_bf16 a[76:79], a[220:223], v[152:155], a[76:79]\n"
"v_mfma_f32_16x16x32_bf16 a[80:83], a[208:211], v[120:123], a[80:83]\n"
"buffer_load_dwordx4 v[236:239], v1, s[36:39], s74 offen offset:3072\n"
"v_mfma_f32_16x16x32_bf16 a[80:83], a[212:215], v[132:135], a[80:83]\n"
"ds_read_b128 a[208:211], v3 offset:14336\n"
"ds_read_b128 a[212:215], v3 offset:23552\n"
"v_mfma_f32_16x16x32_bf16 a[80:83], a[216:219], v[144:147], a[80:83]\n"
"ds_read_b128 a[216:219], v3 offset:32768\n"
"v_mfma_f32_16x16x32_bf16 a[80:83], a[220:223], v[156:159], a[80:83]\n"
"ds_read_b128 a[220:223], v3 offset:41984\n"
"v_mfma_f32_16x16x32_bf16 a[84:87], a[224:227], v[112:115], a[84:87]\n"
"v_mfma_f32_16x16x32_bf16 a[84:87], a[228:231], v[124:127], a[84:87]\n"
"s_add_u32 s75, s56, 8192\n"
"s_add_u32 s56, s56, 12288\n"
"v_mfma_f32_16x16x32_bf16 a[84:87], a[232:235], v[136:139], a[84:87]\n"
"buffer_load_dwordx4 v[240:243], v1, s[36:39], s75 offen offset:0\n"
"v_mfma_f32_16x16x32_bf16 a[84:87], a[236:239], v[148:151], a[84:87]\n"
"v_mfma_f32_16x16x32_bf16 a[88:91], a[224:227], v[116:119], a[88:91]\n"
"v_mfma_f32_16x16x32_bf16 a[88:91], a[228:231], v[128:131], a[88:91]\n"
"v_mfma_f32_16x16x32_bf16 a[88:91], a[232:235], v[140:143], a[88:91]\n"
"buffer_load_dwordx4 v[244:247], v1, s[36:39], s75 offen offset:1024\n"
"v_mfma_f32_16x16x32_bf16 a[88:91], a[236:239], v[152:155], a[88:91]\n"
"v_mfma_f32_16x16x32_bf16 a[92:95], a[224:227], v[120:123], a[92:95]\n"
"ds_read_b128 a[224:227], v3 offset:15360\n"
"v_mfma_f32_16x16x32_bf16 a[92:95], a[228:231], v[132:135], a[92:95]\n"
"ds_read_b128 a[228:231], v3 offset:24576\n"
"v_mfma_f32_16x16x32_bf16 a[92:95], a[232:235], v[144:147], a[92:95]\n"
"ds_read_b128 a[232:235], v3 offset:33792\n"
"v_mfma_f32_16x16x32_bf16 a[92:95], a[236:239], v[156:159], a[92:95]\n"
"buffer_load_dwordx4 v[248:251], v1, s[36:39], s75 offen offset:2048\n"
"v_mfma_f32_16x16x32_bf16 a[96:99], a[240:243], v[112:115], a[96:99]\n"
"ds_read_b128 a[236:239], v3 offset:43008\n"
"v_mfma_f32_16x16x32_bf16 a[96:99], a[244:247], v[124:127], a[96:99]\n"
"v_mfma_f32_16x16x32_bf16 a[96:99], a[248:251], v[136:139], a[96:99]\n"
"v_mfma_f32_16x16x32_bf16 a[96:99], a[252:255], v[148:151], a[96:99]\n"
"v_mfma_f32_16x16x32_bf16 a[100:103], a[240:243], v[116:119], a[100:103]\n"
"buffer_load_dwordx4 v[252:255], v1, s[36:39], s75 offen offset:3072\n"
"v_mfma_f32_16x16x32_bf16 a[100:103], a[244:247], v[128:131], a[100:103]\n"
"v_mfma_f32_16x16x32_bf16 a[100:103], a[248:251], v[140:143], a[100:103]\n"
"v_mfma_f32_16x16x32_bf16 a[100:103], a[252:255], v[152:155], a[100:103]\n"
"v_mfma_f32_16x16x32_bf16 a[104:107], a[240:243], v[120:123], a[104:107]\n"
"ds_read_b128 a[240:243], v3 offset:16384\n"
"v_mfma_f32_16x16x32_bf16 a[104:107], a[244:247], v[132:135], a[104:107]\n"
"ds_read_b128 a[244:247], v3 offset:25600\n"
"v_mfma_f32_16x16x32_bf16 a[104:107], a[248:251], v[144:147], a[104:107]\n"
"ds_read_b128 a[248:251], v3 offset:34816\n"
"v_mfma_f32_16x16x32_bf16 a[104:107], a[252:255], v[156:159], a[104:107]\n"
"ds_read_b128 a[252:255], v3 offset:44032\n"
"s_waitcnt lgkmcnt(4)\n"
"v_mfma_f32_16x16x32_bf16 a[0:3], a[112:115], v[160:163], a[0:3]\n"
"s_add_u32 m0, s64, 0\n"
"s_add_u32 s68, s57, s58\n"
"v_mfma_f32_16x16x32_bf16 a[0:3], a[116:119], v[172:175], a[0:3]\n"
"buffer_load_dwordx4 v1, s[40:43], s68 offen offset:0 lds\n"
"s_waitcnt vmcnt(27)\n"
"v_mfma_f32_16x16x32_bf16 a[0:3], a[120:123], v[184:187], a[0:3]\n"
"s_waitcnt vmcnt(24)\n"
"v_mfma_f32_16x16x32_bf16 a[0:3], a[124:127], v[196:199], a[0:3]\n"
"v_mfma_f32_16x16x32_bf16 a[4:7], a[112:115], v[164:167], a[4:7]\n"
"v_mfma_f32_16x16x32_bf16 a[4:7], a[116:119], v[176:179], a[4:7]\n"
"buffer_load_dwordx4 v1, s[40:43], s68 offen offset:1024 lds\n"
"v_mfma_f32_16x16x32_bf16 a[4:7], a[120:123], v[188:191], a[4:7]\n"
"s_waitcnt vmcnt(24)\n"
"v_mfma_f32_16x16x32_bf16 a[4:7], a[124:127], v[200:203], a[4:7]\n"
"v_mfma_f32_16x16x32_bf16 a[8:11], a[112:115], v[168:171], a[8:11]\n"
"v_mfma_f32_16x16x32_bf16 a[8:11], a[116:119], v[180:183], a[8:11]\n"
"v_mfma_f32_16x16x32_bf16 a[8:11], a[120:123], v[192:195], a[8:11]\n"
"buffer_load_dwordx4 v1, s[40:43], s68 offen offset:2048 lds\n"
"s_waitcnt vmcnt(24)\n"
"v_mfma_f32_16x16x32_bf16 a[8:11], a[124:127], v[204:207], a[8:11]\n"
"v_mfma_f32_16x16x32_bf16 a[12:15], a[128:131], v[160:163], a[12:15]\n"
"v_mfma_f32_16x16x32_bf16 a[12:15], a[132:135], v[172:175], a[12:15]\n"
"v_mfma_f32_16x16x32_bf16 a[12:15], a[136:139], v[184:187], a[12:15]\n"
"v_mfma_f32_16x16x32_bf16 a[12:15], a[140:143], v[196:199], a[12:15]\n"
"buffer_load_dwordx4 v1, s[40:43], s68 offen offset:3072 lds\n"
"v_mfma_f32_16x16x32_bf16 a[16:19], a[128:131], v[164:167], a[16:19]\n"
"v_mfma_f32_16x16x32_bf16 a[16:19], a[132:135], v[176:179], a[16:19]\n"
"v_mfma_f32_16x16x32_bf16 a[16:19], a[136:139], v[188:191], a[16:19]\n"
"v_mfma_f32_16x16x32_bf16 a[16:19], a[140:143], v[200:203], a[16:19]\n"
"v_mfma_f32_16x16x32_bf16 a[20:23], a[128:131], v[168:171], a[20:23]\n"
"s_add_u32 m0, s64, 4096\n"
"s_add_u32 s69, s57, s59\n"
"v_mfma_f32_16x16x32_bf16 a[20:23], a[132:135], v[180:183], a[20:23]\n"
"buffer_load_dwordx4 v1, s[40:43], s69 offen offset:0 lds\n"
"v_mfma_f32_16x16x32_bf16 a[20:23], a[136:139], v[192:195], a[20:23]\n"
"v_mfma_f32_16x16x32_bf16 a[20:23], a[140:143], v[204:207], a[20:23]\n"
"v_mfma_f32_16x16x32_bf16 a[24:27], a[144:147], v[160:163], a[24:27]\n"
"v_mfma_f32_16x16x32_bf16 a[24:27], a[148:151], v[172:175], a[24:27]\n"
"s_add_u32 s70, s56, 0\n"
"v_mfma_f32_16x16x32_bf16 a[24:27], a[152:155], v[184:187], a[24:27]\n"
"buffer_load_dwordx4 v[64:67], v1, s[36:39], s70 offen offset:0\n"
"v_mfma_f32_16x16x32_bf16 a[24:27], a[156:159], v[196:199], a[24:27]\n"
"v_mfma_f32_16x16x32_bf16 a[28:31], a[144:147], v[164:167], a[28:31]\n"
"v_mfma_f32_16x16x32_bf16 a[28:31], a[148:151], v[176:179], a[28:31]\n"
"v_mfma_f32_16x16x32_bf16 a[28:31], a[152:155], v[188:191], a[28:31]\n"
"buffer_load_dwordx4 v[68:71], v1, s[36:39], s70 offen offset:1024\n"
"v_mfma_f32_16x16x32_bf16 a[28:31], a[156:159], v[200:203], a[28:31]\n"
"v_mfma_f32_16x16x32_bf16 a[32:35], a[144:147], v[168:171], a[32:35]\n"
"v_mfma_f32_16x16x32_bf16 a[32:35], a[148:151], v[180:183], a[32:35]\n"
"v_mfma_f32_16x16x32_bf16 a[32:35], a[152:155], v[192:195], a[32:35]\n"
"v_mfma_f32_16x16x32_bf16 a[32:35], a[156:159], v[204:207], a[32:35]\n"
"buffer_load_dwordx4 v[72:75], v1, s[36:39], s70 offen offset:2048\n"
"v_mfma_f32_16x16x32_bf16 a[36:39], a[160:163], v[160:163], a[36:39]\n"
"v_mfma_f32_16x16x32_bf16 a[36:39], a[164:167], v[172:175], a[36:39]\n"
"v_mfma_f32_16x16x32_bf16 a[36:39], a[168:171], v[184:187], a[36:39]\n"
"v_mfma_f32_16x16x32_bf16 a[36:39], a[172:175], v[196:199], a[36:39]\n"
"v_mfma_f32_16x16x32_bf16 a[40:43], a[160:163], v[164:167], a[40:43]\n"
"buffer_load_dwordx4 v[76:79], v1, s[36:39], s70 offen offset:3072\n"
"v_mfma_f32_16x16x32_bf16 a[40:43], a[164:167], v[176:179], a[40:43]\n"
"v_mfma_f32_16x16x32_bf16 a[40:43], a[168:171], v[188:191], a[40:43]\n"
"v_mfma_f32_16x16x32_bf16 a[40:43], a[172:175], v[200:203], a[40:43]\n"
"v_mfma_f32_16x16x32_bf16 a[44:47], a[160:163], v[168:171], a[44:47]\n"
"v_mfma_f32_16x16x32_bf16 a[44:47], a[164:167], v[180:183], a[44:47]\n"
"s_add_u32 m0, s64, 5120\n"
"s_add_u32 s71, s57, s60\n"
"v_mfma_f32_16x16x32_bf16 a[44:47], a[168:171], v[192:195], a[44:47]\n"
"buffer_load_dwordx4 v1, s[40:43], s71 offen offset:0 lds\n"
"v_mfma_f32_16x16x32_bf16 a[44:47], a[172:175], v[204:207], a[44:47]\n"
"s_add_u32 s57, s57, 147456\n"
"v_mfma_f32_16x16x32_bf16 a[48:51], a[176:179], v[160:163], a[48:51]\n"
"v_mfma_f32_16x16x32_bf16 a[48:51], a[180:183], v[172:175], a[48:51]\n"
"v_mfma_f32_16x16x32_bf16 a[48:51], a[184:187], v[184:187], a[48:51]\n"
"buffer_load_dwordx4 v1, s[40:43], s71 offen offset:1024 lds\n"
"v_mfma_f32_16x16x32_bf16 a[48:51], a[188:191], v[196:199], a[48:51]\n"
"v_mfma_f32_16x16x32_bf16 a[52:55], a[176:179], v[164:167], a[52:55]\n"
"v_mfma_f32_16x16x32_bf16 a[52:55], a[180:183], v[176:179], a[52:55]\n"
"v_mfma_f32_16x16x32_bf16 a[52:55], a[184:187], v[188:191], a[52:55]\n"
"v_mfma_f32_16x16x32_bf16 a[52:55], a[188:191], v[200:203], a[52:55]\n"
"buffer_load_dwordx4 v1, s[40:43], s71 offen offset:2048 lds\n"
"v_mfma_f32_16x16x32_bf16 a[56:59], a[176:179], v[168:171], a[56:59]\n"
"v_mfma_f32_16x16x32_bf16 a[56:59], a[180:183], v[180:183], a[56:59]\n"
"v_mfma_f32_16x16x32_bf16 a[56:59], a[184:187], v[192:195], a[56:59]\n"
"v_mfma_f32_16x16x32_bf16 a[56:59], a[188:191], v[204:207], a[56:59]\n"
"v_mfma_f32_16x16x32_bf16 a[60:63], a[192:195], v[160:163], a[60:63]\n"
"buffer_load_dwordx4 v1, s[40:43], s71 offen offset:3072 lds\n"
"v_mfma_f32_16x16x32_bf16 a[60:63], a[196:199], v[172:175], a[60:63]\n"
"s_waitcnt vmcnt(21) lgkmcnt(0)\n"
"s_barrier\n"
"v_mfma_f32_16x16x32_bf16 a[60:63], a[200:203], v[184:187], a[60:63]\n"
"ds_read_b128 a[112:115], v3 offset:45056\n"
"ds_read_b128 a[116:119], v3 offset:54272\n"
"v_mfma_f32_16x16x32_bf16 a[60:63], a[204:207], v[196:199], a[60:63]\n"
"ds_read_b128 a[120:123], v3 offset:63488\n"
"ds_read_b128 a[124:127], v4 offset:7168\n"
"v_mfma_f32_16x16x32_bf16 a[64:67], a[192:195], v[164:167], a[64:67]\n"
"ds_read_b128 a[128:131], v3 offset:46080\n"
"ds_read_b128 a[132:135], v3 offset:55296\n"
"v_mfma_f32_16x16x32_bf16 a[64:67], a[196:199], v[176:179], a[64:67]\n"
"s_add_u32 s72, s56, 4096\n"
"ds_read_b128 a[136:139], v3 offset:64512\n"
"v_mfma_f32_16x16x32_bf16 a[64:67], a[200:203], v[188:191], a[64:67]\n"
"buffer_load_dwordx4 v[80:83], v1, s[36:39], s72 offen offset:0\n"
"v_mfma_f32_16x16x32_bf16 a[64:67], a[204:207], v[200:203], a[64:67]\n"
"ds_read_b128 a[140:143], v4 offset:8192\n"
"ds_read_b128 a[144:147], v3 offset:47104\n"
"v_mfma_f32_16x16x32_bf16 a[68:71], a[192:195], v[168:171], a[68:71]\n"
"ds_read_b128 a[148:151], v3 offset:56320\n"
"ds_read_b128 a[152:155], v4 offset:0\n"
"v_mfma_f32_16x16x32_bf16 a[68:71], a[196:199], v[180:183], a[68:71]\n"
"ds_read_b128 a[156:159], v4 offset:9216\n"
"ds_read_b128 a[160:163], v3 offset:48128\n"
"v_mfma_f32_16x16x32_bf16 a[68:71], a[200:203], v[192:195], a[68:71]\n"
"buffer_load_dwordx4 v[84:87], v1, s[36:39], s72 offen offset:1024\n"
"v_mfma_f32_16x16x32_bf16 a[68:71], a[204:207], v[204:207], a[68:71]\n"
"ds_read_b128 a[164:167], v3 offset:57344\n"
"ds_read_b128 a[168:171], v4 offset:1024\n"
"v_mfma_f32_16x16x32_bf16 a[72:75], a[208:211], v[160:163], a[72:75]\n"
"ds_read_b128 a[172:175], v4 offset:10240\n"
"ds_read_b128 a[176:179], v3 offset:49152\n"
"v_mfma_f32_16x16x32_bf16 a[72:75], a[212:215], v[172:175], a[72:75]\n"
"ds_read_b128 a[180:183], v3 offset:58368\n"
"ds_read_b128 a[184:187], v4 offset:2048\n"
"v_mfma_f32_16x16x32_bf16 a[72:75], a[216:219], v[184:187], a[72:75]\n"
"ds_read_b128 a[188:191], v4 offset:11264\n"
"ds_read_b128 a[192:195], v3 offset:50176\n"
"v_mfma_f32_16x16x32_bf16 a[72:75], a[220:223], v[196:199], a[72:75]\n"
"buffer_load_dwordx4 v[88:91], v1, s[36:39], s72 offen offset:2048\n"
"v_mfma_f32_16x16x32_bf16 a[76:79], a[208:211], v[164:167], a[76:79]\n"
"ds_read_b128 a[196:199], v3 offset:59392\n"
"ds_read_b128 a[200:203], v4 offset:3072\n"
"v_mfma_f32_16x16x32_bf16 a[76:79], a[212:215], v[176:179], a[76:79]\n"
"ds_read_b128 a[204:207], v4 offset:12288\n"
"v_mfma_f32_16x16x32_bf16 a[76:79], a[216:219], v[188:191], a[76:79]\n"
"v_mfma_f32_16x16x32_bf16 a[76:79], a[220:223], v[200:203], a[76:79]\n"
"v_mfma_f32_16x16x32_bf16 a[80:83], a[208:211], v[168:171], a[80:83]\n"
"buffer_load_dwordx4 v[92:95], v1, s[36:39], s72 offen offset:3072\n"
"v_mfma_f32_16x16x32_bf16 a[80:83], a[212:215], v[180:183], a[80:83]\n"
"ds_read_b128 a[208:211], v3 offset:51200\n"
"ds_read_b128 a[212:215], v3 offset:60416\n"
"v_mfma_f32_16x16x32_bf16 a[80:83], a[216:219], v[192:195], a[80:83]\n"
"ds_read_b128 a[216:219], v4 offset:4096\n"
"v_mfma_f32_16x16x32_bf16 a[80:83], a[220:223], v[204:207], a[80:83]\n"
"ds_read_b128 a[220:223], v4 offset:13312\n"
"v_mfma_f32_16x16x32_bf16 a[84:87], a[224:227], v[160:163], a[84:87]\n"
"v_mfma_f32_16x16x32_bf16 a[84:87], a[228:231], v[172:175], a[84:87]\n"
"s_add_u32 s73, s56, 8192\n"
"s_add_u32 s56, s56, 12288\n"
"v_mfma_f32_16x16x32_bf16 a[84:87], a[232:235], v[184:187], a[84:87]\n"
"buffer_load_dwordx4 v[96:99], v1, s[36:39], s73 offen offset:0\n"
"v_mfma_f32_16x16x32_bf16 a[84:87], a[236:239], v[196:199], a[84:87]\n"
"v_mfma_f32_16x16x32_bf16 a[88:91], a[224:227], v[164:167], a[88:91]\n"
"v_mfma_f32_16x16x32_bf16 a[88:91], a[228:231], v[176:179], a[88:91]\n"
"v_mfma_f32_16x16x32_bf16 a[88:91], a[232:235], v[188:191], a[88:91]\n"
"buffer_load_dwordx4 v[100:103], v1, s[36:39], s73 offen offset:1024\n"
"v_mfma_f32_16x16x32_bf16 a[88:91], a[236:239], v[200:203], a[88:91]\n"
"v_mfma_f32_16x16x32_bf16 a[92:95], a[224:227], v[168:171], a[92:95]\n"
"ds_read_b128 a[224:227], v3 offset:52224\n"
"v_mfma_f32_16x16x32_bf16 a[92:95], a[228:231], v[180:183], a[92:95]\n"
"ds_read_b128 a[228:231], v3 offset:61440\n"
"v_mfma_f32_16x16x32_bf16 a[92:95], a[232:235], v[192:195], a[92:95]\n"
"ds_read_b128 a[232:235], v4 offset:5120\n"
"v_mfma_f32_16x16x32_bf16 a[92:95], a[236:239], v[204:207], a[92:95]\n"
"buffer_load_dwordx4 v[104:107], v1, s[36:39], s73 offen offset:2048\n"
"v_mfma_f32_16x16x32_bf16 a[96:99], a[240:243], v[160:163], a[96:99]\n"
"ds_read_b128 a[236:239], v4 offset:14336\n"
"v_mfma_f32_16x16x32_bf16 a[96:99], a[244:247], v[172:175], a[96:99]\n"
"v_mfma_f32_16x16x32_bf16 a[96:99], a[248:251], v[184:187], a[96:99]\n"
"v_mfma_f32_16x16x32_bf16 a[96:99], a[252:255], v[196:199], a[96:99]\n"
"v_mfma_f32_16x16x32_bf16 a[100:103], a[240:243], v[164:167], a[100:103]\n"
"buffer_load_dwordx4 v[108:111], v1, s[36:39], s73 offen offset:3072\n"
"v_mfma_f32_16x16x32_bf16 a[100:103], a[244:247], v[176:179], a[100:103]\n"
"v_mfma_f32_16x16x32_bf16 a[100:103], a[248:251], v[188:191], a[100:103]\n"
"v_mfma_f32_16x16x32_bf16 a[100:103], a[252:255], v[200:203], a[100:103]\n"
"v_mfma_f32_16x16x32_bf16 a[104:107], a[240:243], v[168:171], a[104:107]\n"
"ds_read_b128 a[240:243], v3 offset:53248\n"
"v_mfma_f32_16x16x32_bf16 a[104:107], a[244:247], v[180:183], a[104:107]\n"
"ds_read_b128 a[244:247], v3 offset:62464\n"
"v_mfma_f32_16x16x32_bf16 a[104:107], a[248:251], v[192:195], a[104:107]\n"
"ds_read_b128 a[248:251], v4 offset:6144\n"
"v_mfma_f32_16x16x32_bf16 a[104:107], a[252:255], v[204:207], a[104:107]\n"
"ds_read_b128 a[252:255], v4 offset:15360\n"
"s_waitcnt lgkmcnt(4)\n"
"v_mfma_f32_16x16x32_bf16 a[0:3], a[112:115], v[208:211], a[0:3]\n"
"s_add_u32 m0, s64, 36864\n"
"s_add_u32 s74, s57, s58\n"
"v_mfma_f32_16x16x32_bf16 a[0:3], a[116:119], v[220:223], a[0:3]\n"
"buffer_load_dwordx4 v1, s[40:43], s74 offen offset:0 lds\n"
"s_waitcnt vmcnt(27)\n"
"v_mfma_f32_16x16x32_bf16 a[0:3], a[120:123], v[232:235], a[0:3]\n"
"s_waitcnt vmcnt(24)\n"
"v_mfma_f32_16x16x32_bf16 a[0:3], a[124:127], v[244:247], a[0:3]\n"
"v_mfma_f32_16x16x32_bf16 a[4:7], a[112:115], v[212:215], a[4:7]\n"
"v_mfma_f32_16x16x32_bf16 a[4:7], a[116:119], v[224:227], a[4:7]\n"
"buffer_load_dwordx4 v1, s[40:43], s74 offen offset:1024 lds\n"
"v_mfma_f32_16x16x32_bf16 a[4:7], a[120:123], v[236:239], a[4:7]\n"
"s_waitcnt vmcnt(24)\n"
"v_mfma_f32_16x16x32_bf16 a[4:7], a[124:127], v[248:251], a[4:7]\n"
"v_mfma_f32_16x16x32_bf16 a[8:11], a[112:115], v[216:219], a[8:11]\n"
"v_mfma_f32_16x16x32_bf16 a[8:11], a[116:119], v[228:231], a[8:11]\n"
"v_mfma_f32_16x16x32_bf16 a[8:11], a[120:123], v[240:243], a[8:11]\n"
"buffer_load_dwordx4 v1, s[40:43], s74 offen offset:2048 lds\n"
"s_waitcnt vmcnt(24)\n"
"v_mfma_f32_16x16x32_bf16 a[8:11], a[124:127], v[252:255], a[8:11]\n"
"v_mfma_f32_16x16x32_bf16 a[12:15], a[128:131], v[208:211], a[12:15]\n"
"v_mfma_f32_16x16x32_bf16 a[12:15], a[132:135], v[220:223], a[12:15]\n"
"v_mfma_f32_16x16x32_bf16 a[12:15], a[136:139], v[232:235], a[12:15]\n"
"v_mfma_f32_16x16x32_bf16 a[12:15], a[140:143], v[244:247], a[12:15]\n"
"buffer_load_dwordx4 v1, s[40:43], s74 offen offset:3072 lds\n"
"v_mfma_f32_16x16x32_bf16 a[16:19], a[128:131], v[212:215], a[16:19]\n"
"v_mfma_f32_16x16x32_bf16 a[16:19], a[132:135], v[224:227], a[16:19]\n"
"v_mfma_f32_16x16x32_bf16 a[16:19], a[136:139], v[236:239], a[16:19]\n"
"v_mfma_f32_16x16x32_bf16 a[16:19], a[140:143], v[248:251], a[16:19]\n"
"v_mfma_f32_16x16x32_bf16 a[20:23], a[128:131], v[216:219], a[20:23]\n"
"s_add_u32 m0, s64, 40960\n"
"s_add_u32 s75, s57, s59\n"
"v_mfma_f32_16x16x32_bf16 a[20:23], a[132:135], v[228:231], a[20:23]\n"
"buffer_load_dwordx4 v1, s[40:43], s75 offen offset:0 lds\n"
"v_mfma_f32_16x16x32_bf16 a[20:23], a[136:139], v[240:243], a[20:23]\n"
"v_mfma_f32_16x16x32_bf16 a[20:23], a[140:143], v[252:255], a[20:23]\n"
"v_mfma_f32_16x16x32_bf16 a[24:27], a[144:147], v[208:211], a[24:27]\n"
"v_mfma_f32_16x16x32_bf16 a[24:27], a[148:151], v[220:223], a[24:27]\n"
"s_add_u32 s68, s56, 0\n"
"v_mfma_f32_16x16x32_bf16 a[24:27], a[152:155], v[232:235], a[24:27]\n"
"buffer_load_dwordx4 v[112:115], v1, s[36:39], s68 offen offset:0\n"
"v_mfma_f32_16x16x32_bf16 a[24:27], a[156:159], v[244:247], a[24:27]\n"
"v_mfma_f32_16x16x32_bf16 a[28:31], a[144:147], v[212:215], a[28:31]\n"
"v_mfma_f32_16x16x32_bf16 a[28:31], a[148:151], v[224:227], a[28:31]\n"
"v_mfma_f32_16x16x32_bf16 a[28:31], a[152:155], v[236:239], a[28:31]\n"
"buffer_load_dwordx4 v[116:119], v1, s[36:39], s68 offen offset:1024\n"
"v_mfma_f32_16x16x32_bf16 a[28:31], a[156:159], v[248:251], a[28:31]\n"
"v_mfma_f32_16x16x32_bf16 a[32:35], a[144:147], v[216:219], a[32:35]\n"
"v_mfma_f32_16x16x32_bf16 a[32:35], a[148:151], v[228:231], a[32:35]\n"
"v_mfma_f32_16x16x32_bf16 a[32:35], a[152:155], v[240:243], a[32:35]\n"
"v_mfma_f32_16x16x32_bf16 a[32:35], a[156:159], v[252:255], a[32:35]\n"
"buffer_load_dwordx4 v[120:123], v1, s[36:39], s68 offen offset:2048\n"
"v_mfma_f32_16x16x32_bf16 a[36:39], a[160:163], v[208:211], a[36:39]\n"
"v_mfma_f32_16x16x32_bf16 a[36:39], a[164:167], v[220:223], a[36:39]\n"
"v_mfma_f32_16x16x32_bf16 a[36:39], a[168:171], v[232:235], a[36:39]\n"
"v_mfma_f32_16x16x32_bf16 a[36:39], a[172:175], v[244:247], a[36:39]\n"
"v_mfma_f32_16x16x32_bf16 a[40:43], a[160:163], v[212:215], a[40:43]\n"
"buffer_load_dwordx4 v[124:127], v1, s[36:39], s68 offen offset:3072\n"
"v_mfma_f32_16x16x32_bf16 a[40:43], a[164:167], v[224:227], a[40:43]\n"
"v_mfma_f32_16x16x32_bf16 a[40:43], a[168:171], v[236:239], a[40:43]\n"
"v_mfma_f32_16x16x32_bf16 a[40:43], a[172:175], v[248:251], a[40:43]\n"
"v_mfma_f32_16x16x32_bf16 a[44:47], a[160:163], v[216:219], a[44:47]\n"
"v_mfma_f32_16x16x32_bf16 a[44:47], a[164:167], v[228:231], a[44:47]\n"
"s_add_u32 m0, s64, 41984\n"
"s_add_u32 s69, s57, s60\n"
"v_mfma_f32_16x16x32_bf16 a[44:47], a[168:171], v[240:243], a[44:47]\n"
"buffer_load_dwordx4 v1, s[40:43], s69 offen offset:0 lds\n"
"v_mfma_f32_16x16x32_bf16 a[44:47], a[172:175], v[252:255], a[44:47]\n"
"s_add_u32 s57, s57, 147456\n"
"v_mfma_f32_16x16x32_bf16 a[48:51], a[176:179], v[208:211], a[48:51]\n"
"v_mfma_f32_16x16x32_bf16 a[48:51], a[180:183], v[220:223], a[48:51]\n"
"v_mfma_f32_16x16x32_bf16 a[48:51], a[184:187], v[232:235], a[48:51]\n"
"buffer_load_dwordx4 v1, s[40:43], s69 offen offset:1024 lds\n"
"v_mfma_f32_16x16x32_bf16 a[48:51], a[188:191], v[244:247], a[48:51]\n"
"v_mfma_f32_16x16x32_bf16 a[52:55], a[176:179], v[212:215], a[52:55]\n"
"v_mfma_f32_16x16x32_bf16 a[52:55], a[180:183], v[224:227], a[52:55]\n"
"v_mfma_f32_16x16x32_bf16 a[52:55], a[184:187], v[236:239], a[52:55]\n"
"v_mfma_f32_16x16x32_bf16 a[52:55], a[188:191], v[248:251], a[52:55]\n"
"buffer_load_dwordx4 v1, s[40:43], s69 offen offset:2048 lds\n"
"v_mfma_f32_16x16x32_bf16 a[56:59], a[176:179], v[216:219], a[56:59]\n"
"v_mfma_f32_16x16x32_bf16 a[56:59], a[180:183], v[228:231], a[56:59]\n"
"v_mfma_f32_16x16x32_bf16 a[56:59], a[184:187], v[240:243], a[56:59]\n"
"v_mfma_f32_16x16x32_bf16 a[56:59], a[188:191], v[252:255], a[56:59]\n"
"v_mfma_f32_16x16x32_bf16 a[60:63], a[192:195], v[208:211], a[60:63]\n"
"buffer_load_dwordx4 v1, s[40:43], s69 offen offset:3072 lds\n"
"v_mfma_f32_16x16x32_bf16 a[60:63], a[196:199], v[220:223], a[60:63]\n"
"s_waitcnt vmcnt(21) lgkmcnt(0)\n"
"s_barrier\n"
"v_mfma_f32_16x16x32_bf16 a[60:63], a[200:203], v[232:235], a[60:63]\n"
"ds_read_b128 a[112:115], v2 offset:0\n"
"ds_read_b128 a[116:119], v2 offset:9216\n"
"v_mfma_f32_16x16x32_bf16 a[60:63], a[204:207], v[244:247], a[60:63]\n"
"ds_read_b128 a[120:123], v2 offset:18432\n"
"ds_read_b128 a[124:127], v2 offset:27648\n"
"v_mfma_f32_16x16x32_bf16 a[64:67], a[192:195], v[212:215], a[64:67]\n"
"ds_read_b128 a[128:131], v2 offset:1024\n"
"ds_read_b128 a[132:135], v2 offset:10240\n"
"v_mfma_f32_16x16x32_bf16 a[64:67], a[196:199], v[224:227], a[64:67]\n"
"s_add_u32 s70, s56, 4096\n"
"ds_read_b128 a[136:139], v2 offset:19456\n"
"v_mfma_f32_16x16x32_bf16 a[64:67], a[200:203], v[236:239], a[64:67]\n"
"buffer_load_dwordx4 v[128:131], v1, s[36:39], s70 offen offset:0\n"
"v_mfma_f32_16x16x32_bf16 a[64:67], a[204:207], v[248:251], a[64:67]\n"
"ds_read_b128 a[140:143], v2 offset:28672\n"
"v_mfma_f32_16x16x32_bf16 a[68:71], a[192:195], v[216:219], a[68:71]\n"
"v_mfma_f32_16x16x32_bf16 a[68:71], a[196:199], v[228:231], a[68:71]\n"
"v_mfma_f32_16x16x32_bf16 a[68:71], a[200:203], v[240:243], a[68:71]\n"
"buffer_load_dwordx4 v[132:135], v1, s[36:39], s70 offen offset:1024\n"
"v_mfma_f32_16x16x32_bf16 a[68:71], a[204:207], v[252:255], a[68:71]\n"
"v_mfma_f32_16x16x32_bf16 a[72:75], a[208:211], v[208:211], a[72:75]\n"
"v_mfma_f32_16x16x32_bf16 a[72:75], a[212:215], v[220:223], a[72:75]\n"
"v_mfma_f32_16x16x32_bf16 a[72:75], a[216:219], v[232:235], a[72:75]\n"
"v_mfma_f32_16x16x32_bf16 a[72:75], a[220:223], v[244:247], a[72:75]\n"
"buffer_load_dwordx4 v[136:139], v1, s[36:39], s70 offen offset:2048\n"
"v_mfma_f32_16x16x32_bf16 a[76:79], a[208:211], v[212:215], a[76:79]\n"
"v_mfma_f32_16x16x32_bf16 a[76:79], a[212:215], v[224:227], a[76:79]\n"
"v_mfma_f32_16x16x32_bf16 a[76:79], a[216:219], v[236:239], a[76:79]\n"
"v_mfma_f32_16x16x32_bf16 a[76:79], a[220:223], v[248:251], a[76:79]\n"
"v_mfma_f32_16x16x32_bf16 a[80:83], a[208:211], v[216:219], a[80:83]\n"
"buffer_load_dwordx4 v[140:143], v1, s[36:39], s70 offen offset:3072\n"
"v_mfma_f32_16x16x32_bf16 a[80:83], a[212:215], v[228:231], a[80:83]\n"
"v_mfma_f32_16x16x32_bf16 a[80:83], a[216:219], v[240:243], a[80:83]\n"
"v_mfma_f32_16x16x32_bf16 a[80:83], a[220:223], v[252:255], a[80:83]\n"
"v_mfma_f32_16x16x32_bf16 a[84:87], a[224:227], v[208:211], a[84:87]\n"
"v_mfma_f32_16x16x32_bf16 a[84:87], a[228:231], v[220:223], a[84:87]\n"
"s_add_u32 s71, s56, 8192\n"
"s_add_u32 s56, s56, 12288\n"
"v_mfma_f32_16x16x32_bf16 a[84:87], a[232:235], v[232:235], a[84:87]\n"
"buffer_load_dwordx4 v[144:147], v1, s[36:39], s71 offen offset:0\n"
"v_mfma_f32_16x16x32_bf16 a[84:87], a[236:239], v[244:247], a[84:87]\n"
"v_mfma_f32_16x16x32_bf16 a[88:91], a[224:227], v[212:215], a[88:91]\n"
"v_mfma_f32_16x16x32_bf16 a[88:91], a[228:231], v[224:227], a[88:91]\n"
"v_mfma_f32_16x16x32_bf16 a[88:91], a[232:235], v[236:239], a[88:91]\n"
"buffer_load_dwordx4 v[148:151], v1, s[36:39], s71 offen offset:1024\n"
"v_mfma_f32_16x16x32_bf16 a[88:91], a[236:239], v[248:251], a[88:91]\n"
"v_mfma_f32_16x16x32_bf16 a[92:95], a[224:227], v[216:219], a[92:95]\n"
"v_mfma_f32_16x16x32_bf16 a[92:95], a[228:231], v[228:231], a[92:95]\n"
"v_mfma_f32_16x16x32_bf16 a[92:95], a[232:235], v[240:243], a[92:95]\n"
"v_mfma_f32_16x16x32_bf16 a[92:95], a[236:239], v[252:255], a[92:95]\n"
"buffer_load_dwordx4 v[152:155], v1, s[36:39], s71 offen offset:2048\n"
"v_mfma_f32_16x16x32_bf16 a[96:99], a[240:243], v[208:211], a[96:99]\n"
"v_mfma_f32_16x16x32_bf16 a[96:99], a[244:247], v[220:223], a[96:99]\n"
"v_mfma_f32_16x16x32_bf16 a[96:99], a[248:251], v[232:235], a[96:99]\n"
"v_mfma_f32_16x16x32_bf16 a[96:99], a[252:255], v[244:247], a[96:99]\n"
"v_mfma_f32_16x16x32_bf16 a[100:103], a[240:243], v[212:215], a[100:103]\n"
"buffer_load_dwordx4 v[156:159], v1, s[36:39], s71 offen offset:3072\n"
"v_mfma_f32_16x16x32_bf16 a[100:103], a[244:247], v[224:227], a[100:103]\n"
"v_mfma_f32_16x16x32_bf16 a[100:103], a[248:251], v[236:239], a[100:103]\n"
"v_mfma_f32_16x16x32_bf16 a[100:103], a[252:255], v[248:251], a[100:103]\n"
"v_mfma_f32_16x16x32_bf16 a[104:107], a[240:243], v[216:219], a[104:107]\n"
"v_mfma_f32_16x16x32_bf16 a[104:107], a[244:247], v[228:231], a[104:107]\n"
"v_mfma_f32_16x16x32_bf16 a[104:107], a[248:251], v[240:243], a[104:107]\n"
"v_mfma_f32_16x16x32_bf16 a[104:107], a[252:255], v[252:255], a[104:107]\n"
"s_sub_u32 s65, s65, 1\n"
"s_cmp_lg_u32 s65, 0\n"
"s_waitcnt lgkmcnt(0)\n"
"s_cbranch_scc1 LOOP_%=\n"
"v_mfma_f32_16x16x32_bf16 a[0:3], a[112:115], v[64:67], a[0:3]\n"
"s_add_u32 m0, s64, 73728\n"
"s_add_u32 s72, s57, s58\n"
"v_mfma_f32_16x16x32_bf16 a[0:3], a[116:119], v[76:79], a[0:3]\n"
"buffer_load_dwordx4 v1, s[40:43], s72 offen offset:0 lds\n"
"s_waitcnt vmcnt(27)\n"
"v_mfma_f32_16x16x32_bf16 a[0:3], a[120:123], v[88:91], a[0:3]\n"
"ds_read_b128 a[144:147], v2 offset:2048\n"
"ds_read_b128 a[148:151], v2 offset:11264\n"
"s_waitcnt vmcnt(24)\n"
"v_mfma_f32_16x16x32_bf16 a[0:3], a[124:127], v[100:103], a[0:3]\n"
"buffer_load_dwordx4 v1, s[40:43], s72 offen offset:1024 lds\n"
"v_mfma_f32_16x16x32_bf16 a[4:7], a[112:115], v[68:71], a[4:7]\n"
"ds_read_b128 a[152:155], v2 offset:20480\n"
"ds_read_b128 a[156:159], v2 offset:29696\n"
"v_mfma_f32_16x16x32_bf16 a[4:7], a[116:119], v[80:83], a[4:7]\n"
"ds_read_b128 a[160:163], v2 offset:3072\n"
"ds_read_b128 a[164:167], v2 offset:12288\n"
"v_mfma_f32_16x16x32_bf16 a[4:7], a[120:123], v[92:95], a[4:7]\n"
"ds_read_b128 a[168:171], v2 offset:21504\n"
"ds_read_b128 a[172:175], v2 offset:30720\n"
"s_waitcnt vmcnt(24)\n"
"v_mfma_f32_16x16x32_bf16 a[4:7], a[124:127], v[104:107], a[4:7]\n"
"buffer_load_dwordx4 v1, s[40:43], s72 offen offset:2048 lds\n"
"v_mfma_f32_16x16x32_bf16 a[8:11], a[112:115], v[72:75], a[8:11]\n"
"ds_read_b128 a[176:179], v2 offset:4096\n"
"ds_read_b128 a[180:183], v2 offset:13312\n"
"v_mfma_f32_16x16x32_bf16 a[8:11], a[116:119], v[84:87], a[8:11]\n"
"ds_read_b128 a[184:187], v2 offset:22528\n"
"ds_read_b128 a[188:191], v2 offset:31744\n"
"v_mfma_f32_16x16x32_bf16 a[8:11], a[120:123], v[96:99], a[8:11]\n"
"ds_read_b128 a[192:195], v2 offset:5120\n"
"ds_read_b128 a[196:199], v2 offset:14336\n"
"s_waitcnt vmcnt(24)\n"
"v_mfma_f32_16x16x32_bf16 a[8:11], a[124:127], v[108:111], a[8:11]\n"
"buffer_load_dwordx4 v1, s[40:43], s72 offen offset:3072 lds\n"
"v_mfma_f32_16x16x32_bf16 a[12:15], a[128:131], v[64:67], a[12:15]\n"
"ds_read_b128 a[200:203], v2 offset:23552\n"
"ds_read_b128 a[204:207], v2 offset:32768\n"
"v_mfma_f32_16x16x32_bf16 a[12:15], a[132:135], v[76:79], a[12:15]\n"
"ds_read_b128 a[208:211], v2 offset:6144\n"
"ds_read_b128 a[212:215], v2 offset:15360\n"
"v_mfma_f32_16x16x32_bf16 a[12:15], a[136:139], v[88:91], a[12:15]\n"
"s_add_u32 m0, s64, 77824\n"
"s_add_u32 s73, s57, s59\n"
"v_mfma_f32_16x16x32_bf16 a[12:15], a[140:143], v[100:103], a[12:15]\n"
"buffer_load_dwordx4 v1, s[40:43], s73 offen offset:0 lds\n"
"v_mfma_f32_16x16x32_bf16 a[16:19], a[128:131], v[68:71], a[16:19]\n"
"ds_read_b128 a[216:219], v2 offset:24576\n"
"ds_read_b128 a[220:223], v2 offset:33792\n"
"v_mfma_f32_16x16x32_bf16 a[16:19], a[132:135], v[80:83], a[16:19]\n"
"ds_read_b128 a[224:227], v2 offset:7168\n"
"ds_read_b128 a[228:231], v2 offset:16384\n"
"v_mfma_f32_16x16x32_bf16 a[16:19], a[136:139], v[92:95], a[16:19]\n"
"s_add_u32 s74, s56, 0\n"
"ds_read_b128 a[232:235], v2 offset:25600\n"
"v_mfma_f32_16x16x32_bf16 a[16:19], a[140:143], v[104:107], a[16:19]\n"
"buffer_load_dwordx4 v[160:163], v1, s[36:39], s74 offen offset:0\n"
"v_mfma_f32_16x16x32_bf16 a[20:23], a[128:131], v[72:75], a[20:23]\n"
"ds_read_b128 a[236:239], v2 offset:34816\n"
"ds_read_b128 a[240:243], v2 offset:8192\n"
"v_mfma_f32_16x16x32_bf16 a[20:23], a[132:135], v[84:87], a[20:23]\n"
"ds_read_b128 a[244:247], v2 offset:17408\n"
"ds_read_b128 a[248:251], v2 offset:26624\n"
"v_mfma_f32_16x16x32_bf16 a[20:23], a[136:139], v[96:99], a[20:23]\n"
"buffer_load_dwordx4 v[164:167], v1, s[36:39], s74 offen offset:1024\n"
"v_mfma_f32_16x16x32_bf16 a[20:23], a[140:143], v[108:111], a[20:23]\n"
"ds_read_b128 a[252:255], v2 offset:35840\n"
"s_waitcnt lgkmcnt(10)\n"
"v_mfma_f32_16x16x32_bf16 a[24:27], a[144:147], v[64:67], a[24:27]\n"
"v_mfma_f32_16x16x32_bf16 a[24:27], a[148:151], v[76:79], a[24:27]\n"
"v_mfma_f32_16x16x32_bf16 a[24:27], a[152:155], v[88:91], a[24:27]\n"
"buffer_load_dwordx4 v[168:171], v1, s[36:39], s74 offen offset:2048\n"
"v_mfma_f32_16x16x32_bf16 a[24:27], a[156:159], v[100:103], a[24:27]\n"
"v_mfma_f32_16x16x32_bf16 a[28:31], a[144:147], v[68:71], a[28:31]\n"
"v_mfma_f32_16x16x32_bf16 a[28:31], a[148:151], v[80:83], a[28:31]\n"
"buffer_load_dwordx4 v[172:175], v1, s[36:39], s74 offen offset:3072\n"
"v_mfma_f32_16x16x32_bf16 a[28:31], a[152:155], v[92:95], a[28:31]\n"
"v_mfma_f32_16x16x32_bf16 a[28:31], a[156:159], v[104:107], a[28:31]\n"
"v_mfma_f32_16x16x32_bf16 a[32:35], a[144:147], v[72:75], a[32:35]\n"
"v_mfma_f32_16x16x32_bf16 a[32:35], a[148:151], v[84:87], a[32:35]\n"
"s_add_u32 m0, s64, 78848\n"
"s_add_u32 s75, s57, s60\n"
"v_mfma_f32_16x16x32_bf16 a[32:35], a[152:155], v[96:99], a[32:35]\n"
"buffer_load_dwordx4 v1, s[40:43], s75 offen offset:0 lds\n"
"v_mfma_f32_16x16x32_bf16 a[32:35], a[156:159], v[108:111], a[32:35]\n"
"s_add_u32 s57, s57, 147456\n"
"v_mfma_f32_16x16x32_bf16 a[36:39], a[160:163], v[64:67], a[36:39]\n"
"v_mfma_f32_16x16x32_bf16 a[36:39], a[164:167], v[76:79], a[36:39]\n"
"buffer_load_dwordx4 v1, s[40:43], s75 offen offset:1024 lds\n"
"v_mfma_f32_16x16x32_bf16 a[36:39], a[168:171], v[88:91], a[36:39]\n"
"v_mfma_f32_16x16x32_bf16 a[36:39], a[172:175], v[100:103], a[36:39]\n"
"v_mfma_f32_16x16x32_bf16 a[40:43], a[160:163], v[68:71], a[40:43]\n"
"buffer_load_dwordx4 v1, s[40:43], s75 offen offset:2048 lds\n"
"v_mfma_f32_16x16x32_bf16 a[40:43], a[164:167], v[80:83], a[40:43]\n"
"v_mfma_f32_16x16x32_bf16 a[40:43], a[168:171], v[92:95], a[40:43]\n"
"v_mfma_f32_16x16x32_bf16 a[40:43], a[172:175], v[104:107], a[40:43]\n"
"v_mfma_f32_16x16x32_bf16 a[44:47], a[160:163], v[72:75], a[44:47]\n"
"buffer_load_dwordx4 v1, s[40:43], s75 offen offset:3072 lds\n"
"v_mfma_f32_16x16x32_bf16 a[44:47], a[164:167], v[84:87], a[44:47]\n"
"v_mfma_f32_16x16x32_bf16 a[44:47], a[168:171], v[96:99], a[44:47]\n"
"v_mfma_f32_16x16x32_bf16 a[44:47], a[172:175], v[108:111], a[44:47]\n"
"v_mfma_f32_16x16x32_bf16 a[48:51], a[176:179], v[64:67], a[48:51]\n"
"s_add_u32 s68, s56, 4096\n"
"v_mfma_f32_16x16x32_bf16 a[48:51], a[180:183], v[76:79], a[48:51]\n"
"buffer_load_dwordx4 v[176:179], v1, s[36:39], s68 offen offset:0\n"
"v_mfma_f32_16x16x32_bf16 a[48:51], a[184:187], v[88:91], a[48:51]\n"
"v_mfma_f32_16x16x32_bf16 a[48:51], a[188:191], v[100:103], a[48:51]\n"
"v_mfma_f32_16x16x32_bf16 a[52:55], a[176:179], v[68:71], a[52:55]\n"
"buffer_load_dwordx4 v[180:183], v1, s[36:39], s68 offen offset:1024\n"
"v_mfma_f32_16x16x32_bf16 a[52:55], a[180:183], v[80:83], a[52:55]\n"
"v_mfma_f32_16x16x32_bf16 a[52:55], a[184:187], v[92:95], a[52:55]\n"
"v_mfma_f32_16x16x32_bf16 a[52:55], a[188:191], v[104:107], a[52:55]\n"
"buffer_load_dwordx4 v[184:187], v1, s[36:39], s68 offen offset:2048\n"
"v_mfma_f32_16x16x32_bf16 a[56:59], a[176:179], v[72:75], a[56:59]\n"
"v_mfma_f32_16x16x32_bf16 a[56:59], a[180:183], v[84:87], a[56:59]\n"
"v_mfma_f32_16x16x32_bf16 a[56:59], a[184:187], v[96:99], a[56:59]\n"
"v_mfma_f32_16x16x32_bf16 a[56:59], a[188:191], v[108:111], a[56:59]\n"
"buffer_load_dwordx4 v[188:191], v1, s[36:39], s68 offen offset:3072\n"
"v_mfma_f32_16x16x32_bf16 a[60:63], a[192:195], v[64:67], a[60:63]\n"
"v_mfma_f32_16x16x32_bf16 a[60:63], a[196:199], v[76:79], a[60:63]\n"
"s_waitcnt vmcnt(25) lgkmcnt(0)\n"
"s_barrier\n"
"v_mfma_f32_16x16x32_bf16 a[60:63], a[200:203], v[88:91], a[60:63]\n"
"ds_read_b128 a[112:115], v2 offset:36864\n"
"ds_read_b128 a[116:119], v2 offset:46080\n"
"v_mfma_f32_16x16x32_bf16 a[60:63], a[204:207], v[100:103], a[60:63]\n"
"s_add_u32 s69, s56, 8192\n"
"ds_read_b128 a[120:123], v2 offset:55296\n"
"v_mfma_f32_16x16x32_bf16 a[64:67], a[192:195], v[68:71], a[64:67]\n"
"buffer_load_dwordx4 v[192:195], v1, s[36:39], s69 offen offset:0\n"
"v_mfma_f32_16x16x32_bf16 a[64:67], a[196:199], v[80:83], a[64:67]\n"
"ds_read_b128 a[124:127], v2 offset:64512\n"
"s_add_u32 s56, s56, 12288\n"
"v_mfma_f32_16x16x32_bf16 a[64:67], a[200:203], v[92:95], a[64:67]\n"
"buffer_load_dwordx4 v[196:199], v1, s[36:39], s69 offen offset:1024\n"
"v_mfma_f32_16x16x32_bf16 a[64:67], a[204:207], v[104:107], a[64:67]\n"
"ds_read_b128 a[128:131], v2 offset:37888\n"
"ds_read_b128 a[132:135], v2 offset:47104\n"
"v_mfma_f32_16x16x32_bf16 a[68:71], a[192:195], v[72:75], a[68:71]\n"
"ds_read_b128 a[136:139], v2 offset:56320\n"
"ds_read_b128 a[140:143], v3 offset:0\n"
"v_mfma_f32_16x16x32_bf16 a[68:71], a[196:199], v[84:87], a[68:71]\n"
"ds_read_b128 a[144:147], v2 offset:38912\n"
"ds_read_b128 a[148:151], v2 offset:48128\n"
"v_mfma_f32_16x16x32_bf16 a[68:71], a[200:203], v[96:99], a[68:71]\n"
"buffer_load_dwordx4 v[200:203], v1, s[36:39], s69 offen offset:2048\n"
"v_mfma_f32_16x16x32_bf16 a[68:71], a[204:207], v[108:111], a[68:71]\n"
"ds_read_b128 a[152:155], v2 offset:57344\n"
"ds_read_b128 a[156:159], v3 offset:1024\n"
"v_mfma_f32_16x16x32_bf16 a[72:75], a[208:211], v[64:67], a[72:75]\n"
"ds_read_b128 a[160:163], v2 offset:39936\n"
"ds_read_b128 a[164:167], v2 offset:49152\n"
"v_mfma_f32_16x16x32_bf16 a[72:75], a[212:215], v[76:79], a[72:75]\n"
"ds_read_b128 a[168:171], v2 offset:58368\n"
"ds_read_b128 a[172:175], v3 offset:2048\n"
"v_mfma_f32_16x16x32_bf16 a[72:75], a[216:219], v[88:91], a[72:75]\n"
"buffer_load_dwordx4 v[204:207], v1, s[36:39], s69 offen offset:3072\n"
"v_mfma_f32_16x16x32_bf16 a[72:75], a[220:223], v[100:103], a[72:75]\n"
"ds_read_b128 a[176:179], v2 offset:40960\n"
"ds_read_b128 a[180:183], v2 offset:50176\n"
"v_mfma_f32_16x16x32_bf16 a[76:79], a[208:211], v[68:71], a[76:79]\n"
"ds_read_b128 a[184:187], v2 offset:59392\n"
"ds_read_b128 a[188:191], v3 offset:3072\n"
"v_mfma_f32_16x16x32_bf16 a[76:79], a[212:215], v[80:83], a[76:79]\n"
"ds_read_b128 a[192:195], v2 offset:41984\n"
"ds_read_b128 a[196:199], v2 offset:51200\n"
"v_mfma_f32_16x16x32_bf16 a[76:79], a[216:219], v[92:95], a[76:79]\n"
"ds_read_b128 a[200:203], v2 offset:60416\n"
"ds_read_b128 a[204:207], v3 offset:4096\n"
"v_mfma_f32_16x16x32_bf16 a[76:79], a[220:223], v[104:107], a[76:79]\n"
"v_mfma_f32_16x16x32_bf16 a[80:83], a[208:211], v[72:75], a[80:83]\n"
"ds_read_b128 a[208:211], v2 offset:43008\n"
"v_mfma_f32_16x16x32_bf16 a[80:83], a[212:215], v[84:87], a[80:83]\n"
"s_add_u32 m0, s64, 110592\n"
"s_add_u32 s70, s57, s58\n"
"v_mfma_f32_16x16x32_bf16 a[80:83], a[216:219], v[96:99], a[80:83]\n"
"buffer_load_dwordx4 v1, s[40:43], s70 offen offset:0 lds\n"
"v_mfma_f32_16x16x32_bf16 a[80:83], a[220:223], v[108:111], a[80:83]\n"
"ds_read_b128 a[212:215], v2 offset:52224\n"
"ds_read_b128 a[216:219], v2 offset:61440\n"
"v_mfma_f32_16x16x32_bf16 a[84:87], a[224:227], v[64:67], a[84:87]\n"
"buffer_load_dwordx4 v1, s[40:43], s70 offen offset:1024 lds\n"
"v_mfma_f32_16x16x32_bf16 a[84:87], a[228:231], v[76:79], a[84:87]\n"
"ds_read_b128 a[220:223], v3 offset:5120\n"
"v_mfma_f32_16x16x32_bf16 a[84:87], a[232:235], v[88:91], a[84:87]\n"
"v_mfma_f32_16x16x32_bf16 a[84:87], a[236:239], v[100:103], a[84:87]\n"
"v_mfma_f32_16x16x32_bf16 a[88:91], a[224:227], v[68:71], a[88:91]\n"
"buffer_load_dwordx4 v1, s[40:43], s70 offen offset:2048 lds\n"
"v_mfma_f32_16x16x32_bf16 a[88:91], a[228:231], v[80:83], a[88:91]\n"
"v_mfma_f32_16x16x32_bf16 a[88:91], a[232:235], v[92:95], a[88:91]\n"
"v_mfma_f32_16x16x32_bf16 a[88:91], a[236:239], v[104:107], a[88:91]\n"
"v_mfma_f32_16x16x32_bf16 a[92:95], a[224:227], v[72:75], a[92:95]\n"
"buffer_load_dwordx4 v1, s[40:43], s70 offen offset:3072 lds\n"
"v_mfma_f32_16x16x32_bf16 a[92:95], a[228:231], v[84:87], a[92:95]\n"
"ds_read_b128 a[224:227], v2 offset:44032\n"
"ds_read_b128 a[228:231], v2 offset:53248\n"
"v_mfma_f32_16x16x32_bf16 a[92:95], a[232:235], v[96:99], a[92:95]\n"
"ds_read_b128 a[232:235], v2 offset:62464\n"
"v_mfma_f32_16x16x32_bf16 a[92:95], a[236:239], v[108:111], a[92:95]\n"
"s_add_u32 m0, s64, 114688\n"
"s_add_u32 s71, s57, s59\n"
"v_mfma_f32_16x16x32_bf16 a[96:99], a[240:243], v[64:67], a[96:99]\n"
"buffer_load_dwordx4 v1, s[40:43], s71 offen offset:0 lds\n"
"v_mfma_f32_16x16x32_bf16 a[96:99], a[244:247], v[76:79], a[96:99]\n"
"ds_read_b128 a[236:239], v3 offset:6144\n"
"v_mfma_f32_16x16x32_bf16 a[96:99], a[248:251], v[88:91], a[96:99]\n"
"v_mfma_f32_16x16x32_bf16 a[96:99], a[252:255], v[100:103], a[96:99]\n"
"s_add_u32 s72, s56, 0\n"
"v_mfma_f32_16x16x32_bf16 a[100:103], a[240:243], v[68:71], a[100:103]\n"
"buffer_load_dwordx4 v[208:211], v1, s[36:39], s72 offen offset:0\n"
"v_mfma_f32_16x16x32_bf16 a[100:103], a[244:247], v[80:83], a[100:103]\n"
"v_mfma_f32_16x16x32_bf16 a[100:103], a[248:251], v[92:95], a[100:103]\n"
"v_mfma_f32_16x16x32_bf16 a[100:103], a[252:255], v[104:107], a[100:103]\n"
"buffer_load_dwordx4 v[212:215], v1, s[36:39], s72 offen offset:1024\n"
"v_mfma_f32_16x16x32_bf16 a[104:107], a[240:243], v[72:75], a[104:107]\n"
"ds_read_b128 a[240:243], v2 offset:45056\n"
"v_mfma_f32_16x16x32_bf16 a[104:107], a[244:247], v[84:87], a[104:107]\n"
"ds_read_b128 a[244:247], v2 offset:54272\n"
"v_mfma_f32_16x16x32_bf16 a[104:107], a[248:251], v[96:99], a[104:107]\n"
"ds_read_b128 a[248:251], v2 offset:63488\n"
"v_mfma_f32_16x16x32_bf16 a[104:107], a[252:255], v[108:111], a[104:107]\n"
"ds_read_b128 a[252:255], v3 offset:7168\n"
"s_waitcnt lgkmcnt(4)\n"
"v_mfma_f32_16x16x32_bf16 a[0:3], a[112:115], v[112:115], a[0:3]\n"
"buffer_load_dwordx4 v[216:219], v1, s[36:39], s72 offen offset:2048\n"
"v_mfma_f32_16x16x32_bf16 a[0:3], a[116:119], v[124:127], a[0:3]\n"
"s_waitcnt vmcnt(34)\n"
"v_mfma_f32_16x16x32_bf16 a[0:3], a[120:123], v[136:139], a[0:3]\n"
"buffer_load_dwordx4 v[220:223], v1, s[36:39], s72 offen offset:3072\n"
"s_waitcnt vmcnt(32)\n"
"v_mfma_f32_16x16x32_bf16 a[0:3], a[124:127], v[148:151], a[0:3]\n"
"v_mfma_f32_16x16x32_bf16 a[4:7], a[112:115], v[116:119], a[4:7]\n"
"v_mfma_f32_16x16x32_bf16 a[4:7], a[116:119], v[128:131], a[4:7]\n"
"v_mfma_f32_16x16x32_bf16 a[4:7], a[120:123], v[140:143], a[4:7]\n"
"s_add_u32 m0, s64, 115712\n"
"s_add_u32 s73, s57, s60\n"
"s_waitcnt vmcnt(31)\n"
"v_mfma_f32_16x16x32_bf16 a[4:7], a[124:127], v[152:155], a[4:7]\n"
"buffer_load_dwordx4 v1, s[40:43], s73 offen offset:0 lds\n"
"v_mfma_f32_16x16x32_bf16 a[8:11], a[112:115], v[120:123], a[8:11]\n"
"s_add_u32 s57, s57, 147456\n"
"v_mfma_f32_16x16x32_bf16 a[8:11], a[116:119], v[132:135], a[8:11]\n"
"v_mfma_f32_16x16x32_bf16 a[8:11], a[120:123], v[144:147], a[8:11]\n"
"buffer_load_dwordx4 v1, s[40:43], s73 offen offset:1024 lds\n"
"s_waitcnt vmcnt(32)\n"
"v_mfma_f32_16x16x32_bf16 a[8:11], a[124:127], v[156:159], a[8:11]\n"
"v_mfma_f32_16x16x32_bf16 a[12:15], a[128:131], v[112:115], a[12:15]\n"
"v_mfma_f32_16x16x32_bf16 a[12:15], a[132:135], v[124:127], a[12:15]\n"
"buffer_load_dwordx4 v1, s[40:43], s73 offen offset:2048 lds\n"
"v_mfma_f32_16x16x32_bf16 a[12:15], a[136:139], v[136:139], a[12:15]\n"
"v_mfma_f32_16x16x32_bf16 a[12:15], a[140:143], v[148:151], a[12:15]\n"
"v_mfma_f32_16x16x32_bf16 a[16:19], a[128:131], v[116:119], a[16:19]\n"
"v_mfma_f32_16x16x32_bf16 a[16:19], a[132:135], v[128:131], a[16:19]\n"
"buffer_load_dwordx4 v1, s[40:43], s73 offen offset:3072 lds\n"
"v_mfma_f32_16x16x32_bf16 a[16:19], a[136:139], v[140:143], a[16:19]\n"
"v_mfma_f32_16x16x32_bf16 a[16:19], a[140:143], v[152:155], a[16:19]\n"
"v_mfma_f32_16x16x32_bf16 a[20:23], a[128:131], v[120:123], a[20:23]\n"
"v_mfma_f32_16x16x32_bf16 a[20:23], a[132:135], v[132:135], a[20:23]\n"
"s_add_u32 s74, s56, 4096\n"
"v_mfma_f32_16x16x32_bf16 a[20:23], a[136:139], v[144:147], a[20:23]\n"
"buffer_load_dwordx4 v[224:227], v1, s[36:39], s74 offen offset:0\n"
"v_mfma_f32_16x16x32_bf16 a[20:23], a[140:143], v[156:159], a[20:23]\n"
"v_mfma_f32_16x16x32_bf16 a[24:27], a[144:147], v[112:115], a[24:27]\n"
"v_mfma_f32_16x16x32_bf16 a[24:27], a[148:151], v[124:127], a[24:27]\n"
"buffer_load_dwordx4 v[228:231], v1, s[36:39], s74 offen offset:1024\n"
"v_mfma_f32_16x16x32_bf16 a[24:27], a[152:155], v[136:139], a[24:27]\n"
"v_mfma_f32_16x16x32_bf16 a[24:27], a[156:159], v[148:151], a[24:27]\n"
"v_mfma_f32_16x16x32_bf16 a[28:31], a[144:147], v[116:119], a[28:31]\n"
"buffer_load_dwordx4 v[232:235], v1, s[36:39], s74 offen offset:2048\n"
"v_mfma_f32_16x16x32_bf16 a[28:31], a[148:151], v[128:131], a[28:31]\n"
"v_mfma_f32_16x16x32_bf16 a[28:31], a[152:155], v[140:143], a[28:31]\n"
"v_mfma_f32_16x16x32_bf16 a[28:31], a[156:159], v[152:155], a[28:31]\n"
"v_mfma_f32_16x16x32_bf16 a[32:35], a[144:147], v[120:123], a[32:35]\n"
"buffer_load_dwordx4 v[236:239], v1, s[36:39], s74 offen offset:3072\n"
"v_mfma_f32_16x16x32_bf16 a[32:35], a[148:151], v[132:135], a[32:35]\n"
"v_mfma_f32_16x16x32_bf16 a[32:35], a[152:155], v[144:147], a[32:35]\n"
"v_mfma_f32_16x16x32_bf16 a[32:35], a[156:159], v[156:159], a[32:35]\n"
"v_mfma_f32_16x16x32_bf16 a[36:39], a[160:163], v[112:115], a[36:39]\n"
"s_add_u32 s75, s56, 8192\n"
"s_add_u32 s56, s56, 12288\n"
"v_mfma_f32_16x16x32_bf16 a[36:39], a[164:167], v[124:127], a[36:39]\n"
"buffer_load_dwordx4 v[240:243], v1, s[36:39], s75 offen offset:0\n"
"v_mfma_f32_16x16x32_bf16 a[36:39], a[168:171], v[136:139], a[36:39]\n"
"v_mfma_f32_16x16x32_bf16 a[36:39], a[172:175], v[148:151], a[36:39]\n"
"buffer_load_dwordx4 v[244:247], v1, s[36:39], s75 offen offset:1024\n"
"v_mfma_f32_16x16x32_bf16 a[40:43], a[160:163], v[116:119], a[40:43]\n"
"v_mfma_f32_16x16x32_bf16 a[40:43], a[164:167], v[128:131], a[40:43]\n"
"v_mfma_f32_16x16x32_bf16 a[40:43], a[168:171], v[140:143], a[40:43]\n"
"v_mfma_f32_16x16x32_bf16 a[40:43], a[172:175], v[152:155], a[40:43]\n"
"buffer_load_dwordx4 v[248:251], v1, s[36:39], s75 offen offset:2048\n"
"v_mfma_f32_16x16x32_bf16 a[44:47], a[160:163], v[120:123], a[44:47]\n"
"v_mfma_f32_16x16x32_bf16 a[44:47], a[164:167], v[132:135], a[44:47]\n"
"v_mfma_f32_16x16x32_bf16 a[44:47], a[168:171], v[144:147], a[44:47]\n"
"v_mfma_f32_16x16x32_bf16 a[44:47], a[172:175], v[156:159], a[44:47]\n"
"buffer_load_dwordx4 v[252:255], v1, s[36:39], s75 offen offset:3072\n"
"v_mfma_f32_16x16x32_bf16 a[48:51], a[176:179], v[112:115], a[48:51]\n"
"v_mfma_f32_16x16x32_bf16 a[48:51], a[180:183], v[124:127], a[48:51]\n"
"v_mfma_f32_16x16x32_bf16 a[48:51], a[184:187], v[136:139], a[48:51]\n"
"v_mfma_f32_16x16x32_bf16 a[48:51], a[188:191], v[148:151], a[48:51]\n"
"v_mfma_f32_16x16x32_bf16 a[52:55], a[176:179], v[116:119], a[52:55]\n"
"v_mfma_f32_16x16x32_bf16 a[52:55], a[180:183], v[128:131], a[52:55]\n"
"v_mfma_f32_16x16x32_bf16 a[52:55], a[184:187], v[140:143], a[52:55]\n"
"v_mfma_f32_16x16x32_bf16 a[52:55], a[188:191], v[152:155], a[52:55]\n"
"v_mfma_f32_16x16x32_bf16 a[56:59], a[176:179], v[120:123], a[56:59]\n"
"v_mfma_f32_16x16x32_bf16 a[56:59], a[180:183], v[132:135], a[56:59]\n"
"v_mfma_f32_16x16x32_bf16 a[56:59], a[184:187], v[144:147], a[56:59]\n"
"v_mfma_f32_16x16x32_bf16 a[56:59], a[188:191], v[156:159], a[56:59]\n"
"v_mfma_f32_16x16x32_bf16 a[60:63], a[192:195], v[112:115], a[60:63]\n"
"v_mfma_f32_16x16x32_bf16 a[60:63], a[196:199], v[124:127], a[60:63]\n"
"v_mfma_f32_16x16x32_bf16 a[60:63], a[200:203], v[136:139], a[60:63]\n"
"v_mfma_f32_16x16x32_bf16 a[60:63], a[204:207], v[148:151], a[60:63]\n"
"v_mfma_f32_16x16x32_bf16 a[64:67], a[192:195], v[116:119], a[64:67]\n"
"v_mfma_f32_16x16x32_bf16 a[64:67], a[196:199], v[128:131], a[64:67]\n"
"v_mfma_f32_16x16x32_bf16 a[64:67], a[200:203], v[140:143], a[64:67]\n"
"v_mfma_f32_16x16x32_bf16 a[64:67], a[204:207], v[152:155], a[64:67]\n"
"v_mfma_f32_16x16x32_bf16 a[68:71], a[192:195], v[120:123], a[68:71]\n"
"v_mfma_f32_16x16x32_bf16 a[68:71], a[196:199], v[132:135], a[68:71]\n"
"v_mfma_f32_16x16x32_bf16 a[68:71], a[200:203], v[144:147], a[68:71]\n"
"v_mfma_f32_16x16x32_bf16 a[68:71], a[204:207], v[156:159], a[68:71]\n"
"v_mfma_f32_16x16x32_bf16 a[72:75], a[208:211], v[112:115], a[72:75]\n"
"v_mfma_f32_16x16x32_bf16 a[72:75], a[212:215], v[124:127], a[72:75]\n"
"v_mfma_f32_16x16x32_bf16 a[72:75], a[216:219], v[136:139], a[72:75]\n"
"v_mfma_f32_16x16x32_bf16 a[72:75], a[220:223], v[148:151], a[72:75]\n"
"v_mfma_f32_16x16x32_bf16 a[76:79], a[208:211], v[116:119], a[76:79]\n"
"v_mfma_f32_16x16x32_bf16 a[76:79], a[212:215], v[128:131], a[76:79]\n"
"s_waitcnt vmcnt(29)\n"
"s_barrier\n"
"v_mfma_f32_16x16x32_bf16 a[76:79], a[216:219], v[140:143], a[76:79]\n"
"ds_read_b128 a[112:115], v3 offset:8192\n"
"ds_read_b128 a[116:119], v3 offset:17408\n"
"v_mfma_f32_16x16x32_bf16 a[76:79], a[220:223], v[152:155], a[76:79]\n"
"ds_read_b128 a[120:123], v3 offset:26624\n"
"ds_read_b128 a[124:127], v3 offset:35840\n"
"v_mfma_f32_16x16x32_bf16 a[80:83], a[208:211], v[120:123], a[80:83]\n"
"ds_read_b128 a[128:131], v3 offset:9216\n"
"ds_read_b128 a[132:135], v3 offset:18432\n"
"v_mfma_f32_16x16x32_bf16 a[80:83], a[212:215], v[132:135], a[80:83]\n"
"ds_read_b128 a[136:139], v3 offset:27648\n"
"ds_read_b128 a[140:143], v3 offset:36864\n"
"v_mfma_f32_16x16x32_bf16 a[80:83], a[216:219], v[144:147], a[80:83]\n"
"ds_read_b128 a[144:147], v3 offset:10240\n"
"ds_read_b128 a[148:151], v3 offset:19456\n"
"v_mfma_f32_16x16x32_bf16 a[80:83], a[220:223], v[156:159], a[80:83]\n"
"ds_read_b128 a[152:155], v3 offset:28672\n"
"ds_read_b128 a[156:159], v3 offset:37888\n"
"v_mfma_f32_16x16x32_bf16 a[84:87], a[224:227], v[112:115], a[84:87]\n"
"ds_read_b128 a[160:163], v3 offset:11264\n"
"ds_read_b128 a[164:167], v3 offset:20480\n"
"v_mfma_f32_16x16x32_bf16 a[84:87], a[228:231], v[124:127], a[84:87]\n"
"ds_read_b128 a[168:171], v3 offset:29696\n"
"ds_read_b128 a[172:175], v3 offset:38912\n"
"v_mfma_f32_16x16x32_bf16 a[84:87], a[232:235], v[136:139], a[84:87]\n"
"ds_read_b128 a[176:179], v3 offset:12288\n"
"ds_read_b128 a[180:183], v3 offset:21504\n"
"v_mfma_f32_16x16x32_bf16 a[84:87], a[236:239], v[148:151], a[84:87]\n"
"ds_read_b128 a[184:187], v3 offset:30720\n"
"ds_read_b128 a[188:191], v3 offset:39936\n"
"v_mfma_f32_16x16x32_bf16 a[88:91], a[224:227], v[116:119], a[88:91]\n"
"v_mfma_f32_16x16x32_bf16 a[88:91], a[228:231], v[128:131], a[88:91]\n"
"v_mfma_f32_16x16x32_bf16 a[88:91], a[232:235], v[140:143], a[88:91]\n"
"v_mfma_f32_16x16x32_bf16 a[88:91], a[236:239], v[152:155], a[88:91]\n"
"v_mfma_f32_16x16x32_bf16 a[92:95], a[224:227], v[120:123], a[92:95]\n"
"v_mfma_f32_16x16x32_bf16 a[92:95], a[228:231], v[132:135], a[92:95]\n"
"v_mfma_f32_16x16x32_bf16 a[92:95], a[232:235], v[144:147], a[92:95]\n"
"v_mfma_f32_16x16x32_bf16 a[92:95], a[236:239], v[156:159], a[92:95]\n"
"s_waitcnt lgkmcnt(3)\n"
"v_mfma_f32_16x16x32_bf16 a[96:99], a[240:243], v[112:115], a[96:99]\n"
"v_mfma_f32_16x16x32_bf16 a[96:99], a[244:247], v[124:127], a[96:99]\n"
"v_mfma_f32_16x16x32_bf16 a[96:99], a[248:251], v[136:139], a[96:99]\n"
"v_mfma_f32_16x16x32_bf16 a[96:99], a[252:255], v[148:151], a[96:99]\n"
"v_mfma_f32_16x16x32_bf16 a[100:103], a[240:243], v[116:119], a[100:103]\n"
"v_mfma_f32_16x16x32_bf16 a[100:103], a[244:247], v[128:131], a[100:103]\n"
"v_mfma_f32_16x16x32_bf16 a[100:103], a[248:251], v[140:143], a[100:103]\n"
"v_mfma_f32_16x16x32_bf16 a[100:103], a[252:255], v[152:155], a[100:103]\n"
"v_mfma_f32_16x16x32_bf16 a[104:107], a[240:243], v[120:123], a[104:107]\n"
"v_mfma_f32_16x16x32_bf16 a[104:107], a[244:247], v[132:135], a[104:107]\n"
"v_mfma_f32_16x16x32_bf16 a[104:107], a[248:251], v[144:147], a[104:107]\n"
"v_mfma_f32_16x16x32_bf16 a[104:107], a[252:255], v[156:159], a[104:107]\n"
"v_mfma_f32_16x16x32_bf16 a[0:3], a[112:115], v[160:163], a[0:3]\n"
"v_mfma_f32_16x16x32_bf16 a[0:3], a[116:119], v[172:175], a[0:3]\n"
"s_waitcnt vmcnt(26)\n"
"v_mfma_f32_16x16x32_bf16 a[0:3], a[120:123], v[184:187], a[0:3]\n"
"s_waitcnt vmcnt(23)\n"
"v_mfma_f32_16x16x32_bf16 a[0:3], a[124:127], v[196:199], a[0:3]\n"
"v_mfma_f32_16x16x32_bf16 a[4:7], a[112:115], v[164:167], a[4:7]\n"
"v_mfma_f32_16x16x32_bf16 a[4:7], a[116:119], v[176:179], a[4:7]\n"
"v_mfma_f32_16x16x32_bf16 a[4:7], a[120:123], v[188:191], a[4:7]\n"
"s_waitcnt vmcnt(22)\n"
"v_mfma_f32_16x16x32_bf16 a[4:7], a[124:127], v[200:203], a[4:7]\n"
"v_mfma_f32_16x16x32_bf16 a[8:11], a[112:115], v[168:171], a[8:11]\n"
"v_mfma_f32_16x16x32_bf16 a[8:11], a[116:119], v[180:183], a[8:11]\n"
"v_mfma_f32_16x16x32_bf16 a[8:11], a[120:123], v[192:195], a[8:11]\n"
"s_waitcnt vmcnt(21)\n"
"v_mfma_f32_16x16x32_bf16 a[8:11], a[124:127], v[204:207], a[8:11]\n"
"v_mfma_f32_16x16x32_bf16 a[12:15], a[128:131], v[160:163], a[12:15]\n"
"v_mfma_f32_16x16x32_bf16 a[12:15], a[132:135], v[172:175], a[12:15]\n"
"v_mfma_f32_16x16x32_bf16 a[12:15], a[136:139], v[184:187], a[12:15]\n"
"v_mfma_f32_16x16x32_bf16 a[12:15], a[140:143], v[196:199], a[12:15]\n"
"v_mfma_f32_16x16x32_bf16 a[16:19], a[128:131], v[164:167], a[16:19]\n"
"v_mfma_f32_16x16x32_bf16 a[16:19], a[132:135], v[176:179], a[16:19]\n"
"v_mfma_f32_16x16x32_bf16 a[16:19], a[136:139], v[188:191], a[16:19]\n"
"v_mfma_f32_16x16x32_bf16 a[16:19], a[140:143], v[200:203], a[16:19]\n"
"v_mfma_f32_16x16x32_bf16 a[20:23], a[128:131], v[168:171], a[20:23]\n"
"ds_read_b128 a[128:131], v3 offset:13312\n"
"v_mfma_f32_16x16x32_bf16 a[20:23], a[132:135], v[180:183], a[20:23]\n"
"ds_read_b128 a[132:135], v3 offset:22528\n"
"v_mfma_f32_16x16x32_bf16 a[20:23], a[136:139], v[192:195], a[20:23]\n"
"ds_read_b128 a[136:139], v3 offset:31744\n"
"v_mfma_f32_16x16x32_bf16 a[20:23], a[140:143], v[204:207], a[20:23]\n"
"ds_read_b128 a[140:143], v3 offset:40960\n"
"v_mfma_f32_16x16x32_bf16 a[24:27], a[144:147], v[160:163], a[24:27]\n"
"v_mfma_f32_16x16x32_bf16 a[24:27], a[148:151], v[172:175], a[24:27]\n"
"v_mfma_f32_16x16x32_bf16 a[24:27], a[152:155], v[184:187], a[24:27]\n"
"v_mfma_f32_16x16x32_bf16 a[24:27], a[156:159], v[196:199], a[24:27]\n"
"v_mfma_f32_16x16x32_bf16 a[28:31], a[144:147], v[164:167], a[28:31]\n"
"v_mfma_f32_16x16x32_bf16 a[28:31], a[148:151], v[176:179], a[28:31]\n"
"s_waitcnt vmcnt(8)\n"
"s_barrier\n"
"v_mfma_f32_16x16x32_bf16 a[28:31], a[152:155], v[188:191], a[28:31]\n"
"ds_read_b128 a[192:195], v3 offset:45056\n"
"ds_read_b128 a[196:199], v3 offset:54272\n"
"v_mfma_f32_16x16x32_bf16 a[28:31], a[156:159], v[200:203], a[28:31]\n"
"ds_read_b128 a[200:203], v3 offset:63488\n"
"ds_read_b128 a[204:207], v4 offset:7168\n"
"v_mfma_f32_16x16x32_bf16 a[32:35], a[144:147], v[168:171], a[32:35]\n"
"ds_read_b128 a[208:211], v3 offset:46080\n"
"ds_read_b128 a[212:215], v3 offset:55296\n"
"v_mfma_f32_16x16x32_bf16 a[32:35], a[148:151], v[180:183], a[32:35]\n"
"ds_read_b128 a[216:219], v3 offset:64512\n"
"ds_read_b128 a[220:223], v4 offset:8192\n"
"v_mfma_f32_16x16x32_bf16 a[32:35], a[152:155], v[192:195], a[32:35]\n"
"ds_read_b128 a[224:227], v3 offset:47104\n"
"ds_read_b128 a[228:231], v3 offset:56320\n"
"v_mfma_f32_16x16x32_bf16 a[32:35], a[156:159], v[204:207], a[32:35]\n"
"ds_read_b128 a[232:235], v4 offset:0\n"
"ds_read_b128 a[236:239], v4 offset:9216\n"
"v_mfma_f32_16x16x32_bf16 a[36:39], a[160:163], v[160:163], a[36:39]\n"
"ds_read_b128 a[240:243], v3 offset:48128\n"
"ds_read_b128 a[244:247], v3 offset:57344\n"
"v_mfma_f32_16x16x32_bf16 a[36:39], a[164:167], v[172:175], a[36:39]\n"
"ds_read_b128 a[248:251], v4 offset:1024\n"
"ds_read_b128 a[252:255], v4 offset:10240\n"
"v_mfma_f32_16x16x32_bf16 a[36:39], a[168:171], v[184:187], a[36:39]\n"
"ds_read_b128 a[112:115], v3 offset:49152\n"
"ds_read_b128 a[116:119], v3 offset:58368\n"
"v_mfma_f32_16x16x32_bf16 a[36:39], a[172:175], v[196:199], a[36:39]\n"
"ds_read_b128 a[120:123], v4 offset:2048\n"
"ds_read_b128 a[124:127], v4 offset:11264\n"
"v_mfma_f32_16x16x32_bf16 a[40:43], a[160:163], v[164:167], a[40:43]\n"
"ds_read_b128 a[144:147], v3 offset:14336\n"
"ds_read_b128 a[148:151], v3 offset:23552\n"
"v_mfma_f32_16x16x32_bf16 a[40:43], a[164:167], v[176:179], a[40:43]\n"
"ds_read_b128 a[152:155], v3 offset:32768\n"
"ds_read_b128 a[156:159], v3 offset:41984\n"
"v_mfma_f32_16x16x32_bf16 a[40:43], a[168:171], v[188:191], a[40:43]\n"
"v_mfma_f32_16x16x32_bf16 a[40:43], a[172:175], v[200:203], a[40:43]\n"
"v_mfma_f32_16x16x32_bf16 a[44:47], a[160:163], v[168:171], a[44:47]\n"
"ds_read_b128 a[160:163], v3 offset:15360\n"
"v_mfma_f32_16x16x32_bf16 a[44:47], a[164:167], v[180:183], a[44:47]\n"
"ds_read_b128 a[164:167], v3 offset:24576\n"
"v_mfma_f32_16x16x32_bf16 a[44:47], a[168:171], v[192:195], a[44:47]\n"
"ds_read_b128 a[168:171], v3 offset:33792\n"
"v_mfma_f32_16x16x32_bf16 a[44:47], a[172:175], v[204:207], a[44:47]\n"
"ds_read_b128 a[172:175], v3 offset:43008\n"
"v_mfma_f32_16x16x32_bf16 a[48:51], a[176:179], v[160:163], a[48:51]\n"
"s_waitcnt lgkmcnt(9)\n"
"v_mfma_f32_16x16x32_bf16 a[48:51], a[180:183], v[172:175], a[48:51]\n"
"v_mfma_f32_16x16x32_bf16 a[48:51], a[184:187], v[184:187], a[48:51]\n"
"v_mfma_f32_16x16x32_bf16 a[48:51], a[188:191], v[196:199], a[48:51]\n"
"v_mfma_f32_16x16x32_bf16 a[52:55], a[176:179], v[164:167], a[52:55]\n"
"v_mfma_f32_16x16x32_bf16 a[52:55], a[180:183], v[176:179], a[52:55]\n"
"v_mfma_f32_16x16x32_bf16 a[52:55], a[184:187], v[188:191], a[52:55]\n"
"v_mfma_f32_16x16x32_bf16 a[52:55], a[188:191], v[200:203], a[52:55]\n"
"v_mfma_f32_16x16x32_bf16 a[56:59], a[176:179], v[168:171], a[56:59]\n"
"ds_read_b128 a[176:179], v3 offset:16384\n"
"v_mfma_f32_16x16x32_bf16 a[56:59], a[180:183], v[180:183], a[56:59]\n"
"ds_read_b128 a[180:183], v3 offset:25600\n"
"v_mfma_f32_16x16x32_bf16 a[56:59], a[184:187], v[192:195], a[56:59]\n"
"ds_read_b128 a[184:187], v3 offset:34816\n"
"v_mfma_f32_16x16x32_bf16 a[56:59], a[188:191], v[204:207], a[56:59]\n"
"ds_read_b128 a[188:191], v3 offset:44032\n"
"v_mfma_f32_16x16x32_bf16 a[0:3], a[192:195], v[208:211], a[0:3]\n"
"v_mfma_f32_16x16x32_bf16 a[0:3], a[196:199], v[220:223], a[0:3]\n"
"s_waitcnt vmcnt(5)\n"
"v_mfma_f32_16x16x32_bf16 a[0:3], a[200:203], v[232:235], a[0:3]\n"
"s_waitcnt vmcnt(2)\n"
"v_mfma_f32_16x16x32_bf16 a[0:3], a[204:207], v[244:247], a[0:3]\n"
"v_mfma_f32_16x16x32_bf16 a[4:7], a[192:195], v[212:215], a[4:7]\n"
"v_mfma_f32_16x16x32_bf16 a[4:7], a[196:199], v[224:227], a[4:7]\n"
"v_mfma_f32_16x16x32_bf16 a[4:7], a[200:203], v[236:239], a[4:7]\n"
"s_nop 4\n"
"v_accvgpr_read_b32 v16, a0\n"
"v_accvgpr_read_b32 v17, a1\n"
"s_waitcnt vmcnt(1)\n"
"v_mfma_f32_16x16x32_bf16 a[4:7], a[204:207], v[248:251], a[4:7]\n"
"v_accvgpr_read_b32 v18, a2\n"
"v_accvgpr_read_b32 v19, a3\n"
"v_mfma_f32_16x16x32_bf16 a[8:11], a[192:195], v[216:219], a[8:11]\n"
"ds_read_b128 a[192:195], v3 offset:50176\n"
"v_cvt_pk_bf16_f32 v16, v16, v17\n"
"v_mfma_f32_16x16x32_bf16 a[8:11], a[196:199], v[228:231], a[8:11]\n"
"ds_read_b128 a[196:199], v3 offset:59392\n"
"v_cvt_pk_bf16_f32 v17, v18, v19\n"
"v_mfma_f32_16x16x32_bf16 a[8:11], a[200:203], v[240:243], a[8:11]\n"
"ds_read_b128 a[200:203], v4 offset:3072\n"
"v_accvgpr_read_b32 v20, a4\n"
"s_waitcnt vmcnt(0)\n"
"v_mfma_f32_16x16x32_bf16 a[8:11], a[204:207], v[252:255], a[8:11]\n"
"ds_read_b128 a[204:207], v4 offset:12288\n"
"v_accvgpr_read_b32 v21, a5\n"
"v_mfma_f32_16x16x32_bf16 a[12:15], a[208:211], v[208:211], a[12:15]\n"
"v_accvgpr_read_b32 v22, a6\n"
"v_accvgpr_read_b32 v23, a7\n"
"v_mfma_f32_16x16x32_bf16 a[12:15], a[212:215], v[220:223], a[12:15]\n"
"v_cvt_pk_bf16_f32 v18, v20, v21\n"
"v_cvt_pk_bf16_f32 v19, v22, v23\n"
"v_mfma_f32_16x16x32_bf16 a[12:15], a[216:219], v[232:235], a[12:15]\n"
"v_accvgpr_read_b32 v24, a8\n"
"v_accvgpr_read_b32 v25, a9\n"
"v_mfma_f32_16x16x32_bf16 a[12:15], a[220:223], v[244:247], a[12:15]\n"
"v_accvgpr_read_b32 v26, a10\n"
"v_accvgpr_read_b32 v27, a11\n"
"v_mfma_f32_16x16x32_bf16 a[16:19], a[208:211], v[212:215], a[16:19]\n"
"v_cvt_pk_bf16_f32 v24, v24, v25\n"
"v_cvt_pk_bf16_f32 v25, v26, v27\n"
"v_mfma_f32_16x16x32_bf16 a[16:19], a[212:215], v[224:227], a[16:19]\n"
"buffer_store_dwordx4 v[16:19], v1, s[44:47], s61 offen offset:0 sc1\n"
"v_mfma_f32_16x16x32_bf16 a[16:19], a[216:219], v[236:239], a[16:19]\n"
"v_accvgpr_read_b32 v28, a12\n"
"v_accvgpr_read_b32 v29, a13\n"
"v_mfma_f32_16x16x32_bf16 a[16:19], a[220:223], v[248:251], a[16:19]\n"
"v_accvgpr_read_b32 v30, a14\n"
"v_accvgpr_read_b32 v31, a15\n"
"v_mfma_f32_16x16x32_bf16 a[20:23], a[208:211], v[216:219], a[20:23]\n"
"ds_read_b128 a[208:211], v3 offset:51200\n"
"v_cvt_pk_bf16_f32 v26, v28, v29\n"
"v_mfma_f32_16x16x32_bf16 a[20:23], a[212:215], v[228:231], a[20:23]\n"
"ds_read_b128 a[212:215], v3 offset:60416\n"
"v_cvt_pk_bf16_f32 v27, v30, v31\n"
"v_mfma_f32_16x16x32_bf16 a[20:23], a[216:219], v[240:243], a[20:23]\n"
"ds_read_b128 a[216:219], v4 offset:4096\n"
"v_accvgpr_read_b32 v32, a16\n"
"v_mfma_f32_16x16x32_bf16 a[20:23], a[220:223], v[252:255], a[20:23]\n"
"ds_read_b128 a[220:223], v4 offset:13312\n"
"v_accvgpr_read_b32 v33, a17\n"
"v_mfma_f32_16x16x32_bf16 a[24:27], a[224:227], v[208:211], a[24:27]\n"
"buffer_store_dwordx4 v[24:27], v1, s[44:47], s61 offen offset:1024 sc1\n"
"v_mfma_f32_16x16x32_bf16 a[24:27], a[228:231], v[220:223], a[24:27]\n"
"v_accvgpr_read_b32 v34, a18\n"
"v_accvgpr_read_b32 v35, a19\n"
"v_mfma_f32_16x16x32_bf16 a[24:27], a[232:235], v[232:235], a[24:27]\n"
"v_cvt_pk_bf16_f32 v32, v32, v33\n"
"v_accvgpr_read_b32 v36, a20\n"
"v_mfma_f32_16x16x32_bf16 a[24:27], a[236:239], v[244:247], a[24:27]\n"
"v_accvgpr_read_b32 v37, a21\n"
"v_accvgpr_read_b32 v38, a22\n"
"v_mfma_f32_16x16x32_bf16 a[28:31], a[224:227], v[212:215], a[28:31]\n"
"v_accvgpr_read_b32 v39, a23\n"
"v_cvt_pk_bf16_f32 v33, v34, v35\n"
"v_mfma_f32_16x16x32_bf16 a[28:31], a[228:231], v[224:227], a[28:31]\n"
"v_cvt_pk_bf16_f32 v34, v36, v37\n"
"v_cvt_pk_bf16_f32 v35, v38, v39\n"
"v_mfma_f32_16x16x32_bf16 a[28:31], a[232:235], v[236:239], a[28:31]\n"
"v_accvgpr_read_b32 v40, a24\n"
"v_accvgpr_read_b32 v41, a25\n"
"v_mfma_f32_16x16x32_bf16 a[28:31], a[236:239], v[248:251], a[28:31]\n"
"v_accvgpr_read_b32 v42, a26\n"
"v_accvgpr_read_b32 v43, a27\n"
"v_mfma_f32_16x16x32_bf16 a[32:35], a[224:227], v[216:219], a[32:35]\n"
"ds_read_b128 a[224:227], v3 offset:52224\n"
"v_cvt_pk_bf16_f32 v40, v40, v41\n"
"v_mfma_f32_16x16x32_bf16 a[32:35], a[228:231], v[228:231], a[32:35]\n"
"ds_read_b128 a[228:231], v3 offset:61440\n"
"v_cvt_pk_bf16_f32 v41, v42, v43\n"
"v_mfma_f32_16x16x32_bf16 a[32:35], a[232:235], v[240:243], a[32:35]\n"
"ds_read_b128 a[232:235], v4 offset:5120\n"
"v_accvgpr_read_b32 v44, a28\n"
"v_mfma_f32_16x16x32_bf16 a[32:35], a[236:239], v[252:255], a[32:35]\n"
"ds_read_b128 a[236:239], v4 offset:14336\n"
"v_accvgpr_read_b32 v45, a29\n"
"v_mfma_f32_16x16x32_bf16 a[36:39], a[240:243], v[208:211], a[36:39]\n"
"v_accvgpr_read_b32 v46, a30\n"
"v_accvgpr_read_b32 v47, a31\n"
"v_mfma_f32_16x16x32_bf16 a[36:39], a[244:247], v[220:223], a[36:39]\n"
"v_cvt_pk_bf16_f32 v42, v44, v45\n"
"v_cvt_pk_bf16_f32 v43, v46, v47\n"
"v_mfma_f32_16x16x32_bf16 a[36:39], a[248:251], v[232:235], a[36:39]\n"
"v_accvgpr_read_b32 v48, a32\n"
"v_accvgpr_read_b32 v49, a33\n"
"v_mfma_f32_16x16x32_bf16 a[36:39], a[252:255], v[244:247], a[36:39]\n"
"buffer_store_dwordx4 v[32:35], v1, s[44:47], s61 offen offset:2048 sc1\n"
"v_mfma_f32_16x16x32_bf16 a[40:43], a[240:243], v[212:215], a[40:43]\n"
"buffer_store_dwordx4 v[40:43], v1, s[44:47], s61 offen offset:3072 sc1\n"
"v_mfma_f32_16x16x32_bf16 a[40:43], a[244:247], v[224:227], a[40:43]\n"
"v_accvgpr_read_b32 v50, a34\n"
"v_accvgpr_read_b32 v51, a35\n"
"v_mfma_f32_16x16x32_bf16 a[40:43], a[248:251], v[236:239], a[40:43]\n"
"v_cvt_pk_bf16_f32 v48, v48, v49\n"
"v_accvgpr_read_b32 v52, a36\n"
"v_mfma_f32_16x16x32_bf16 a[40:43], a[252:255], v[248:251], a[40:43]\n"
"v_accvgpr_read_b32 v53, a37\n"
"v_accvgpr_read_b32 v54, a38\n"
"v_mfma_f32_16x16x32_bf16 a[44:47], a[240:243], v[216:219], a[44:47]\n"
"ds_read_b128 a[240:243], v3 offset:53248\n"
"v_accvgpr_read_b32 v55, a39\n"
"v_mfma_f32_16x16x32_bf16 a[44:47], a[244:247], v[228:231], a[44:47]\n"
"ds_read_b128 a[244:247], v3 offset:62464\n"
"v_cvt_pk_bf16_f32 v49, v50, v51\n"
"v_mfma_f32_16x16x32_bf16 a[44:47], a[248:251], v[240:243], a[44:47]\n"
"ds_read_b128 a[248:251], v4 offset:6144\n"
"v_accvgpr_read_b32 v56, a40\n"
"v_mfma_f32_16x16x32_bf16 a[44:47], a[252:255], v[252:255], a[44:47]\n"
"ds_read_b128 a[252:255], v4 offset:15360\n"
"v_mfma_f32_16x16x32_bf16 a[48:51], a[112:115], v[208:211], a[48:51]\n"
"v_accvgpr_read_b32 v57, a41\n"
"v_accvgpr_read_b32 v58, a42\n"
"v_mfma_f32_16x16x32_bf16 a[48:51], a[116:119], v[220:223], a[48:51]\n"
"v_accvgpr_read_b32 v59, a43\n"
"v_cvt_pk_bf16_f32 v56, v56, v57\n"
"v_mfma_f32_16x16x32_bf16 a[48:51], a[120:123], v[232:235], a[48:51]\n"
"v_accvgpr_read_b32 v60, a44\n"
"v_accvgpr_read_b32 v61, a45\n"
"s_waitcnt lgkmcnt(4)\n"
"v_mfma_f32_16x16x32_bf16 a[48:51], a[124:127], v[244:247], a[48:51]\n"
"v_accvgpr_read_b32 v62, a46\n"
"v_accvgpr_read_b32 v63, a47\n"
"v_mfma_f32_16x16x32_bf16 a[52:55], a[112:115], v[212:215], a[52:55]\n"
"v_cvt_pk_bf16_f32 v57, v58, v59\n"
"v_cvt_pk_bf16_f32 v50, v52, v53\n"
"v_mfma_f32_16x16x32_bf16 a[52:55], a[116:119], v[224:227], a[52:55]\n"
"v_cvt_pk_bf16_f32 v51, v54, v55\n"
"s_add_u32 s61, s61, 4096\n"
"v_mfma_f32_16x16x32_bf16 a[52:55], a[120:123], v[236:239], a[52:55]\n"
"v_accvgpr_read_b32 v16, a48\n"
"v_accvgpr_read_b32 v17, a49\n"
"v_mfma_f32_16x16x32_bf16 a[52:55], a[124:127], v[248:251], a[52:55]\n"
"v_accvgpr_read_b32 v18, a50\n"
"v_accvgpr_read_b32 v19, a51\n"
"v_mfma_f32_16x16x32_bf16 a[56:59], a[112:115], v[216:219], a[56:59]\n"
"v_cvt_pk_bf16_f32 v16, v16, v17\n"
"v_cvt_pk_bf16_f32 v17, v18, v19\n"
"v_mfma_f32_16x16x32_bf16 a[56:59], a[116:119], v[228:231], a[56:59]\n"
"v_cvt_pk_bf16_f32 v58, v60, v61\n"
"v_cvt_pk_bf16_f32 v59, v62, v63\n"
"v_mfma_f32_16x16x32_bf16 a[56:59], a[120:123], v[240:243], a[56:59]\n"
"v_accvgpr_read_b32 v20, a52\n"
"v_accvgpr_read_b32 v21, a53\n"
"v_mfma_f32_16x16x32_bf16 a[56:59], a[124:127], v[252:255], a[56:59]\n"
"v_accvgpr_read_b32 v22, a54\n"
"v_accvgpr_read_b32 v23, a55\n"
"v_mfma_f32_16x16x32_bf16 a[60:63], a[128:131], v[160:163], a[60:63]\n"
"v_cvt_pk_bf16_f32 v18, v20, v21\n"
"v_cvt_pk_bf16_f32 v19, v22, v23\n"
"v_mfma_f32_16x16x32_bf16 a[60:63], a[132:135], v[172:175], a[60:63]\n"
"buffer_store_dwordx4 v[48:51], v1, s[44:47], s61 offen offset:0 sc1\n"
"v_mfma_f32_16x16x32_bf16 a[60:63], a[136:139], v[184:187], a[60:63]\n"
"v_accvgpr_read_b32 v24, a56\n"
"v_accvgpr_read_b32 v25, a57\n"
"v_mfma_f32_16x16x32_bf16 a[60:63], a[140:143], v[196:199], a[60:63]\n"
"v_accvgpr_read_b32 v26, a58\n"
"v_accvgpr_read_b32 v27, a59\n"
"v_mfma_f32_16x16x32_bf16 a[64:67], a[128:131], v[164:167], a[64:67]\n"
"v_cvt_pk_bf16_f32 v24, v24, v25\n"
"v_cvt_pk_bf16_f32 v25, v26, v27\n"
"v_mfma_f32_16x16x32_bf16 a[64:67], a[132:135], v[176:179], a[64:67]\n"
"buffer_store_dwordx4 v[56:59], v1, s[44:47], s61 offen offset:1024 sc1\n"
"v_mfma_f32_16x16x32_bf16 a[64:67], a[136:139], v[188:191], a[64:67]\n"
"buffer_store_dwordx4 v[16:19], v1, s[44:47], s61 offen offset:2048 sc1\n"
"v_mfma_f32_16x16x32_bf16 a[64:67], a[140:143], v[200:203], a[64:67]\n"
"buffer_store_dwordx4 v[24:27], v1, s[44:47], s61 offen offset:3072 sc1\n"
"v_mfma_f32_16x16x32_bf16 a[68:71], a[128:131], v[168:171], a[68:71]\n"
"v_mfma_f32_16x16x32_bf16 a[68:71], a[132:135], v[180:183], a[68:71]\n"
"v_mfma_f32_16x16x32_bf16 a[68:71], a[136:139], v[192:195], a[68:71]\n"
"v_mfma_f32_16x16x32_bf16 a[68:71], a[140:143], v[204:207], a[68:71]\n"
"v_mfma_f32_16x16x32_bf16 a[72:75], a[144:147], v[160:163], a[72:75]\n"
"v_mfma_f32_16x16x32_bf16 a[72:75], a[148:151], v[172:175], a[72:75]\n"
"v_mfma_f32_16x16x32_bf16 a[72:75], a[152:155], v[184:187], a[72:75]\n"
"v_mfma_f32_16x16x32_bf16 a[72:75], a[156:159], v[196:199], a[72:75]\n"
"v_mfma_f32_16x16x32_bf16 a[76:79], a[144:147], v[164:167], a[76:79]\n"
"v_mfma_f32_16x16x32_bf16 a[76:79], a[148:151], v[176:179], a[76:79]\n"
"v_mfma_f32_16x16x32_bf16 a[76:79], a[152:155], v[188:191], a[76:79]\n"
"v_mfma_f32_16x16x32_bf16 a[76:79], a[156:159], v[200:203], a[76:79]\n"
"v_mfma_f32_16x16x32_bf16 a[80:83], a[144:147], v[168:171], a[80:83]\n"
"v_mfma_f32_16x16x32_bf16 a[80:83], a[148:151], v[180:183], a[80:83]\n"
"v_mfma_f32_16x16x32_bf16 a[80:83], a[152:155], v[192:195], a[80:83]\n"
"v_mfma_f32_16x16x32_bf16 a[80:83], a[156:159], v[204:207], a[80:83]\n"
"v_mfma_f32_16x16x32_bf16 a[84:87], a[160:163], v[160:163], a[84:87]\n"
"v_mfma_f32_16x16x32_bf16 a[84:87], a[164:167], v[172:175], a[84:87]\n"
"v_mfma_f32_16x16x32_bf16 a[84:87], a[168:171], v[184:187], a[84:87]\n"
"v_mfma_f32_16x16x32_bf16 a[84:87], a[172:175], v[196:199], a[84:87]\n"
"v_mfma_f32_16x16x32_bf16 a[88:91], a[160:163], v[164:167], a[88:91]\n"
"v_mfma_f32_16x16x32_bf16 a[88:91], a[164:167], v[176:179], a[88:91]\n"
"v_mfma_f32_16x16x32_bf16 a[88:91], a[168:171], v[188:191], a[88:91]\n"
"v_mfma_f32_16x16x32_bf16 a[88:91], a[172:175], v[200:203], a[88:91]\n"
"v_mfma_f32_16x16x32_bf16 a[92:95], a[160:163], v[168:171], a[92:95]\n"
"v_mfma_f32_16x16x32_bf16 a[92:95], a[164:167], v[180:183], a[92:95]\n"
"v_mfma_f32_16x16x32_bf16 a[92:95], a[168:171], v[192:195], a[92:95]\n"
"v_mfma_f32_16x16x32_bf16 a[92:95], a[172:175], v[204:207], a[92:95]\n"
"v_mfma_f32_16x16x32_bf16 a[96:99], a[176:179], v[160:163], a[96:99]\n"
"v_mfma_f32_16x16x32_bf16 a[96:99], a[180:183], v[172:175], a[96:99]\n"
"v_mfma_f32_16x16x32_bf16 a[96:99], a[184:187], v[184:187], a[96:99]\n"
"v_mfma_f32_16x16x32_bf16 a[96:99], a[188:191], v[196:199], a[96:99]\n"
"v_mfma_f32_16x16x32_bf16 a[100:103], a[176:179], v[164:167], a[100:103]\n"
"v_mfma_f32_16x16x32_bf16 a[100:103], a[180:183], v[176:179], a[100:103]\n"
"v_mfma_f32_16x16x32_bf16 a[100:103], a[184:187], v[188:191], a[100:103]\n"
"v_mfma_f32_16x16x32_bf16 a[100:103], a[188:191], v[200:203], a[100:103]\n"
"v_mfma_f32_16x16x32_bf16 a[104:107], a[176:179], v[168:171], a[104:107]\n"
"v_mfma_f32_16x16x32_bf16 a[104:107], a[180:183], v[180:183], a[104:107]\n"
"v_mfma_f32_16x16x32_bf16 a[104:107], a[184:187], v[192:195], a[104:107]\n"
"v_mfma_f32_16x16x32_bf16 a[104:107], a[188:191], v[204:207], a[104:107]\n"
"v_mfma_f32_16x16x32_bf16 a[60:63], a[192:195], v[208:211], a[60:63]\n"
"v_mfma_f32_16x16x32_bf16 a[60:63], a[196:199], v[220:223], a[60:63]\n"
"v_mfma_f32_16x16x32_bf16 a[60:63], a[200:203], v[232:235], a[60:63]\n"
"v_mfma_f32_16x16x32_bf16 a[60:63], a[204:207], v[244:247], a[60:63]\n"
"v_mfma_f32_16x16x32_bf16 a[64:67], a[192:195], v[212:215], a[64:67]\n"
"v_mfma_f32_16x16x32_bf16 a[64:67], a[196:199], v[224:227], a[64:67]\n"
"v_mfma_f32_16x16x32_bf16 a[64:67], a[200:203], v[236:239], a[64:67]\n"
"v_mfma_f32_16x16x32_bf16 a[64:67], a[204:207], v[248:251], a[64:67]\n"
"v_mfma_f32_16x16x32_bf16 a[68:71], a[192:195], v[216:219], a[68:71]\n"
"v_mfma_f32_16x16x32_bf16 a[68:71], a[196:199], v[228:231], a[68:71]\n"
"v_mfma_f32_16x16x32_bf16 a[68:71], a[200:203], v[240:243], a[68:71]\n"
"v_mfma_f32_16x16x32_bf16 a[68:71], a[204:207], v[252:255], a[68:71]\n"
"v_mfma_f32_16x16x32_bf16 a[72:75], a[208:211], v[208:211], a[72:75]\n"
"v_mfma_f32_16x16x32_bf16 a[72:75], a[212:215], v[220:223], a[72:75]\n"
"v_mfma_f32_16x16x32_bf16 a[72:75], a[216:219], v[232:235], a[72:75]\n"
"v_mfma_f32_16x16x32_bf16 a[72:75], a[220:223], v[244:247], a[72:75]\n"
"v_mfma_f32_16x16x32_bf16 a[76:79], a[208:211], v[212:215], a[76:79]\n"
"v_mfma_f32_16x16x32_bf16 a[76:79], a[212:215], v[224:227], a[76:79]\n"
"v_mfma_f32_16x16x32_bf16 a[76:79], a[216:219], v[236:239], a[76:79]\n"
"v_mfma_f32_16x16x32_bf16 a[76:79], a[220:223], v[248:251], a[76:79]\n"
"v_mfma_f32_16x16x32_bf16 a[80:83], a[208:211], v[216:219], a[80:83]\n"
"v_mfma_f32_16x16x32_bf16 a[80:83], a[212:215], v[228:231], a[80:83]\n"
"v_mfma_f32_16x16x32_bf16 a[80:83], a[216:219], v[240:243], a[80:83]\n"
"v_mfma_f32_16x16x32_bf16 a[80:83], a[220:223], v[252:255], a[80:83]\n"
"v_mfma_f32_16x16x32_bf16 a[84:87], a[224:227], v[208:211], a[84:87]\n"
"v_mfma_f32_16x16x32_bf16 a[84:87], a[228:231], v[220:223], a[84:87]\n"
"v_mfma_f32_16x16x32_bf16 a[84:87], a[232:235], v[232:235], a[84:87]\n"
"v_mfma_f32_16x16x32_bf16 a[84:87], a[236:239], v[244:247], a[84:87]\n"
"v_mfma_f32_16x16x32_bf16 a[88:91], a[224:227], v[212:215], a[88:91]\n"
"v_mfma_f32_16x16x32_bf16 a[88:91], a[228:231], v[224:227], a[88:91]\n"
"v_mfma_f32_16x16x32_bf16 a[88:91], a[232:235], v[236:239], a[88:91]\n"
"v_mfma_f32_16x16x32_bf16 a[88:91], a[236:239], v[248:251], a[88:91]\n"
"v_mfma_f32_16x16x32_bf16 a[92:95], a[224:227], v[216:219], a[92:95]\n"
"v_mfma_f32_16x16x32_bf16 a[92:95], a[228:231], v[228:231], a[92:95]\n"
"v_mfma_f32_16x16x32_bf16 a[92:95], a[232:235], v[240:243], a[92:95]\n"
"v_mfma_f32_16x16x32_bf16 a[92:95], a[236:239], v[252:255], a[92:95]\n"
"s_waitcnt lgkmcnt(0)\n"
"v_mfma_f32_16x16x32_bf16 a[96:99], a[240:243], v[208:211], a[96:99]\n"
"v_mfma_f32_16x16x32_bf16 a[96:99], a[244:247], v[220:223], a[96:99]\n"
"v_mfma_f32_16x16x32_bf16 a[96:99], a[248:251], v[232:235], a[96:99]\n"
"v_mfma_f32_16x16x32_bf16 a[96:99], a[252:255], v[244:247], a[96:99]\n"
"v_mfma_f32_16x16x32_bf16 a[100:103], a[240:243], v[212:215], a[100:103]\n"
"v_mfma_f32_16x16x32_bf16 a[100:103], a[244:247], v[224:227], a[100:103]\n"
"v_mfma_f32_16x16x32_bf16 a[100:103], a[248:251], v[236:239], a[100:103]\n"
"v_mfma_f32_16x16x32_bf16 a[100:103], a[252:255], v[248:251], a[100:103]\n"
"v_mfma_f32_16x16x32_bf16 a[104:107], a[240:243], v[216:219], a[104:107]\n"
"v_mfma_f32_16x16x32_bf16 a[104:107], a[244:247], v[228:231], a[104:107]\n"
"v_mfma_f32_16x16x32_bf16 a[104:107], a[248:251], v[240:243], a[104:107]\n"
"v_mfma_f32_16x16x32_bf16 a[104:107], a[252:255], v[252:255], a[104:107]\n"
"s_nop 7\n"
"s_waitcnt vmcnt(0)\n"
"s_barrier\n"
"s_cmp_lg_u32 s66, 0\n"
"s_cbranch_scc1 EW_%=\n"
"v_mov_b32 v8, 1\n"
"v_mov_b32 v9, 0\n"
"s_mov_b64 exec, 1\n"
"buffer_store_dword v8, v9, s[52:55], s63 offen sc1\n"
"s_mov_b32 s78, 65536\n"
"EL_%=:\n"
"buffer_load_dword v10, v9, s[52:55], s67 offen sc1\n"
"s_waitcnt vmcnt(0)\n"
"v_readfirstlane_b32 s79, v10\n"
"s_sub_u32 s78, s78, 1\n"
"s_cmp_eq_u32 s79, 1\n"
"s_cbranch_scc1 ED_%=\n"
"s_sleep 1\n"
"s_cmp_lg_u32 s78, 0\n"
"s_cbranch_scc1 EL_%=\n"
"ED_%=:\n"
"s_mov_b64 exec, -1\n"
"EW_%=:\n"
"s_barrier\n"
"buffer_load_dwordx4 v[64:67], v1, s[44:47], s62 offen offset:0 sc1\n"
"buffer_load_dwordx4 v[68:71], v1, s[44:47], s62 offen offset:1024 sc1\n"
"buffer_load_dwordx4 v[72:75], v1, s[44:47], s62 offen offset:2048 sc1\n"
"buffer_load_dwordx4 v[76:79], v1, s[44:47], s62 offen offset:3072 sc1\n"
"s_add_u32 s62, s62, 4096\n"
"buffer_load_dwordx4 v[80:83], v1, s[44:47], s62 offen offset:0 sc1\n"
"buffer_load_dwordx4 v[84:87], v1, s[44:47], s62 offen offset:1024 sc1\n"
"buffer_load_dwordx4 v[88:91], v1, s[44:47], s62 offen offset:2048 sc1\n"
"buffer_load_dwordx4 v[92:95], v1, s[44:47], s62 offen offset:3072 sc1\n"
"s_add_u32 s76, s88, 0\n"
"s_add_u32 s80, s89, 0\n"
"s_add_u32 s77, s88, 36864\n"
"s_add_u32 s81, s89, 36864\n"
"s_add_u32 s78, s88, 73728\n"
"s_add_u32 s82, s89, 73728\n"
"v_accvgpr_read_b32 v128, a60\n"
"v_accvgpr_read_b32 v129, a61\n"
"v_accvgpr_read_b32 v130, a62\n"
"v_accvgpr_read_b32 v131, a63\n"
"v_accvgpr_read_b32 v132, a64\n"
"v_accvgpr_read_b32 v133, a65\n"
"v_accvgpr_read_b32 v134, a66\n"
"v_accvgpr_read_b32 v135, a67\n"
"v_accvgpr_read_b32 v136, a68\n"
"v_accvgpr_read_b32 v137, a69\n"
"v_accvgpr_read_b32 v138, a70\n"
"v_accvgpr_read_b32 v139, a71\n"
"v_accvgpr_read_b32 v140, a72\n"
"v_accvgpr_read_b32 v141, a73\n"
"v_accvgpr_read_b32 v142, a74\n"
"v_accvgpr_read_b32 v143, a75\n"
"v_accvgpr_read_b32 v144, a76\n"
"v_accvgpr_read_b32 v145, a77\n"
"v_accvgpr_read_b32 v146, a78\n"
"v_accvgpr_read_b32 v147, a79\n"
"v_accvgpr_read_b32 v148, a80\n"
"v_accvgpr_read_b32 v149, a81\n"
"v_accvgpr_read_b32 v150, a82\n"
"v_accvgpr_read_b32 v151, a83\n"
"v_accvgpr_read_b32 v152, a84\n"
"v_accvgpr_read_b32 v153, a85\n"
"v_accvgpr_read_b32 v154, a86\n"
"v_accvgpr_read_b32 v155, a87\n"
"v_accvgpr_read_b32 v156, a88\n"
"v_accvgpr_read_b32 v157, a89\n"
"v_accvgpr_read_b32 v158, a90\n"
"v_accvgpr_read_b32 v159, a91\n"
"v_accvgpr_read_b32 v160, a92\n"
"v_accvgpr_read_b32 v161, a93\n"
"v_accvgpr_read_b32 v162, a94\n"
"v_accvgpr_read_b32 v163, a95\n"
"v_accvgpr_read_b32 v164, a96\n"
"v_accvgpr_read_b32 v165, a97\n"
"v_accvgpr_read_b32 v166, a98\n"
"v_accvgpr_read_b32 v167, a99\n"
"v_accvgpr_read_b32 v168, a100\n"
"v_accvgpr_read_b32 v169, a101\n"
"v_accvgpr_read_b32 v170, a102\n"
"v_accvgpr_read_b32 v171, a103\n"
"v_accvgpr_read_b32 v172, a104\n"
"v_accvgpr_read_b32 v173, a105\n"
"v_accvgpr_read_b32 v174, a106\n"
"v_accvgpr_read_b32 v175, a107\n"
"v_accvgpr_read_b32 v176, a48\n"
"v_accvgpr_read_b32 v177, a49\n"
"v_accvgpr_read_b32 v178, a50\n"
"v_accvgpr_read_b32 v179, a51\n"
"v_accvgpr_read_b32 v180, a52\n"
"v_accvgpr_read_b32 v181, a53\n"
"v_accvgpr_read_b32 v182, a54\n"
"v_accvgpr_read_b32 v183, a55\n"
"v_accvgpr_read_b32 v184, a56\n"
"v_accvgpr_read_b32 v185, a57\n"
"v_accvgpr_read_b32 v186, a58\n"
"v_accvgpr_read_b32 v187, a59\n"
"s_waitcnt vmcnt(7)\n"
"v_lshlrev_b32 v192, 16, v64\n"
"v_and_b32 v193, 0xffff0000, v64\n"
"v_lshlrev_b32 v194, 16, v65\n"
"v_and_b32 v195, 0xffff0000, v65\n"
"v_add_f32 v128, v128, v192\n"
"v_add_f32 v129, v129, v193\n"
"v_add_f32 v130, v130, v194\n"
"v_add_f32 v131, v131, v195\n"
"v_cvt_pk_bf16_f32 v128, v128, v129\n"
"v_cvt_pk_bf16_f32 v129, v130, v131\n"
"buffer_store_dwordx2 v[128:129], v5, s[48:51], s76 offen offset:0\n"
"v_lshlrev_b32 v196, 16, v66\n"
"v_and_b32 v197, 0xffff0000, v66\n"
"v_lshlrev_b32 v198, 16, v67\n"
"v_and_b32 v199, 0xffff0000, v67\n"
"v_add_f32 v132, v132, v196\n"
"v_add_f32 v133, v133, v197\n"
"v_add_f32 v134, v134, v198\n"
"v_add_f32 v135, v135, v199\n"
"v_cvt_pk_bf16_f32 v132, v132, v133\n"
"v_cvt_pk_bf16_f32 v133, v134, v135\n"
"buffer_store_dwordx2 v[132:133], v5, s[48:51], s77 offen offset:0\n"
"s_waitcnt vmcnt(8)\n"
"v_lshlrev_b32 v200, 16, v68\n"
"v_and_b32 v201, 0xffff0000, v68\n"
"v_lshlrev_b32 v202, 16, v69\n"
"v_and_b32 v203, 0xffff0000, v69\n"
"v_add_f32 v136, v136, v200\n"
"v_add_f32 v137, v137, v201\n"
"v_add_f32 v138, v138, v202\n"
"v_add_f32 v139, v139, v203\n"
"v_cvt_pk_bf16_f32 v136, v136, v137\n"
"v_cvt_pk_bf16_f32 v137, v138, v139\n"
"buffer_store_dwordx2 v[136:137], v5, s[48:51], s78 offen offset:0\n"
"v_lshlrev_b32 v204, 16, v70\n"
"v_and_b32 v205, 0xffff0000, v70\n"
"v_lshlrev_b32 v206, 16, v71\n"
"v_and_b32 v207, 0xffff0000, v71\n"
"v_add_f32 v140, v140, v204\n"
"v_add_f32 v141, v141, v205\n"
"v_add_f32 v142, v142, v206\n"
"v_add_f32 v143, v143, v207\n"
"v_cvt_pk_bf16_f32 v140, v140, v141\n"
"v_cvt_pk_bf16_f32 v141, v142, v143\n"
"buffer_store_dwordx2 v[140:141], v5, s[48:51], s76 offen offset:32\n"
"s_waitcnt vmcnt(9)\n"
"v_lshlrev_b32 v192, 16, v72\n"
"v_and_b32 v193, 0xffff0000, v72\n"
"v_lshlrev_b32 v194, 16, v73\n"
"v_and_b32 v195, 0xffff0000, v73\n"
"v_add_f32 v144, v144, v192\n"
"v_add_f32 v145, v145, v193\n"
"v_add_f32 v146, v146, v194\n"
"v_add_f32 v147, v147, v195\n"
"v_cvt_pk_bf16_f32 v144, v144, v145\n"
"v_cvt_pk_bf16_f32 v145, v146, v147\n"
"buffer_store_dwordx2 v[144:145], v5, s[48:51], s77 offen offset:32\n"
"v_lshlrev_b32 v196, 16, v74\n"
"v_and_b32 v197, 0xffff0000, v74\n"
"v_lshlrev_b32 v198, 16, v75\n"
"v_and_b32 v199, 0xffff0000, v75\n"
"v_add_f32 v148, v148, v196\n"
"v_add_f32 v149, v149, v197\n"
"v_add_f32 v150, v150, v198\n"
"v_add_f32 v151, v151, v199\n"
"v_cvt_pk_bf16_f32 v148, v148, v149\n"
"v_cvt_pk_bf16_f32 v149, v150, v151\n"
"buffer_store_dwordx2 v[148:149], v5, s[48:51], s78 offen offset:32\n"
"s_waitcnt vmcnt(10)\n"
"v_lshlrev_b32 v200, 16, v76\n"
"v_and_b32 v201, 0xffff0000, v76\n"
"v_lshlrev_b32 v202, 16, v77\n"
"v_and_b32 v203, 0xffff0000, v77\n"
"v_add_f32 v152, v152, v200\n"
"v_add_f32 v153, v153, v201\n"
"v_add_f32 v154, v154, v202\n"
"v_add_f32 v155, v155, v203\n"
"v_cvt_pk_bf16_f32 v152, v152, v153\n"
"v_cvt_pk_bf16_f32 v153, v154, v155\n"
"buffer_store_dwordx2 v[152:153], v5, s[48:51], s76 offen offset:64\n"
"v_lshlrev_b32 v204, 16, v78\n"
"v_and_b32 v205, 0xffff0000, v78\n"
"v_lshlrev_b32 v206, 16, v79\n"
"v_and_b32 v207, 0xffff0000, v79\n"
"v_add_f32 v156, v156, v204\n"
"v_add_f32 v157, v157, v205\n"
"v_add_f32 v158, v158, v206\n"
"v_add_f32 v159, v159, v207\n"
"v_cvt_pk_bf16_f32 v156, v156, v157\n"
"v_cvt_pk_bf16_f32 v157, v158, v159\n"
"buffer_store_dwordx2 v[156:157], v5, s[48:51], s77 offen offset:64\n"
"s_waitcnt vmcnt(11)\n"
"v_lshlrev_b32 v192, 16, v80\n"
"v_and_b32 v193, 0xffff0000, v80\n"
"v_lshlrev_b32 v194, 16, v81\n"
"v_and_b32 v195, 0xffff0000, v81\n"
"v_add_f32 v160, v160, v192\n"
"v_add_f32 v161, v161, v193\n"
"v_add_f32 v162, v162, v194\n"
"v_add_f32 v163, v163, v195\n"
"v_cvt_pk_bf16_f32 v160, v160, v161\n"
"v_cvt_pk_bf16_f32 v161, v162, v163\n"
"buffer_store_dwordx2 v[160:161], v5, s[48:51], s78 offen offset:64\n"
"v_lshlrev_b32 v196, 16, v82\n"
"v_and_b32 v197, 0xffff0000, v82\n"
"v_lshlrev_b32 v198, 16, v83\n"
"v_and_b32 v199, 0xffff0000, v83\n"
"v_add_f32 v164, v164, v196\n"
"v_add_f32 v165, v165, v197\n"
"v_add_f32 v166, v166, v198\n"
"v_add_f32 v167, v167, v199\n"
"v_cvt_pk_bf16_f32 v164, v164, v165\n"
"v_cvt_pk_bf16_f32 v165, v166, v167\n"
"buffer_store_dwordx2 v[164:165], v5, s[48:51], s76 offen offset:96\n"
"s_waitcnt vmcnt(12)\n"
"v_lshlrev_b32 v200, 16, v84\n"
"v_and_b32 v201, 0xffff0000, v84\n"
"v_lshlrev_b32 v202, 16, v85\n"
"v_and_b32 v203, 0xffff0000, v85\n"
"v_add_f32 v168, v168, v200\n"
"v_add_f32 v169, v169, v201\n"
"v_add_f32 v170, v170, v202\n"
"v_add_f32 v171, v171, v203\n"
"v_cvt_pk_bf16_f32 v168, v168, v169\n"
"v_cvt_pk_bf16_f32 v169, v170, v171\n"
"buffer_store_dwordx2 v[168:169], v5, s[48:51], s77 offen offset:96\n"
"v_lshlrev_b32 v204, 16, v86\n"
"v_and_b32 v205, 0xffff0000, v86\n"
"v_lshlrev_b32 v206, 16, v87\n"
"v_and_b32 v207, 0xffff0000, v87\n"
"v_add_f32 v172, v172, v204\n"
"v_add_f32 v173, v173, v205\n"
"v_add_f32 v174, v174, v206\n"
"v_add_f32 v175, v175, v207\n"
"v_cvt_pk_bf16_f32 v172, v172, v173\n"
"v_cvt_pk_bf16_f32 v173, v174, v175\n"
"buffer_store_dwordx2 v[172:173], v5, s[48:51], s78 offen offset:96\n"
"s_waitcnt vmcnt(13)\n"
"v_lshlrev_b32 v192, 16, v88\n"
"v_and_b32 v193, 0xffff0000, v88\n"
"v_lshlrev_b32 v194, 16, v89\n"
"v_and_b32 v195, 0xffff0000, v89\n"
"v_add_f32 v176, v176, v192\n"
"v_add_f32 v177, v177, v193\n"
"v_add_f32 v178, v178, v194\n"
"v_add_f32 v179, v179, v195\n"
"v_cvt_pk_bf16_f32 v176, v176, v177\n"
"v_cvt_pk_bf16_f32 v177, v178, v179\n"
"buffer_store_dwordx2 v[176:177], v5, s[48:51], s80 offen offset:0\n"
"v_lshlrev_b32 v196, 16, v90\n"
"v_and_b32 v197, 0xffff0000, v90\n"
"v_lshlrev_b32 v198, 16, v91\n"
"v_and_b32 v199, 0xffff0000, v91\n"
"v_add_f32 v180, v180, v196\n"
"v_add_f32 v181, v181, v197\n"
"v_add_f32 v182, v182, v198\n"
"v_add_f32 v183, v183, v199\n"
"v_cvt_pk_bf16_f32 v180, v180, v181\n"
"v_cvt_pk_bf16_f32 v181, v182, v183\n"
"buffer_store_dwordx2 v[180:181], v5, s[48:51], s81 offen offset:0\n"
"s_waitcnt vmcnt(14)\n"
"v_lshlrev_b32 v200, 16, v92\n"
"v_and_b32 v201, 0xffff0000, v92\n"
"v_lshlrev_b32 v202, 16, v93\n"
"v_and_b32 v203, 0xffff0000, v93\n"
"v_add_f32 v184, v184, v200\n"
"v_add_f32 v185, v185, v201\n"
"v_add_f32 v186, v186, v202\n"
"v_add_f32 v187, v187, v203\n"
"v_cvt_pk_bf16_f32 v184, v184, v185\n"
"v_cvt_pk_bf16_f32 v185, v186, v187\n"
"buffer_store_dwordx2 v[184:185], v5, s[48:51], s82 offen offset:0\n"
"s_waitcnt vmcnt(0)\n"
  :: [o] "s"(o), [wt] "s"(wt), [part] "s"(part), [flag] "s"(flag), [out] "s"(out), [kh] "s"(kh), [lds] "s"(ldsb), [tid] "v"(tid)
  : "v1","v2","v3","v4","v5","v6","v7","v8","v9","v10","v11","v12","v13","v14","v15","v16","v17","v18","v19","v20","v21","v22","v23","v24","v25","v26","v27","v28","v29","v30","v31","v32","v33","v34","v35","v36","v37","v38","v39","v40","v41","v42","v43","v44","v45","v46","v47","v48","v49","v50","v51","v52","v53","v54","v55","v56","v57","v58","v59","v60","v61","v62","v63","v64","v65","v66","v67","v68","v69","v70","v71","v72","v73","v74","v75","v76","v77","v78","v79","v80","v81","v82","v83","v84","v85","v86","v87","v88","v89","v90","v91","v92","v93","v94","v95","v96","v97","v98","v99","v100","v101","v102","v103","v104","v105","v106","v107","v108","v109","v110","v111","v112","v113","v114","v115","v116","v117","v118","v119","v120","v121","v122","v123","v124","v125","v126","v127","v128","v129","v130","v131","v132","v133","v134","v135","v136","v137","v138","v139","v140","v141","v142","v143","v144","v145","v146","v147","v148","v149","v150","v151","v152","v153","v154","v155","v156","v157","v158","v159","v160","v161","v162","v163","v164","v165","v166","v167","v168","v169","v170","v171","v172","v173","v174","v175","v176","v177","v178","v179","v180","v181","v182","v183","v184","v185","v186","v187","v188","v189","v190","v191","v192","v193","v194","v195","v196","v197","v198","v199","v200","v201","v202","v203","v204","v205","v206","v207","v208","v209","v210","v211","v212","v213","v214","v215","v216","v217","v218","v219","v220","v221","v222","v223","v224","v225","v226","v227","v228","v229","v230","v231","v232","v233","v234","v235","v236","v237","v238","v239","v240","v241","v242","v243","v244","v245","v246","v247","v248","v249","v250","v251","v252","v253","v254","v255","a0","a1","a2","a3","a4","a5","a6","a7","a8","a9","a10","a11","a12","a13","a14","a15","a16","a17","a18","a19","a20","a21","a22","a23","a24","a25","a26","a27","a28","a29","a30","a31","a32","a33","a34","a35","a36","a37","a38","a39","a40","a41","a42","a43","a44","a45","a46","a47","a48","a49","a50","a51","a52","a53","a54","a55","a56","a57","a58","a59","a60","a61","a62","a63","a64","a65","a66","a67","a68","a69","a70","a71","a72","a73","a74","a75","a76","a77","a78","a79","a80","a81","a82","a83","a84","a85","a86","a87","a88","a89","a90","a91","a92","a93","a94","a95","a96","a97","a98","a99","a100","a101","a102","a103","a104","a105","a106","a107","a108","a109","a110","a111","a112","a113","a114","a115","a116","a117","a118","a119","a120","a121","a122","a123","a124","a125","a126","a127","a128","a129","a130","a131","a132","a133","a134","a135","a136","a137","a138","a139","a140","a141","a142","a143","a144","a145","a146","a147","a148","a149","a150","a151","a152","a153","a154","a155","a156","a157","a158","a159","a160","a161","a162","a163","a164","a165","a166","a167","a168","a169","a170","a171","a172","a173","a174","a175","a176","a177","a178","a179","a180","a181","a182","a183","a184","a185","a186","a187","a188","a189","a190","a191","a192","a193","a194","a195","a196","a197","a198","a199","a200","a201","a202","a203","a204","a205","a206","a207","a208","a209","a210","a211","a212","a213","a214","a215","a216","a217","a218","a219","a220","a221","a222","a223","a224","a225","a226","a227","a228","a229","a230","a231","a232","a233","a234","a235","a236","a237","a238","a239","a240","a241","a242","a243","a244","a245","a246","a247","a248","a249","a250","a251","a252","a253","a254","a255","s36","s37","s38","s39","s40","s41","s42","s43","s44","s45","s46","s47","s48","s49","s50","s51","s52","s53","s54","s55","s56","s57","s58","s59","s60","s61","s62","s63","s64","s65","s66","s67","s68","s69","s70","s71","s72","s73","s74","s75","s76","s77","s78","s79","s80","s81","s82","s83","s84","s85","s86","s87","s88","s89","s90","s91","s92","s93","s94","s95","s96","s97","s98","s99","vcc","scc","memory");
}

extern "C" void kernel_launch(void* const* d_in, const int* in_sizes, int n_in, void* d_out, int out_size, void* d_ws, size_t ws_size, hipStream_t stream) {
  (void)in_sizes; (void)n_in; (void)out_size; (void)ws_size;
  (void)hipFuncSetAttribute((const void*)k1_qattn, hipFuncAttributeMaxDynamicSharedMemorySize, 163840);
  k1_qattn<<<256, 256, 163840, stream>>>((const u16*)d_in[0], (const u16*)d_in[1], (const u16*)d_in[2], (const u16*)d_in[3], (const u16*)d_in[4],
                                         (const u16*)d_in[5], (const u16*)d_in[6], (const u16*)d_in[7], (const u16*)d_in[8], (const u16*)d_in[9], (char*)d_ws);
  (void)hipFuncSetAttribute((const void*)k2_oproj, hipFuncAttributeMaxDynamicSharedMemorySize, 147456);
  k2_oproj<<<256, 256, 147456, stream>>>((char*)d_ws, (u16*)d_out);
}
```
